```python
import torch

def _softmax(x, axis=-1):
    return torch.softmax(x, dim=axis)
def _gelu(x):
    return torch.nn.functional.gelu(x, approximate="tanh")
def _silu(x):
    return torch.nn.functional.silu(x)
def _sigmoid(x):
    return torch.sigmoid(x)
def _layernorm(x, axis=-1, eps=1e-5):
    m = x.mean(dim=axis, keepdim=True)
    v = x.var(dim=axis, keepdim=True, unbiased=False)
    return (x - m) / torch.sqrt(v + eps)
def _scatter_add(tgt, idx, src, axis=0):
    return tgt.clone().index_add_(axis, idx, src)
def _segment_sum(src, seg, n):
    out = torch.zeros((n,) + src.shape[1:], dtype=src.dtype)
    return out.index_add_(0, seg, src)
def _pad(x, pw):
    flat = tuple(v for pair in reversed(pw) for v in pair)
    return torch.nn.functional.pad(x, flat)

kernel_name = 'concat_heads__mha_block_cached__gqa_block_cached__near_ridge'

def setup_inputs():

    _w = (lambda a: (lambda t: t.to(torch.bfloat16) if t.is_floating_point() else t)(torch.as_tensor(a)))
    def _A_setup():
        B, Sq, D = 16, 96, 640
        H, Sk, Dh = 32, 512, 192
        return {
            "x": _w((lambda *s: torch.randn(*s, dtype=torch.bfloat16) * 0.8)(B, Sq, D)),
            "k_cache": _w((lambda *s: torch.randn(*s, dtype=torch.bfloat16) * 0.8)(B, H, Sk, Dh)),
            "v_cache": _w((lambda *s: torch.randn(*s, dtype=torch.bfloat16) * 0.05)(B, H, Sk, Dh)),
            "wq": _w((lambda *s: torch.randn(*s, dtype=torch.bfloat16) * 0.8)(D, H * Dh) * (D ** -0.5)),
            "wo": _w((lambda *s: torch.randn(*s, dtype=torch.bfloat16) * 0.05)(H * Dh, D) * ((H * Dh) ** -0.5)),
        }
    _A_in = _A_setup()

    def _B_setup():
        B, Sq, D = 16, 96, 640
        Hq, Hkv, Sk, Dh = 32, 32, 512, 192
        assert Hq % Hkv == 0, (Hq, Hkv)
        return {
            "x": _w((lambda *s: torch.randn(*s, dtype=torch.bfloat16) * 0.8)(B, Sq, D)),
            "k_cache": _w((lambda *s: torch.randn(*s, dtype=torch.bfloat16) * 0.8)(B, Hkv, Sk, Dh)),
            "v_cache": _w((lambda *s: torch.randn(*s, dtype=torch.bfloat16) * 0.05)(B, Hkv, Sk, Dh)),
            "wq": _w((lambda *s: torch.randn(*s, dtype=torch.bfloat16) * 0.8)(D, Hq * Dh) * (D ** -0.5)),
            "wo": _w((lambda *s: torch.randn(*s, dtype=torch.bfloat16) * 0.05)(Hq * Dh, D) * ((Hq * Dh) ** -0.5)),
        }
    _B_in = _B_setup()

    return {
        "A_x": _A_in["x"],
        "A_k_cache": _A_in["k_cache"],
        "A_v_cache": _A_in["v_cache"],
        "A_wq": _A_in["wq"],
        "A_wo": _A_in["wo"],
        "B_x": _B_in["x"],
        "B_k_cache": _B_in["k_cache"],
        "B_v_cache": _B_in["v_cache"],
        "B_wq": _B_in["wq"],
        "B_wo": _B_in["wo"],
    }

def reference(A_x, A_k_cache, A_v_cache, A_wq, A_wo, B_x, B_k_cache, B_v_cache, B_wq, B_wo):
    def _A_ref(x, k_cache, v_cache, wq, wo):
        B, Sq, D = x.shape
        H, Dh = 32, 192
        q = (lambda x, *d: x.permute(*d))((x @ wq).reshape(B, Sq, H, Dh), 0, 2, 1, 3)
        scores = torch.einsum("bhqd,bhkd->bhqk", q, k_cache) * (Dh ** -0.5)
        p = _softmax(scores, axis=-1)
        o = torch.einsum("bhqk,bhkd->bhqd", p, v_cache)
        return (lambda x, *d: x.permute(*d))(o, 0, 2, 1, 3).reshape(B, Sq, H * Dh) @ wo

    def _B_ref(x, k_cache, v_cache, wq, wo):
        B, Sq, D = x.shape
        Hq, Dh = 32, 192
        Hkv = k_cache.shape[1]
        rep = Hq // Hkv
        q = (lambda x, *d: x.permute(*d))((x @ wq).reshape(B, Sq, Hq, Dh), 0, 2, 1, 3)
        k = (lambda x, n, axis: torch.repeat_interleave(x, n, dim=axis))(k_cache, rep, axis=1)
        v = (lambda x, n, axis: torch.repeat_interleave(x, n, dim=axis))(v_cache, rep, axis=1)
        scores = torch.einsum("bhqd,bhkd->bhqk", q, k) * (Dh ** -0.5)
        p = _softmax(scores, axis=-1)
        o = torch.einsum("bhqk,bhkd->bhqd", p, v)
        return (lambda x, *d: x.permute(*d))(o, 0, 2, 1, 3).reshape(B, Sq, Hq * Dh) @ wo

    a = _A_ref(A_x, A_k_cache, A_v_cache, A_wq, A_wo)
    b = _B_ref(B_x, B_k_cache, B_v_cache, B_wq, B_wo)
    return torch.concatenate([a, b], axis=-1)
```

```cpp
#include <hip/hip_runtime.h>
#include <hip/hip_bf16.h>
typedef int i32x4 __attribute__((ext_vector_type(4)));
typedef unsigned u32x4 __attribute__((ext_vector_type(4)));
typedef unsigned u32x2 __attribute__((ext_vector_type(2)));
#define PART_BYTES (256u * 122880u)
static __device__ __forceinline__ i32x4 mksrd(const void* p, unsigned bytes) {
  unsigned long long q = (unsigned long long)p;
  i32x4 r;
  r.x = __builtin_amdgcn_readfirstlane((int)(unsigned)q);
  r.y = __builtin_amdgcn_readfirstlane((int)(unsigned)(q >> 32) & 0xffff);
  r.z = (int)bytes; r.w = 0x00020000;
  return r;
}
static __device__ __forceinline__ unsigned pack_bf16(float lo, float hi) {
  unsigned r;
  asm("v_cvt_pk_bf16_f32 %0, %1, %2" : "=v"(r) : "v"(lo), "v"(hi));
  return r;
}
typedef __attribute__((address_space(1))) unsigned gu32;
#define RFL(x) ((unsigned)__builtin_amdgcn_readfirstlane((int)(x)))
__global__ __launch_bounds__(256, 1) void attn_fused_kernel(
    const void* xA, const void* kA, const void* vA, const void* wqA, const void* woA,
    const void* xB, const void* kB, const void* vB, const void* wqB, const void* woB, void* ws, u32x2* out) {
  __shared__ __attribute__((aligned(1024))) char lds[159744];
  asm volatile("s_getpc_b64 s[76:77]\n\tglobal_load_dword v254, %0, s[76:77]" :: "v"((threadIdx.x >> 2) * 128) : "s76", "s77", "v254", "memory");
  const unsigned L0 = (unsigned)(size_t)lds, xreg = L0 + 122880;
  unsigned w = blockIdx.x, wid = RFL(threadIdx.x >> 6), l = threadIdx.x & 63;
#define IDS const unsigned xcd = w & 7, idx = w >> 3, br = xcd >> 2, g = (xcd & 3) * 2 + (idx >> 4), b = idx & 15, \
    i = wid >> 1, r = wid & 1, ring = L0 + i * 61440, h = 8 * (g >> 1) + 2 * (g & 1) + (i);
  unsigned dW[3], dX, m0b, m0x, wq0, x0, rot;
  i32x4 rX, rWq;
  {
    IDS
    rX = mksrd(br ? xB : xA, 16u * 96 * 640 * 2);
    rWq = mksrd(br ? wqB : wqA, 640u * 6144 * 2);
#pragma unroll
    for (unsigned qq = 0; qq < 3; ++qq) {
      const unsigned e = 64 * qq + l, rw = (e * 171) >> 12, c = e - rw * 24, uu = (rw >> 1) & 3;
      dW[qq] = (8 * (rw >> 2) + 4 * r + (rw & 3)) * 12288 + 16 * (c ^ ((uu & 1) | ((uu & 2) << 1))) - 1024 * qq;
    }
    dX = (l >> 3) * 1280 + 16 * ((l & 7) ^ (2 * ((l >> 4) & 3)));
    m0b = RFL(ring + r * 6144); m0x = RFL(xreg + wid * 3072); wq0 = RFL(h * 384); x0 = RFL((b * 96 + wid * 24) * 1280);
    rot = RFL(1 * ((b + (g & 1) * 1 + (g >> 1) * 2) % 10) * 128);
  }
  asm volatile(
"s_nop 4\n"
"s_mov_b32 s63, 0\n"
"s_mov_b32 s57, %[wq0]\n"
"s_mov_b32 s56, %[rot]\n"
"s_mov_b32 s61, %[m0b]\n"
"s_mov_b32 m0, s61\n"
"s_mul_i32 s66, s56, 6144\n"
"s_add_u32 s66, s66, s57\n"
"s_add_u32 s68, s66, 196608\n"
"buffer_load_dwordx4 %[dW0], %[rWq], s66 offen offset:0 lds\n"
"buffer_load_dwordx4 %[dW1], %[rWq], s66 offen offset:1024 lds\n"
"buffer_load_dwordx4 %[dW2], %[rWq], s66 offen offset:2048 lds\n"
"s_add_u32 m0, s61, 3072\n"
"s_add_u32 s66, s66, 393216\n"
"buffer_load_dwordx4 %[dW0], %[rWq], s68 offen offset:0 lds\n"
"buffer_load_dwordx4 %[dW1], %[rWq], s68 offen offset:1024 lds\n"
"buffer_load_dwordx4 %[dW2], %[rWq], s68 offen offset:2048 lds\n"
"s_add_u32 m0, %[m0x], s63\n"
"s_add_u32 s67, %[x0], s56\n"
"s_add_u32 s75, s67, 9216\n"
"buffer_load_dwordx4 %[dX], %[rX], s67 offen offset:0 lds\n"
"buffer_load_dwordx4 %[dX], %[rX], s75 offen offset:1024 lds\n"
"s_add_u32 s75, s67, 18432\n"
"buffer_load_dwordx4 %[dX], %[rX], s75 offen offset:2048 lds\n"
"s_add_u32 s56, s56, 128\n"
"s_cmp_eq_u32 s56, 1280\n"
"s_cselect_b32 s56, 0, s56\n"
"s_add_u32 s63, s63, 12288\n"
"s_cmp_eq_u32 s63, 36864\n"
"s_cselect_b32 s63, 0, s63\n"
"s_add_u32 s61, s61, 12288\n"
"s_mov_b32 m0, s61\n"
"s_add_u32 s68, s66, 196608\n"
"buffer_load_dwordx4 %[dW0], %[rWq], s66 offen offset:0 lds\n"
"buffer_load_dwordx4 %[dW1], %[rWq], s66 offen offset:1024 lds\n"
"buffer_load_dwordx4 %[dW2], %[rWq], s66 offen offset:2048 lds\n"
"s_add_u32 m0, s61, 3072\n"
"s_add_u32 s66, s66, 393216\n"
"buffer_load_dwordx4 %[dW0], %[rWq], s68 offen offset:0 lds\n"
"buffer_load_dwordx4 %[dW1], %[rWq], s68 offen offset:1024 lds\n"
"buffer_load_dwordx4 %[dW2], %[rWq], s68 offen offset:2048 lds\n"
"s_add_u32 s61, s61, 12288\n"
    : "+s"(w), "+s"(wid), "+v"(l)
    : [dW0] "v"(dW[0]), [dW1] "v"(dW[1]), [dW2] "v"(dW[2]), [dX] "v"(dX), [rX] "s"(rX), [rWq] "s"(rWq),
      [m0b] "s"(m0b), [m0x] "s"(m0x), [wq0] "s"(wq0), [x0] "s"(x0), [rot] "s"(rot)
    : "memory", "scc", "s56", "s57", "s61", "s63", "s66", "s67", "s68", "s75");
  w = RFL(w); wid = RFL(wid);
  IDS
  const unsigned rho = l & 15, g4 = l >> 4, q = (l >> 2) & 3, p = l & 3;
  const i32x4 rK = mksrd(br ? kB : kA, 16u * 32 * 512 * 192 * 2), rV = mksrd(br ? vB : vA, 16u * 32 * 512 * 192 * 2),
              rWo = mksrd(br ? woB : woA, 640u * 6144 * 2), rWs = mksrd(ws, PART_BYTES + 163840u);
  const unsigned row = 4 * g4 + q, u = (row >> 1) & 3, fV = 2 * u, fW = (u & 1) | ((u & 2) << 1), fK = 2 * ((rho >> 1) & 3);
  const unsigned rdK = ring + rho * 384 + 16 * (g4 ^ fK);
  const unsigned rdV = ring + row * 384 + 16 * ((p >> 1) ^ fV) + 8 * (p & 1);
  const unsigned rdW = ring + row * 384 + 16 * (p ^ fW) + 8 * (p & 1);
  const unsigned rdX = xreg + (48 * r + rho) * 128 + 16 * (g4 ^ fK);
  const unsigned rdO = ring + row * 128 + 16 * ((p >> 1) ^ fV) + 8 * (p & 1);
  unsigned dKV[3];
#pragma unroll
  for (unsigned qq = 0; qq < 3; ++qq) {
    const unsigned e = 64 * qq + l, rw = (e * 171) >> 12, c = e - rw * 24, uu = (rw >> 1) & 3;
    dKV[qq] = (16 * r + rw) * 384 + 16 * (c ^ (2 * uu)) - 1024 * qq;
  }
  const unsigned exW = xreg + wid * 9216 + l * 16, exR = xreg + (wid ^ 2) * 9216 + l * 16, stv = l * 16;
  const unsigned k0 = RFL((b * 32 + h) * 196608);
  const unsigned wo0 = RFL((h * 192 + r * 48) * 1280 + i * 640), wo1 = RFL(((h ^ 1) * 192 + r * 48) * 1280 + i * 640);
  const unsigned st0 = RFL((((br * 16 + b) * 8 + g) * 4 + wid) * 30720);
  const unsigned bb = br * 16 + b, flg = RFL(PART_BYTES + bb * 4352), rotc = RFL(1 * (b % 5) * 128);
  const unsigned fl1 = RFL(256u << g);
  unsigned so[4], oo[4], w2 = wid;
#define UNIT(k, W) { const unsigned un = k < 2 ? 2 * W + k : 4 * k + W, j3 = (un * 11) >> 5, c5 = (rotc >> 7) + j3, \
    c = c5 >= 5 ? c5 - 5 : c5, hu = 3 * g + un - 3 * j3, ww = (hu * 43) >> 8, qh = hu - 6 * ww; \
    so[k] = RFL(bb * 8 * 122880 + ww * 30720 + c * 6144 + (qh >> 1) * 1024 + (qh & 1) * 128); \
    oo[k] = RFL((b * 96 + (ww & 1) * 48 + qh * 8) * 2560 + (br * 640 + (ww >> 1) * 320 + c * 64) * 2); }
  UNIT(0, wid) UNIT(1, wid)
  const unsigned po = ((idx * 4 + wid) * 8 + (l & 7)) * 128;
  unsigned y;
  unsigned cur = rdK, res[16] = {dKV[0], dKV[1], dKV[2], dW[0], dW[1], dW[2], rdV, rdW};
  asm volatile(
"s_nop 4\n"
"s_mov_b32 s60, 0\n"
"s_mov_b32 s62, 0\n"
"s_mov_b32 s63, 0\n"
"s_mov_b32 s64, %[k0]\n"
"s_mov_b32 s65, %[k0]\n"
"s_mov_b32 s57, %[wq0]\n"
"s_mov_b32 s56, %[rot]\n"
"s_mov_b32 s72, %[rotc]\n"
"s_mov_b32 s61, %[m0b]\n"
"s_mov_b32 s78, 0x3dd53b95\n"
"s_mov_b32 s79, 0x3dd53b95\n"
"s_mov_b32 s80, 0xffff0000\n"
"s_mov_b32 s81, 0xffff0000\n"
"s_mul_i32 s73, s72, 48\n"
"s_add_u32 s73, s73, %[st0]\n"
"s_add_u32 s58, %[st0], 30720\n"
"s_mul_i32 s66, s56, 6144\n"
"s_add_u32 s66, s66, s57\n"
"s_add_u32 s68, s66, 196608\n"
"s_add_u32 s66, s66, 393216\n"
"s_add_u32 s67, %[x0], s56\n"
"s_add_u32 s75, s67, 9216\n"
"s_add_u32 s75, s67, 18432\n"
"s_add_u32 s56, s56, 128\n"
"s_cmp_eq_u32 s56, 1280\n"
"s_cselect_b32 s56, 0, s56\n"
"s_add_u32 s63, s63, 12288\n"
"s_cmp_eq_u32 s63, 36864\n"
"s_cselect_b32 s63, 0, s63\n"
"s_add_u32 s61, s61, 12288\n"
"s_add_u32 s68, s66, 196608\n"
"s_add_u32 s66, s66, 393216\n"
"s_add_u32 s61, s61, 12288\n"
"s_mov_b32 m0, s61\n"
"s_mul_i32 s66, s56, 6144\n"
"s_add_u32 s66, s66, s57\n"
"s_add_u32 s68, s66, 196608\n"
"buffer_load_dwordx4 %[dW0], %[rWq], s66 offen offset:0 lds\n"
"buffer_load_dwordx4 %[dW1], %[rWq], s66 offen offset:1024 lds\n"
"buffer_load_dwordx4 %[dW2], %[rWq], s66 offen offset:2048 lds\n"
"s_add_u32 m0, s61, 3072\n"
"s_add_u32 s66, s66, 393216\n"
"buffer_load_dwordx4 %[dW0], %[rWq], s68 offen offset:0 lds\n"
"buffer_load_dwordx4 %[dW1], %[rWq], s68 offen offset:1024 lds\n"
"buffer_load_dwordx4 %[dW2], %[rWq], s68 offen offset:2048 lds\n"
"s_add_u32 m0, %[m0x], s63\n"
"s_add_u32 s67, %[x0], s56\n"
"s_add_u32 s75, s67, 9216\n"
"buffer_load_dwordx4 %[dX], %[rX], s67 offen offset:0 lds\n"
"buffer_load_dwordx4 %[dX], %[rX], s75 offen offset:1024 lds\n"
"s_add_u32 s75, s67, 18432\n"
"buffer_load_dwordx4 %[dX], %[rX], s75 offen offset:2048 lds\n"
"s_add_u32 s56, s56, 128\n"
"s_cmp_eq_u32 s56, 1280\n"
"s_cselect_b32 s56, 0, s56\n"
"s_add_u32 s63, s63, 12288\n"
"s_cmp_eq_u32 s63, 36864\n"
"s_cselect_b32 s63, 0, s63\n"
"s_add_u32 s61, s61, 12288\n"
"s_mov_b32 m0, s61\n"
"s_add_u32 s68, s66, 196608\n"
"buffer_load_dwordx4 %[dW0], %[rWq], s66 offen offset:0 lds\n"
"buffer_load_dwordx4 %[dW1], %[rWq], s66 offen offset:1024 lds\n"
"buffer_load_dwordx4 %[dW2], %[rWq], s66 offen offset:2048 lds\n"
"s_add_u32 m0, s61, 3072\n"
"s_add_u32 s66, s66, 393216\n"
"buffer_load_dwordx4 %[dW0], %[rWq], s68 offen offset:0 lds\n"
"buffer_load_dwordx4 %[dW1], %[rWq], s68 offen offset:1024 lds\n"
"buffer_load_dwordx4 %[dW2], %[rWq], s68 offen offset:2048 lds\n"
"s_add_u32 s61, s61, 12288\n"
"v_lshrrev_b32 v242, 2, %[stv]\n"
"buffer_load_dword %[y], v242, %[rWs], %[flg] offen sc1\n"
"s_getpc_b64 s[76:77]\n"
"Lpcb_%=:\n"
"v_min_u32 v255, Lend_%=-Lpcb_%=+1536, %[po]\n"
"s_nop 0\n"
"global_load_dword v254, v255, s[76:77] \n"
"v_mov_b64 v[16:17], 0\n"
"v_mov_b64 v[18:19], 0\n"
"v_mov_b64 v[20:21], 0\n"
"v_mov_b64 v[22:23], 0\n"
"v_mov_b64 v[24:25], 0\n"
"v_mov_b64 v[26:27], 0\n"
"v_mov_b64 v[28:29], 0\n"
"v_mov_b64 v[30:31], 0\n"
"v_mov_b64 v[32:33], 0\n"
"v_mov_b64 v[34:35], 0\n"
"v_mov_b64 v[36:37], 0\n"
"v_mov_b64 v[38:39], 0\n"
"v_mov_b64 v[40:41], 0\n"
"v_mov_b64 v[42:43], 0\n"
"v_mov_b64 v[44:45], 0\n"
"v_mov_b64 v[46:47], 0\n"
"v_mov_b64 v[48:49], 0\n"
"v_mov_b64 v[50:51], 0\n"
"v_mov_b64 v[52:53], 0\n"
"v_mov_b64 v[54:55], 0\n"
"v_mov_b64 v[56:57], 0\n"
"v_mov_b64 v[58:59], 0\n"
"v_mov_b64 v[60:61], 0\n"
"v_mov_b64 v[62:63], 0\n"
"v_mov_b64 v[64:65], 0\n"
"v_mov_b64 v[66:67], 0\n"
"v_mov_b64 v[68:69], 0\n"
"v_mov_b64 v[70:71], 0\n"
"v_mov_b64 v[72:73], 0\n"
"v_mov_b64 v[74:75], 0\n"
"v_mov_b64 v[76:77], 0\n"
"v_mov_b64 v[78:79], 0\n"
"v_mov_b64 v[80:81], 0\n"
"v_mov_b64 v[82:83], 0\n"
"v_mov_b64 v[84:85], 0\n"
"v_mov_b64 v[86:87], 0\n"
"v_mov_b64 v[88:89], 0\n"
"v_mov_b64 v[90:91], 0\n"
"v_mov_b64 v[92:93], 0\n"
"v_mov_b64 v[94:95], 0\n"
"v_mov_b64 v[96:97], 0\n"
"v_mov_b64 v[98:99], 0\n"
"v_mov_b64 v[100:101], 0\n"
"v_mov_b64 v[102:103], 0\n"
"v_mov_b64 v[104:105], 0\n"
"v_mov_b64 v[106:107], 0\n"
"v_mov_b64 v[108:109], 0\n"
"v_mov_b64 v[110:111], 0\n"
"v_mov_b64 v[112:113], 0\n"
"v_mov_b64 v[114:115], 0\n"
"v_mov_b64 v[116:117], 0\n"
"v_mov_b64 v[118:119], 0\n"
"v_mov_b64 v[120:121], 0\n"
"v_mov_b64 v[122:123], 0\n"
"v_mov_b64 v[124:125], 0\n"
"v_mov_b64 v[126:127], 0\n"
"v_mov_b64 v[128:129], 0\n"
"v_mov_b64 v[130:131], 0\n"
"v_mov_b64 v[132:133], 0\n"
"v_mov_b64 v[134:135], 0\n"
"v_mov_b64 v[136:137], 0\n"
"v_mov_b64 v[138:139], 0\n"
"v_mov_b64 v[140:141], 0\n"
"v_mov_b64 v[142:143], 0\n"
"v_mov_b64 v[144:145], 0\n"
"v_mov_b64 v[146:147], 0\n"
"v_mov_b64 v[148:149], 0\n"
"v_mov_b64 v[150:151], 0\n"
"v_mov_b64 v[152:153], 0\n"
"v_mov_b64 v[154:155], 0\n"
"v_mov_b64 v[156:157], 0\n"
"v_mov_b64 v[158:159], 0\n"
"v_mov_b32 v220, 0x3f803f80\n"
"v_mov_b32 v221, 0x3f803f80\n"
"v_mov_b32 v222, 0x3f803f80\n"
"v_mov_b32 v223, 0x3f803f80\n"
"v_mov_b32 v196, 0\n"
"v_mov_b32 v197, 0\n"
"v_mov_b32 v198, 0\n"
"v_mov_b32 v199, 0\n"
"v_mov_b32 v200, 0\n"
"v_mov_b32 v201, 0\n"
"v_mov_b32 v202, 0\n"
"v_mov_b32 v203, 0\n"
"v_mov_b32 v204, 0\n"
"v_mov_b32 v205, 0\n"
"v_mov_b32 v206, 0\n"
"v_mov_b32 v207, 0\n"
"s_mov_b32 s70, 0xff800000\n"
"s_mov_b32 s71, 0xff800000\n"
"s_mov_b32 s74, 1\n"
"v_add_u32 v236, s60, %[rdW]\n"
"v_xor_b32 v237, 8, v236\n"
"v_xor_b32 v238, 64, v236\n"
"v_xor_b32 v239, 72, v236\n"
"v_add_u32 v240, s62, %[rdX]\n"
"s_add_u32 s62, s62, 12288\n"
"s_cmp_eq_u32 s62, 36864\n"
"s_cselect_b32 s62, 0, s62\n"
"s_waitcnt vmcnt(23) lgkmcnt(0)\n"
"s_barrier\n"
"ds_read_b64_tr_b16 a[216:217], v236 offset:0\n"
"ds_read_b64_tr_b16 a[218:219], v236 offset:6144\n"
"ds_read_b128 a[240:243], v240 offset:0\n"
"ds_read_b64_tr_b16 a[220:221], v237 offset:0\n"
"ds_read_b64_tr_b16 a[222:223], v237 offset:6144\n"
"ds_read_b128 a[244:247], v240 offset:2048\n"
"ds_read_b128 a[248:251], v240 offset:4096\n"
"ds_read_b64_tr_b16 a[224:225], v238 offset:0\n"
"ds_read_b64_tr_b16 a[226:227], v238 offset:6144\n"
"ds_read_b64_tr_b16 a[228:229], v239 offset:0\n"
"ds_read_b64_tr_b16 a[230:231], v239 offset:6144\n"
"s_mov_b32 s69, 8\n"
".p2align 3\n"
"La_%=:\n"
"s_waitcnt lgkmcnt(8)\n"
"v_mfma_f32_16x16x32_bf16 v[16:19], a[216:219], a[240:243], v[16:19]\n"
"ds_read_b64_tr_b16 a[232:233], v236 offset:128\n"
"s_waitcnt lgkmcnt(6)\n"
"v_mfma_f32_16x16x32_bf16 v[20:23], a[216:219], a[244:247], v[20:23]\n"
"ds_read_b64_tr_b16 a[234:235], v236 offset:6272\n"
"s_waitcnt lgkmcnt(6)\n"
"v_mfma_f32_16x16x32_bf16 v[24:27], a[216:219], a[248:251], v[24:27]\n"
"s_mov_b32 m0, s61\n"
"s_mul_i32 s66, s56, 6144\n"
"s_add_u32 s66, s66, s57\n"
"s_add_u32 s68, s66, 196608\n"
"buffer_load_dwordx4 %[dW0], %[rWq], s66 offen offset:0 lds\n"
"v_mfma_f32_16x16x32_bf16 v[28:31], a[220:223], a[240:243], v[28:31]\n"
"ds_read_b64_tr_b16 a[236:237], v237 offset:128\n"
"v_mfma_f32_16x16x32_bf16 v[32:35], a[220:223], a[244:247], v[32:35]\n"
"ds_read_b64_tr_b16 a[238:239], v237 offset:6272\n"
"v_mfma_f32_16x16x32_bf16 v[36:39], a[220:223], a[248:251], v[36:39]\n"
"buffer_load_dwordx4 %[dW1], %[rWq], s66 offen offset:1024 lds\n"
"s_waitcnt lgkmcnt(6)\n"
"v_mfma_f32_16x16x32_bf16 v[40:43], a[224:227], a[240:243], v[40:43]\n"
"ds_read_b64_tr_b16 a[216:217], v238 offset:128\n"
"v_mfma_f32_16x16x32_bf16 v[44:47], a[224:227], a[244:247], v[44:47]\n"
"ds_read_b64_tr_b16 a[218:219], v238 offset:6272\n"
"v_mfma_f32_16x16x32_bf16 v[48:51], a[224:227], a[248:251], v[48:51]\n"
"s_waitcnt lgkmcnt(6)\n"
"v_mfma_f32_16x16x32_bf16 v[52:55], a[228:231], a[240:243], v[52:55]\n"
"ds_read_b64_tr_b16 a[220:221], v239 offset:128\n"
"v_mfma_f32_16x16x32_bf16 v[56:59], a[228:231], a[244:247], v[56:59]\n"
"ds_read_b64_tr_b16 a[222:223], v239 offset:6272\n"
"v_mfma_f32_16x16x32_bf16 v[60:63], a[228:231], a[248:251], v[60:63]\n"
"buffer_load_dwordx4 %[dW2], %[rWq], s66 offen offset:2048 lds\n"
"s_add_u32 m0, s61, 3072\n"
"s_waitcnt lgkmcnt(6)\n"
"v_mfma_f32_16x16x32_bf16 v[64:67], a[232:235], a[240:243], v[64:67]\n"
"ds_read_b64_tr_b16 a[224:225], v236 offset:256\n"
"v_mfma_f32_16x16x32_bf16 v[68:71], a[232:235], a[244:247], v[68:71]\n"
"ds_read_b64_tr_b16 a[226:227], v236 offset:6400\n"
"v_mfma_f32_16x16x32_bf16 v[72:75], a[232:235], a[248:251], v[72:75]\n"
"s_add_u32 s66, s66, 393216\n"
"buffer_load_dwordx4 %[dW0], %[rWq], s68 offen offset:0 lds\n"
"s_waitcnt lgkmcnt(6)\n"
"v_mfma_f32_16x16x32_bf16 v[76:79], a[236:239], a[240:243], v[76:79]\n"
"ds_read_b64_tr_b16 a[228:229], v237 offset:256\n"
"v_mfma_f32_16x16x32_bf16 v[80:83], a[236:239], a[244:247], v[80:83]\n"
"ds_read_b64_tr_b16 a[230:231], v237 offset:6400\n"
"v_mfma_f32_16x16x32_bf16 v[84:87], a[236:239], a[248:251], v[84:87]\n"
"s_waitcnt lgkmcnt(6)\n"
"v_mfma_f32_16x16x32_bf16 v[88:91], a[216:219], a[240:243], v[88:91]\n"
"ds_read_b64_tr_b16 a[232:233], v238 offset:256\n"
"v_mfma_f32_16x16x32_bf16 v[92:95], a[216:219], a[244:247], v[92:95]\n"
"ds_read_b64_tr_b16 a[234:235], v238 offset:6400\n"
"v_mfma_f32_16x16x32_bf16 v[96:99], a[216:219], a[248:251], v[96:99]\n"
"buffer_load_dwordx4 %[dW1], %[rWq], s68 offen offset:1024 lds\n"
"s_waitcnt lgkmcnt(6)\n"
"v_mfma_f32_16x16x32_bf16 v[100:103], a[220:223], a[240:243], v[100:103]\n"
"ds_read_b64_tr_b16 a[236:237], v239 offset:256\n"
"v_mfma_f32_16x16x32_bf16 v[104:107], a[220:223], a[244:247], v[104:107]\n"
"ds_read_b64_tr_b16 a[238:239], v239 offset:6400\n"
"v_mfma_f32_16x16x32_bf16 v[108:111], a[220:223], a[248:251], v[108:111]\n"
"buffer_load_dwordx4 %[dW2], %[rWq], s68 offen offset:2048 lds\n"
"s_waitcnt lgkmcnt(6)\n"
"v_mfma_f32_16x16x32_bf16 v[112:115], a[224:227], a[240:243], v[112:115]\n"
"s_add_u32 s61, %[m0b], s60\n"
"s_add_u32 s60, s60, 12288\n"
"s_cmp_eq_u32 s60, 61440\n"
"s_cselect_b32 s60, 0, s60\n"
"v_add_u32 v236, s60, %[rdW]\n"
"v_mfma_f32_16x16x32_bf16 v[116:119], a[224:227], a[244:247], v[116:119]\n"
"v_xor_b32 v237, 8, v236\n"
"v_xor_b32 v238, 64, v236\n"
"v_xor_b32 v239, 72, v236\n"
"v_xor_b32 v240, 64, v240\n"
"v_mfma_f32_16x16x32_bf16 v[120:123], a[224:227], a[248:251], v[120:123]\n"
"s_waitcnt vmcnt(21) lgkmcnt(0)\n"
"s_barrier\n"
"ds_read_b64_tr_b16 a[216:217], v236 offset:0\n"
"ds_read_b64_tr_b16 a[218:219], v236 offset:6144\n"
"v_mfma_f32_16x16x32_bf16 v[124:127], a[228:231], a[240:243], v[124:127]\n"
"ds_read_b128 v[224:227], v240 offset:0\n"
"ds_read_b64_tr_b16 a[220:221], v237 offset:0\n"
"v_mfma_f32_16x16x32_bf16 v[128:131], a[228:231], a[244:247], v[128:131]\n"
"ds_read_b64_tr_b16 a[222:223], v237 offset:6144\n"
"ds_read_b128 v[228:231], v240 offset:2048\n"
"v_mfma_f32_16x16x32_bf16 v[132:135], a[228:231], a[248:251], v[132:135]\n"
"ds_read_b128 v[232:235], v240 offset:4096\n"
"v_mfma_f32_16x16x32_bf16 v[136:139], a[232:235], a[240:243], v[136:139]\n"
"ds_read_b64_tr_b16 a[224:225], v238 offset:0\n"
"v_mfma_f32_16x16x32_bf16 v[140:143], a[232:235], a[244:247], v[140:143]\n"
"ds_read_b64_tr_b16 a[226:227], v238 offset:6144\n"
"v_mfma_f32_16x16x32_bf16 v[144:147], a[232:235], a[248:251], v[144:147]\n"
"s_add_u32 m0, %[m0x], s63\n"
"s_add_u32 s67, %[x0], s56\n"
"s_add_u32 s75, s67, 9216\n"
"buffer_load_dwordx4 %[dX], %[rX], s67 offen offset:0 lds\n"
"v_mfma_f32_16x16x32_bf16 v[148:151], a[236:239], a[240:243], v[148:151]\n"
"ds_read_b64_tr_b16 a[228:229], v239 offset:0\n"
"v_mfma_f32_16x16x32_bf16 v[152:155], a[236:239], a[244:247], v[152:155]\n"
"ds_read_b64_tr_b16 a[230:231], v239 offset:6144\n"
"v_mfma_f32_16x16x32_bf16 v[156:159], a[236:239], a[248:251], v[156:159]\n"
"buffer_load_dwordx4 %[dX], %[rX], s75 offen offset:1024 lds\n"
"s_add_u32 s75, s67, 18432\n"
"s_waitcnt lgkmcnt(8)\n"
"v_mfma_f32_16x16x32_bf16 v[16:19], a[216:219], v[224:227], v[16:19]\n"
"ds_read_b64_tr_b16 a[232:233], v236 offset:128\n"
"s_waitcnt lgkmcnt(6)\n"
"v_mfma_f32_16x16x32_bf16 v[20:23], a[216:219], v[228:231], v[20:23]\n"
"ds_read_b64_tr_b16 a[234:235], v236 offset:6272\n"
"s_waitcnt lgkmcnt(6)\n"
"v_mfma_f32_16x16x32_bf16 v[24:27], a[216:219], v[232:235], v[24:27]\n"
"buffer_load_dwordx4 %[dX], %[rX], s75 offen offset:2048 lds\n"
"s_add_u32 s56, s56, 128\n"
"s_cmp_eq_u32 s56, 1280\n"
"s_cselect_b32 s56, 0, s56\n"
"s_add_u32 s63, s63, 12288\n"
"s_cmp_eq_u32 s63, 36864\n"
"s_cselect_b32 s63, 0, s63\n"
"v_mfma_f32_16x16x32_bf16 v[28:31], a[220:223], v[224:227], v[28:31]\n"
"ds_read_b64_tr_b16 a[236:237], v237 offset:128\n"
"v_mfma_f32_16x16x32_bf16 v[32:35], a[220:223], v[228:231], v[32:35]\n"
"ds_read_b64_tr_b16 a[238:239], v237 offset:6272\n"
"v_mfma_f32_16x16x32_bf16 v[36:39], a[220:223], v[232:235], v[36:39]\n"
"s_mov_b32 m0, s61\n"
"s_add_u32 s68, s66, 196608\n"
"buffer_load_dwordx4 %[dW0], %[rWq], s66 offen offset:0 lds\n"
"s_waitcnt lgkmcnt(6)\n"
"v_mfma_f32_16x16x32_bf16 v[40:43], a[224:227], v[224:227], v[40:43]\n"
"ds_read_b64_tr_b16 a[216:217], v238 offset:128\n"
"v_mfma_f32_16x16x32_bf16 v[44:47], a[224:227], v[228:231], v[44:47]\n"
"ds_read_b64_tr_b16 a[218:219], v238 offset:6272\n"
"v_mfma_f32_16x16x32_bf16 v[48:51], a[224:227], v[232:235], v[48:51]\n"
"s_waitcnt lgkmcnt(6)\n"
"v_mfma_f32_16x16x32_bf16 v[52:55], a[228:231], v[224:227], v[52:55]\n"
"ds_read_b64_tr_b16 a[220:221], v239 offset:128\n"
"v_mfma_f32_16x16x32_bf16 v[56:59], a[228:231], v[228:231], v[56:59]\n"
"ds_read_b64_tr_b16 a[222:223], v239 offset:6272\n"
"v_mfma_f32_16x16x32_bf16 v[60:63], a[228:231], v[232:235], v[60:63]\n"
"buffer_load_dwordx4 %[dW1], %[rWq], s66 offen offset:1024 lds\n"
"s_waitcnt lgkmcnt(6)\n"
"v_mfma_f32_16x16x32_bf16 v[64:67], a[232:235], v[224:227], v[64:67]\n"
"ds_read_b64_tr_b16 a[224:225], v236 offset:256\n"
"v_mfma_f32_16x16x32_bf16 v[68:71], a[232:235], v[228:231], v[68:71]\n"
"ds_read_b64_tr_b16 a[226:227], v236 offset:6400\n"
"v_mfma_f32_16x16x32_bf16 v[72:75], a[232:235], v[232:235], v[72:75]\n"
"s_waitcnt lgkmcnt(6)\n"
"v_mfma_f32_16x16x32_bf16 v[76:79], a[236:239], v[224:227], v[76:79]\n"
"ds_read_b64_tr_b16 a[228:229], v237 offset:256\n"
"v_mfma_f32_16x16x32_bf16 v[80:83], a[236:239], v[228:231], v[80:83]\n"
"ds_read_b64_tr_b16 a[230:231], v237 offset:6400\n"
"v_mfma_f32_16x16x32_bf16 v[84:87], a[236:239], v[232:235], v[84:87]\n"
"buffer_load_dwordx4 %[dW2], %[rWq], s66 offen offset:2048 lds\n"
"s_add_u32 m0, s61, 3072\n"
"s_waitcnt lgkmcnt(6)\n"
"v_mfma_f32_16x16x32_bf16 v[88:91], a[216:219], v[224:227], v[88:91]\n"
"ds_read_b64_tr_b16 a[232:233], v238 offset:256\n"
"v_mfma_f32_16x16x32_bf16 v[92:95], a[216:219], v[228:231], v[92:95]\n"
"ds_read_b64_tr_b16 a[234:235], v238 offset:6400\n"
"v_mfma_f32_16x16x32_bf16 v[96:99], a[216:219], v[232:235], v[96:99]\n"
"s_add_u32 s66, s66, 393216\n"
"buffer_load_dwordx4 %[dW0], %[rWq], s68 offen offset:0 lds\n"
"s_waitcnt lgkmcnt(6)\n"
"v_mfma_f32_16x16x32_bf16 v[100:103], a[220:223], v[224:227], v[100:103]\n"
"ds_read_b64_tr_b16 a[236:237], v239 offset:256\n"
"v_mfma_f32_16x16x32_bf16 v[104:107], a[220:223], v[228:231], v[104:107]\n"
"ds_read_b64_tr_b16 a[238:239], v239 offset:6400\n"
"v_mfma_f32_16x16x32_bf16 v[108:111], a[220:223], v[232:235], v[108:111]\n"
"buffer_load_dwordx4 %[dW1], %[rWq], s68 offen offset:1024 lds\n"
"s_waitcnt lgkmcnt(6)\n"
"v_mfma_f32_16x16x32_bf16 v[112:115], a[224:227], v[224:227], v[112:115]\n"
"s_add_u32 s61, %[m0b], s60\n"
"s_add_u32 s60, s60, 12288\n"
"s_cmp_eq_u32 s60, 61440\n"
"s_cselect_b32 s60, 0, s60\n"
"v_add_u32 v236, s60, %[rdW]\n"
"v_mfma_f32_16x16x32_bf16 v[116:119], a[224:227], v[228:231], v[116:119]\n"
"v_xor_b32 v237, 8, v236\n"
"v_xor_b32 v238, 64, v236\n"
"v_xor_b32 v239, 72, v236\n"
"v_add_u32 v240, s62, %[rdX]\n"
"s_add_u32 s62, s62, 12288\n"
"s_cmp_eq_u32 s62, 36864\n"
"s_cselect_b32 s62, 0, s62\n"
"v_mfma_f32_16x16x32_bf16 v[120:123], a[224:227], v[232:235], v[120:123]\n"
"s_waitcnt vmcnt(20) lgkmcnt(0)\n"
"s_barrier\n"
"ds_read_b64_tr_b16 a[216:217], v236 offset:0\n"
"ds_read_b64_tr_b16 a[218:219], v236 offset:6144\n"
"v_mfma_f32_16x16x32_bf16 v[124:127], a[228:231], v[224:227], v[124:127]\n"
"ds_read_b128 a[240:243], v240 offset:0\n"
"ds_read_b64_tr_b16 a[220:221], v237 offset:0\n"
"v_mfma_f32_16x16x32_bf16 v[128:131], a[228:231], v[228:231], v[128:131]\n"
"ds_read_b64_tr_b16 a[222:223], v237 offset:6144\n"
"ds_read_b128 a[244:247], v240 offset:2048\n"
"v_mfma_f32_16x16x32_bf16 v[132:135], a[228:231], v[232:235], v[132:135]\n"
"ds_read_b128 a[248:251], v240 offset:4096\n"
"v_mfma_f32_16x16x32_bf16 v[136:139], a[232:235], v[224:227], v[136:139]\n"
"ds_read_b64_tr_b16 a[224:225], v238 offset:0\n"
"v_mfma_f32_16x16x32_bf16 v[140:143], a[232:235], v[228:231], v[140:143]\n"
"ds_read_b64_tr_b16 a[226:227], v238 offset:6144\n"
"v_mfma_f32_16x16x32_bf16 v[144:147], a[232:235], v[232:235], v[144:147]\n"
"buffer_load_dwordx4 %[dW2], %[rWq], s68 offen offset:2048 lds\n"
"v_mfma_f32_16x16x32_bf16 v[148:151], a[236:239], v[224:227], v[148:151]\n"
"ds_read_b64_tr_b16 a[228:229], v239 offset:0\n"
"v_mfma_f32_16x16x32_bf16 v[152:155], a[236:239], v[228:231], v[152:155]\n"
"ds_read_b64_tr_b16 a[230:231], v239 offset:6144\n"
"v_mfma_f32_16x16x32_bf16 v[156:159], a[236:239], v[232:235], v[156:159]\n"
"s_sub_u32 s69, s69, 1\n"
"s_cmp_lg_u32 s69, 0\n"
"s_cbranch_scc1 La_%=\n"
"s_waitcnt lgkmcnt(8)\n"
"v_mfma_f32_16x16x32_bf16 v[16:19], a[216:219], a[240:243], v[16:19]\n"
"ds_read_b64_tr_b16 a[232:233], v236 offset:128\n"
"s_waitcnt lgkmcnt(6)\n"
"v_mfma_f32_16x16x32_bf16 v[20:23], a[216:219], a[244:247], v[20:23]\n"
"ds_read_b64_tr_b16 a[234:235], v236 offset:6272\n"
"s_waitcnt lgkmcnt(6)\n"
"v_mfma_f32_16x16x32_bf16 v[24:27], a[216:219], a[248:251], v[24:27]\n"
"s_mov_b32 m0, s61\n"
"s_add_u32 s68, s64, 3072\n"
"buffer_load_dwordx4 %[dKV0], %[rK], s64 offen offset:0 nt lds\n"
"v_mfma_f32_16x16x32_bf16 v[28:31], a[220:223], a[240:243], v[28:31]\n"
"ds_read_b64_tr_b16 a[236:237], v237 offset:128\n"
"v_mfma_f32_16x16x32_bf16 v[32:35], a[220:223], a[244:247], v[32:35]\n"
"ds_read_b64_tr_b16 a[238:239], v237 offset:6272\n"
"v_mfma_f32_16x16x32_bf16 v[36:39], a[220:223], a[248:251], v[36:39]\n"
"s_waitcnt lgkmcnt(6)\n"
"v_mfma_f32_16x16x32_bf16 v[40:43], a[224:227], a[240:243], v[40:43]\n"
"ds_read_b64_tr_b16 a[216:217], v238 offset:128\n"
"v_mfma_f32_16x16x32_bf16 v[44:47], a[224:227], a[244:247], v[44:47]\n"
"ds_read_b64_tr_b16 a[218:219], v238 offset:6272\n"
"v_mfma_f32_16x16x32_bf16 v[48:51], a[224:227], a[248:251], v[48:51]\n"
"buffer_load_dwordx4 %[dKV1], %[rK], s64 offen offset:1024 nt lds\n"
"s_waitcnt lgkmcnt(6)\n"
"v_mfma_f32_16x16x32_bf16 v[52:55], a[228:231], a[240:243], v[52:55]\n"
"ds_read_b64_tr_b16 a[220:221], v239 offset:128\n"
"v_mfma_f32_16x16x32_bf16 v[56:59], a[228:231], a[244:247], v[56:59]\n"
"ds_read_b64_tr_b16 a[222:223], v239 offset:6272\n"
"v_mfma_f32_16x16x32_bf16 v[60:63], a[228:231], a[248:251], v[60:63]\n"
"s_waitcnt lgkmcnt(6)\n"
"v_mfma_f32_16x16x32_bf16 v[64:67], a[232:235], a[240:243], v[64:67]\n"
"ds_read_b64_tr_b16 a[224:225], v236 offset:256\n"
"v_mfma_f32_16x16x32_bf16 v[68:71], a[232:235], a[244:247], v[68:71]\n"
"ds_read_b64_tr_b16 a[226:227], v236 offset:6400\n"
"v_mfma_f32_16x16x32_bf16 v[72:75], a[232:235], a[248:251], v[72:75]\n"
"buffer_load_dwordx4 %[dKV2], %[rK], s64 offen offset:2048 nt lds\n"
"s_add_u32 m0, s61, 3072\n"
"s_waitcnt lgkmcnt(6)\n"
"v_mfma_f32_16x16x32_bf16 v[76:79], a[236:239], a[240:243], v[76:79]\n"
"ds_read_b64_tr_b16 a[228:229], v237 offset:256\n"
"v_mfma_f32_16x16x32_bf16 v[80:83], a[236:239], a[244:247], v[80:83]\n"
"ds_read_b64_tr_b16 a[230:231], v237 offset:6400\n"
"v_mfma_f32_16x16x32_bf16 v[84:87], a[236:239], a[248:251], v[84:87]\n"
"s_waitcnt lgkmcnt(6)\n"
"v_mfma_f32_16x16x32_bf16 v[88:91], a[216:219], a[240:243], v[88:91]\n"
"ds_read_b64_tr_b16 a[232:233], v238 offset:256\n"
"v_mfma_f32_16x16x32_bf16 v[92:95], a[216:219], a[244:247], v[92:95]\n"
"ds_read_b64_tr_b16 a[234:235], v238 offset:6400\n"
"v_mfma_f32_16x16x32_bf16 v[96:99], a[216:219], a[248:251], v[96:99]\n"
"s_add_u32 s64, s64, 12288\n"
"buffer_load_dwordx4 %[dKV0], %[rK], s68 offen offset:0 nt lds\n"
"s_waitcnt lgkmcnt(6)\n"
"v_mfma_f32_16x16x32_bf16 v[100:103], a[220:223], a[240:243], v[100:103]\n"
"ds_read_b64_tr_b16 a[236:237], v239 offset:256\n"
"v_mfma_f32_16x16x32_bf16 v[104:107], a[220:223], a[244:247], v[104:107]\n"
"ds_read_b64_tr_b16 a[238:239], v239 offset:6400\n"
"v_mfma_f32_16x16x32_bf16 v[108:111], a[220:223], a[248:251], v[108:111]\n"
"buffer_load_dwordx4 %[dKV1], %[rK], s68 offen offset:1024 nt lds\n"
"s_waitcnt lgkmcnt(6)\n"
"v_mfma_f32_16x16x32_bf16 v[112:115], a[224:227], a[240:243], v[112:115]\n"
"s_add_u32 s61, %[m0b], s60\n"
"s_add_u32 s60, s60, 12288\n"
"s_cmp_eq_u32 s60, 61440\n"
"s_cselect_b32 s60, 0, s60\n"
"v_add_u32 v236, s60, %[rdW]\n"
"v_mfma_f32_16x16x32_bf16 v[116:119], a[224:227], a[244:247], v[116:119]\n"
"v_xor_b32 v237, 8, v236\n"
"v_xor_b32 v238, 64, v236\n"
"v_xor_b32 v239, 72, v236\n"
"v_xor_b32 v240, 64, v240\n"
"v_mfma_f32_16x16x32_bf16 v[120:123], a[224:227], a[248:251], v[120:123]\n"
"s_waitcnt vmcnt(20) lgkmcnt(0)\n"
"s_barrier\n"
"ds_read_b64_tr_b16 a[216:217], v236 offset:0\n"
"ds_read_b64_tr_b16 a[218:219], v236 offset:6144\n"
"v_mfma_f32_16x16x32_bf16 v[124:127], a[228:231], a[240:243], v[124:127]\n"
"ds_read_b128 v[224:227], v240 offset:0\n"
"ds_read_b64_tr_b16 a[220:221], v237 offset:0\n"
"v_mfma_f32_16x16x32_bf16 v[128:131], a[228:231], a[244:247], v[128:131]\n"
"ds_read_b64_tr_b16 a[222:223], v237 offset:6144\n"
"ds_read_b128 v[228:231], v240 offset:2048\n"
"v_mfma_f32_16x16x32_bf16 v[132:135], a[228:231], a[248:251], v[132:135]\n"
"ds_read_b128 v[232:235], v240 offset:4096\n"
"v_mfma_f32_16x16x32_bf16 v[136:139], a[232:235], a[240:243], v[136:139]\n"
"ds_read_b64_tr_b16 a[224:225], v238 offset:0\n"
"v_mfma_f32_16x16x32_bf16 v[140:143], a[232:235], a[244:247], v[140:143]\n"
"ds_read_b64_tr_b16 a[226:227], v238 offset:6144\n"
"v_mfma_f32_16x16x32_bf16 v[144:147], a[232:235], a[248:251], v[144:147]\n"
"buffer_load_dwordx4 %[dKV2], %[rK], s68 offen offset:2048 nt lds\n"
"v_mfma_f32_16x16x32_bf16 v[148:151], a[236:239], a[240:243], v[148:151]\n"
"ds_read_b64_tr_b16 a[228:229], v239 offset:0\n"
"v_mfma_f32_16x16x32_bf16 v[152:155], a[236:239], a[244:247], v[152:155]\n"
"ds_read_b64_tr_b16 a[230:231], v239 offset:6144\n"
"v_mfma_f32_16x16x32_bf16 v[156:159], a[236:239], a[248:251], v[156:159]\n"
"s_waitcnt lgkmcnt(8)\n"
"v_mfma_f32_16x16x32_bf16 v[16:19], a[216:219], v[224:227], v[16:19]\n"
"ds_read_b64_tr_b16 a[232:233], v236 offset:128\n"
"s_waitcnt lgkmcnt(6)\n"
"v_mfma_f32_16x16x32_bf16 v[20:23], a[216:219], v[228:231], v[20:23]\n"
"ds_read_b64_tr_b16 a[234:235], v236 offset:6272\n"
"s_waitcnt lgkmcnt(6)\n"
"v_mfma_f32_16x16x32_bf16 v[24:27], a[216:219], v[232:235], v[24:27]\n"
"s_mov_b32 m0, s61\n"
"s_add_u32 s57, s57, 1536\n"
"s_mul_i32 s66, s56, 6144\n"
"s_add_u32 s66, s66, s57\n"
"s_add_u32 s68, s66, 196608\n"
"buffer_load_dwordx4 %[dW0], %[rWq], s66 offen offset:0 lds\n"
"v_mfma_f32_16x16x32_bf16 v[28:31], a[220:223], v[224:227], v[28:31]\n"
"ds_read_b64_tr_b16 a[236:237], v237 offset:128\n"
"v_mfma_f32_16x16x32_bf16 v[32:35], a[220:223], v[228:231], v[32:35]\n"
"ds_read_b64_tr_b16 a[238:239], v237 offset:6272\n"
"v_mfma_f32_16x16x32_bf16 v[36:39], a[220:223], v[232:235], v[36:39]\n"
"buffer_load_dwordx4 %[dW1], %[rWq], s66 offen offset:1024 lds\n"
"s_waitcnt lgkmcnt(6)\n"
"v_mfma_f32_16x16x32_bf16 v[40:43], a[224:227], v[224:227], v[40:43]\n"
"ds_read_b64_tr_b16 a[216:217], v238 offset:128\n"
"v_mfma_f32_16x16x32_bf16 v[44:47], a[224:227], v[228:231], v[44:47]\n"
"ds_read_b64_tr_b16 a[218:219], v238 offset:6272\n"
"v_mfma_f32_16x16x32_bf16 v[48:51], a[224:227], v[232:235], v[48:51]\n"
"s_waitcnt lgkmcnt(6)\n"
"v_mfma_f32_16x16x32_bf16 v[52:55], a[228:231], v[224:227], v[52:55]\n"
"ds_read_b64_tr_b16 a[220:221], v239 offset:128\n"
"v_mfma_f32_16x16x32_bf16 v[56:59], a[228:231], v[228:231], v[56:59]\n"
"ds_read_b64_tr_b16 a[222:223], v239 offset:6272\n"
"v_mfma_f32_16x16x32_bf16 v[60:63], a[228:231], v[232:235], v[60:63]\n"
"buffer_load_dwordx4 %[dW2], %[rWq], s66 offen offset:2048 lds\n"
"s_add_u32 m0, s61, 3072\n"
"s_waitcnt lgkmcnt(6)\n"
"v_mfma_f32_16x16x32_bf16 v[64:67], a[232:235], v[224:227], v[64:67]\n"
"ds_read_b64_tr_b16 a[224:225], v236 offset:256\n"
"v_mfma_f32_16x16x32_bf16 v[68:71], a[232:235], v[228:231], v[68:71]\n"
"ds_read_b64_tr_b16 a[226:227], v236 offset:6400\n"
"v_mfma_f32_16x16x32_bf16 v[72:75], a[232:235], v[232:235], v[72:75]\n"
"s_add_u32 s66, s66, 393216\n"
"buffer_load_dwordx4 %[dW0], %[rWq], s68 offen offset:0 lds\n"
"s_waitcnt lgkmcnt(6)\n"
"v_mfma_f32_16x16x32_bf16 v[76:79], a[236:239], v[224:227], v[76:79]\n"
"ds_read_b64_tr_b16 a[228:229], v237 offset:256\n"
"v_mfma_f32_16x16x32_bf16 v[80:83], a[236:239], v[228:231], v[80:83]\n"
"ds_read_b64_tr_b16 a[230:231], v237 offset:6400\n"
"v_mfma_f32_16x16x32_bf16 v[84:87], a[236:239], v[232:235], v[84:87]\n"
"s_waitcnt lgkmcnt(6)\n"
"v_mfma_f32_16x16x32_bf16 v[88:91], a[216:219], v[224:227], v[88:91]\n"
"ds_read_b64_tr_b16 a[232:233], v238 offset:256\n"
"v_mfma_f32_16x16x32_bf16 v[92:95], a[216:219], v[228:231], v[92:95]\n"
"ds_read_b64_tr_b16 a[234:235], v238 offset:6400\n"
"v_mfma_f32_16x16x32_bf16 v[96:99], a[216:219], v[232:235], v[96:99]\n"
"buffer_load_dwordx4 %[dW1], %[rWq], s68 offen offset:1024 lds\n"
"s_waitcnt lgkmcnt(6)\n"
"v_mfma_f32_16x16x32_bf16 v[100:103], a[220:223], v[224:227], v[100:103]\n"
"ds_read_b64_tr_b16 a[236:237], v239 offset:256\n"
"v_mfma_f32_16x16x32_bf16 v[104:107], a[220:223], v[228:231], v[104:107]\n"
"ds_read_b64_tr_b16 a[238:239], v239 offset:6400\n"
"v_mfma_f32_16x16x32_bf16 v[108:111], a[220:223], v[232:235], v[108:111]\n"
"buffer_load_dwordx4 %[dW2], %[rWq], s68 offen offset:2048 lds\n"
"s_waitcnt lgkmcnt(6)\n"
"v_mfma_f32_16x16x32_bf16 v[112:115], a[224:227], v[224:227], v[112:115]\n"
"s_add_u32 s61, %[m0b], s60\n"
"s_add_u32 s60, s60, 12288\n"
"s_cmp_eq_u32 s60, 61440\n"
"s_cselect_b32 s60, 0, s60\n"
"v_add_u32 v236, s60, %[rdW]\n"
"v_mfma_f32_16x16x32_bf16 v[116:119], a[224:227], v[228:231], v[116:119]\n"
"v_xor_b32 v237, 8, v236\n"
"v_xor_b32 v238, 64, v236\n"
"v_xor_b32 v239, 72, v236\n"
"v_add_u32 v240, s62, %[rdX]\n"
"s_add_u32 s62, s62, 12288\n"
"s_cmp_eq_u32 s62, 36864\n"
"s_cselect_b32 s62, 0, s62\n"
"v_mfma_f32_16x16x32_bf16 v[120:123], a[224:227], v[232:235], v[120:123]\n"
"s_waitcnt vmcnt(18) lgkmcnt(0)\n"
"s_barrier\n"
"ds_read_b64_tr_b16 a[216:217], v236 offset:0\n"
"ds_read_b64_tr_b16 a[218:219], v236 offset:6144\n"
"v_mfma_f32_16x16x32_bf16 v[124:127], a[228:231], v[224:227], v[124:127]\n"
"ds_read_b128 a[240:243], v240 offset:0\n"
"ds_read_b64_tr_b16 a[220:221], v237 offset:0\n"
"v_mfma_f32_16x16x32_bf16 v[128:131], a[228:231], v[228:231], v[128:131]\n"
"ds_read_b64_tr_b16 a[222:223], v237 offset:6144\n"
"ds_read_b128 a[244:247], v240 offset:2048\n"
"v_mfma_f32_16x16x32_bf16 v[132:135], a[228:231], v[232:235], v[132:135]\n"
"ds_read_b128 a[248:251], v240 offset:4096\n"
"v_mfma_f32_16x16x32_bf16 v[136:139], a[232:235], v[224:227], v[136:139]\n"
"ds_read_b64_tr_b16 a[224:225], v238 offset:0\n"
"v_mfma_f32_16x16x32_bf16 v[140:143], a[232:235], v[228:231], v[140:143]\n"
"ds_read_b64_tr_b16 a[226:227], v238 offset:6144\n"
"v_mfma_f32_16x16x32_bf16 v[144:147], a[232:235], v[232:235], v[144:147]\n"
"s_add_u32 m0, %[m0x], s63\n"
"s_add_u32 s67, %[x0], s56\n"
"s_add_u32 s75, s67, 9216\n"
"buffer_load_dwordx4 %[dX], %[rX], s67 offen offset:0 lds\n"
"v_mfma_f32_16x16x32_bf16 v[148:151], a[236:239], v[224:227], v[148:151]\n"
"ds_read_b64_tr_b16 a[228:229], v239 offset:0\n"
"v_mfma_f32_16x16x32_bf16 v[152:155], a[236:239], v[228:231], v[152:155]\n"
"ds_read_b64_tr_b16 a[230:231], v239 offset:6144\n"
"v_mfma_f32_16x16x32_bf16 v[156:159], a[236:239], v[232:235], v[156:159]\n"
"buffer_load_dwordx4 %[dX], %[rX], s75 offen offset:1024 lds\n"
"s_add_u32 s75, s67, 18432\n"
"s_waitcnt lgkmcnt(8)\n"
"v_mfma_f32_16x16x32_bf16 v[16:19], a[216:219], a[240:243], v[16:19]\n"
"ds_read_b64_tr_b16 a[232:233], v236 offset:128\n"
"s_waitcnt lgkmcnt(6)\n"
"v_mfma_f32_16x16x32_bf16 v[20:23], a[216:219], a[244:247], v[20:23]\n"
"ds_read_b64_tr_b16 a[234:235], v236 offset:6272\n"
"s_waitcnt lgkmcnt(6)\n"
"v_mfma_f32_16x16x32_bf16 v[24:27], a[216:219], a[248:251], v[24:27]\n"
"buffer_load_dwordx4 %[dX], %[rX], s75 offen offset:2048 lds\n"
"s_add_u32 s56, s56, 128\n"
"s_cmp_eq_u32 s56, 1280\n"
"s_cselect_b32 s56, 0, s56\n"
"s_add_u32 s63, s63, 12288\n"
"s_cmp_eq_u32 s63, 36864\n"
"s_cselect_b32 s63, 0, s63\n"
"v_mfma_f32_16x16x32_bf16 v[28:31], a[220:223], a[240:243], v[28:31]\n"
"ds_read_b64_tr_b16 a[236:237], v237 offset:128\n"
"v_mfma_f32_16x16x32_bf16 v[32:35], a[220:223], a[244:247], v[32:35]\n"
"ds_read_b64_tr_b16 a[238:239], v237 offset:6272\n"
"v_mfma_f32_16x16x32_bf16 v[36:39], a[220:223], a[248:251], v[36:39]\n"
"s_mov_b32 m0, s61\n"
"s_add_u32 s68, s65, 3072\n"
"buffer_load_dwordx4 %[dKV0], %[rV], s65 offen offset:0 nt lds\n"
"s_waitcnt lgkmcnt(6)\n"
"v_mfma_f32_16x16x32_bf16 v[40:43], a[224:227], a[240:243], v[40:43]\n"
"ds_read_b64_tr_b16 a[216:217], v238 offset:128\n"
"v_mfma_f32_16x16x32_bf16 v[44:47], a[224:227], a[244:247], v[44:47]\n"
"ds_read_b64_tr_b16 a[218:219], v238 offset:6272\n"
"v_mfma_f32_16x16x32_bf16 v[48:51], a[224:227], a[248:251], v[48:51]\n"
"s_waitcnt lgkmcnt(6)\n"
"v_mfma_f32_16x16x32_bf16 v[52:55], a[228:231], a[240:243], v[52:55]\n"
"ds_read_b64_tr_b16 a[220:221], v239 offset:128\n"
"v_mfma_f32_16x16x32_bf16 v[56:59], a[228:231], a[244:247], v[56:59]\n"
"ds_read_b64_tr_b16 a[222:223], v239 offset:6272\n"
"v_mfma_f32_16x16x32_bf16 v[60:63], a[228:231], a[248:251], v[60:63]\n"
"buffer_load_dwordx4 %[dKV1], %[rV], s65 offen offset:1024 nt lds\n"
"s_waitcnt lgkmcnt(6)\n"
"v_mfma_f32_16x16x32_bf16 v[64:67], a[232:235], a[240:243], v[64:67]\n"
"ds_read_b64_tr_b16 a[224:225], v236 offset:256\n"
"v_mfma_f32_16x16x32_bf16 v[68:71], a[232:235], a[244:247], v[68:71]\n"
"ds_read_b64_tr_b16 a[226:227], v236 offset:6400\n"
"v_mfma_f32_16x16x32_bf16 v[72:75], a[232:235], a[248:251], v[72:75]\n"
"s_waitcnt lgkmcnt(6)\n"
"v_mfma_f32_16x16x32_bf16 v[76:79], a[236:239], a[240:243], v[76:79]\n"
"ds_read_b64_tr_b16 a[228:229], v237 offset:256\n"
"v_mfma_f32_16x16x32_bf16 v[80:83], a[236:239], a[244:247], v[80:83]\n"
"ds_read_b64_tr_b16 a[230:231], v237 offset:6400\n"
"v_mfma_f32_16x16x32_bf16 v[84:87], a[236:239], a[248:251], v[84:87]\n"
"buffer_load_dwordx4 %[dKV2], %[rV], s65 offen offset:2048 nt lds\n"
"s_add_u32 m0, s61, 3072\n"
"s_waitcnt lgkmcnt(6)\n"
"v_mfma_f32_16x16x32_bf16 v[88:91], a[216:219], a[240:243], v[88:91]\n"
"ds_read_b64_tr_b16 a[232:233], v238 offset:256\n"
"v_mfma_f32_16x16x32_bf16 v[92:95], a[216:219], a[244:247], v[92:95]\n"
"ds_read_b64_tr_b16 a[234:235], v238 offset:6400\n"
"v_mfma_f32_16x16x32_bf16 v[96:99], a[216:219], a[248:251], v[96:99]\n"
"s_add_u32 s65, s65, 12288\n"
"buffer_load_dwordx4 %[dKV0], %[rV], s68 offen offset:0 nt lds\n"
"s_waitcnt lgkmcnt(6)\n"
"v_mfma_f32_16x16x32_bf16 v[100:103], a[220:223], a[240:243], v[100:103]\n"
"ds_read_b64_tr_b16 a[236:237], v239 offset:256\n"
"v_mfma_f32_16x16x32_bf16 v[104:107], a[220:223], a[244:247], v[104:107]\n"
"ds_read_b64_tr_b16 a[238:239], v239 offset:6400\n"
"v_mfma_f32_16x16x32_bf16 v[108:111], a[220:223], a[248:251], v[108:111]\n"
"buffer_load_dwordx4 %[dKV1], %[rV], s68 offen offset:1024 nt lds\n"
"s_waitcnt lgkmcnt(6)\n"
"v_mfma_f32_16x16x32_bf16 v[112:115], a[224:227], a[240:243], v[112:115]\n"
"s_add_u32 s61, %[m0b], s60\n"
"s_add_u32 s60, s60, 12288\n"
"s_cmp_eq_u32 s60, 61440\n"
"s_cselect_b32 s60, 0, s60\n"
"v_add_u32 v236, s60, %[rdW]\n"
"v_mfma_f32_16x16x32_bf16 v[116:119], a[224:227], a[244:247], v[116:119]\n"
"v_xor_b32 v237, 8, v236\n"
"v_xor_b32 v238, 64, v236\n"
"v_xor_b32 v239, 72, v236\n"
"v_xor_b32 v240, 64, v240\n"
"v_mfma_f32_16x16x32_bf16 v[120:123], a[224:227], a[248:251], v[120:123]\n"
"s_waitcnt vmcnt(20) lgkmcnt(0)\n"
"s_barrier\n"
"ds_read_b64_tr_b16 a[216:217], v236 offset:0\n"
"ds_read_b64_tr_b16 a[218:219], v236 offset:6144\n"
"v_mfma_f32_16x16x32_bf16 v[124:127], a[228:231], a[240:243], v[124:127]\n"
"ds_read_b128 v[224:227], v240 offset:0\n"
"ds_read_b64_tr_b16 a[220:221], v237 offset:0\n"
"v_mfma_f32_16x16x32_bf16 v[128:131], a[228:231], a[244:247], v[128:131]\n"
"ds_read_b64_tr_b16 a[222:223], v237 offset:6144\n"
"ds_read_b128 v[228:231], v240 offset:2048\n"
"v_mfma_f32_16x16x32_bf16 v[132:135], a[228:231], a[248:251], v[132:135]\n"
"ds_read_b128 v[232:235], v240 offset:4096\n"
"v_mfma_f32_16x16x32_bf16 v[136:139], a[232:235], a[240:243], v[136:139]\n"
"ds_read_b64_tr_b16 a[224:225], v238 offset:0\n"
"v_mfma_f32_16x16x32_bf16 v[140:143], a[232:235], a[244:247], v[140:143]\n"
"ds_read_b64_tr_b16 a[226:227], v238 offset:6144\n"
"v_mfma_f32_16x16x32_bf16 v[144:147], a[232:235], a[248:251], v[144:147]\n"
"buffer_load_dwordx4 %[dKV2], %[rV], s68 offen offset:2048 nt lds\n"
"v_mfma_f32_16x16x32_bf16 v[148:151], a[236:239], a[240:243], v[148:151]\n"
"ds_read_b64_tr_b16 a[228:229], v239 offset:0\n"
"v_mfma_f32_16x16x32_bf16 v[152:155], a[236:239], a[244:247], v[152:155]\n"
"ds_read_b64_tr_b16 a[230:231], v239 offset:6144\n"
"v_mfma_f32_16x16x32_bf16 v[156:159], a[236:239], a[248:251], v[156:159]\n"
"s_waitcnt lgkmcnt(8)\n"
"v_mfma_f32_16x16x32_bf16 v[16:19], a[216:219], v[224:227], v[16:19]\n"
"ds_read_b64_tr_b16 a[232:233], v236 offset:128\n"
"s_waitcnt lgkmcnt(6)\n"
"v_mfma_f32_16x16x32_bf16 v[20:23], a[216:219], v[228:231], v[20:23]\n"
"ds_read_b64_tr_b16 a[234:235], v236 offset:6272\n"
"s_waitcnt lgkmcnt(6)\n"
"v_mfma_f32_16x16x32_bf16 v[24:27], a[216:219], v[232:235], v[24:27]\n"
"s_mov_b32 m0, s61\n"
"s_add_u32 s68, s64, 3072\n"
"buffer_load_dwordx4 %[dKV0], %[rK], s64 offen offset:0 nt lds\n"
"v_mfma_f32_16x16x32_bf16 v[28:31], a[220:223], v[224:227], v[28:31]\n"
"ds_read_b64_tr_b16 a[236:237], v237 offset:128\n"
"v_mfma_f32_16x16x32_bf16 v[32:35], a[220:223], v[228:231], v[32:35]\n"
"ds_read_b64_tr_b16 a[238:239], v237 offset:6272\n"
"v_mfma_f32_16x16x32_bf16 v[36:39], a[220:223], v[232:235], v[36:39]\n"
"s_waitcnt lgkmcnt(6)\n"
"v_mfma_f32_16x16x32_bf16 v[40:43], a[224:227], v[224:227], v[40:43]\n"
"ds_read_b64_tr_b16 a[216:217], v238 offset:128\n"
"v_mfma_f32_16x16x32_bf16 v[44:47], a[224:227], v[228:231], v[44:47]\n"
"ds_read_b64_tr_b16 a[218:219], v238 offset:6272\n"
"v_mfma_f32_16x16x32_bf16 v[48:51], a[224:227], v[232:235], v[48:51]\n"
"buffer_load_dwordx4 %[dKV1], %[rK], s64 offen offset:1024 nt lds\n"
"s_waitcnt lgkmcnt(6)\n"
"v_mfma_f32_16x16x32_bf16 v[52:55], a[228:231], v[224:227], v[52:55]\n"
"ds_read_b64_tr_b16 a[220:221], v239 offset:128\n"
"v_mfma_f32_16x16x32_bf16 v[56:59], a[228:231], v[228:231], v[56:59]\n"
"ds_read_b64_tr_b16 a[222:223], v239 offset:6272\n"
"v_mfma_f32_16x16x32_bf16 v[60:63], a[228:231], v[232:235], v[60:63]\n"
"s_waitcnt lgkmcnt(6)\n"
"v_mfma_f32_16x16x32_bf16 v[64:67], a[232:235], v[224:227], v[64:67]\n"
"ds_read_b64_tr_b16 a[224:225], v236 offset:256\n"
"v_mfma_f32_16x16x32_bf16 v[68:71], a[232:235], v[228:231], v[68:71]\n"
"ds_read_b64_tr_b16 a[226:227], v236 offset:6400\n"
"v_mfma_f32_16x16x32_bf16 v[72:75], a[232:235], v[232:235], v[72:75]\n"
"buffer_load_dwordx4 %[dKV2], %[rK], s64 offen offset:2048 nt lds\n"
"s_add_u32 m0, s61, 3072\n"
"s_waitcnt lgkmcnt(6)\n"
"v_mfma_f32_16x16x32_bf16 v[76:79], a[236:239], v[224:227], v[76:79]\n"
"ds_read_b64_tr_b16 a[228:229], v237 offset:256\n"
"v_mfma_f32_16x16x32_bf16 v[80:83], a[236:239], v[228:231], v[80:83]\n"
"ds_read_b64_tr_b16 a[230:231], v237 offset:6400\n"
"v_mfma_f32_16x16x32_bf16 v[84:87], a[236:239], v[232:235], v[84:87]\n"
"s_waitcnt lgkmcnt(6)\n"
"v_mfma_f32_16x16x32_bf16 v[88:91], a[216:219], v[224:227], v[88:91]\n"
"ds_read_b64_tr_b16 a[232:233], v238 offset:256\n"
"v_mfma_f32_16x16x32_bf16 v[92:95], a[216:219], v[228:231], v[92:95]\n"
"ds_read_b64_tr_b16 a[234:235], v238 offset:6400\n"
"v_mfma_f32_16x16x32_bf16 v[96:99], a[216:219], v[232:235], v[96:99]\n"
"s_add_u32 s64, s64, 12288\n"
"buffer_load_dwordx4 %[dKV0], %[rK], s68 offen offset:0 nt lds\n"
"s_waitcnt lgkmcnt(6)\n"
"v_mfma_f32_16x16x32_bf16 v[100:103], a[220:223], v[224:227], v[100:103]\n"
"ds_read_b64_tr_b16 a[236:237], v239 offset:256\n"
"v_mfma_f32_16x16x32_bf16 v[104:107], a[220:223], v[228:231], v[104:107]\n"
"ds_read_b64_tr_b16 a[238:239], v239 offset:6400\n"
"v_mfma_f32_16x16x32_bf16 v[108:111], a[220:223], v[232:235], v[108:111]\n"
"buffer_load_dwordx4 %[dKV1], %[rK], s68 offen offset:1024 nt lds\n"
"s_waitcnt lgkmcnt(6)\n"
"v_mfma_f32_16x16x32_bf16 v[112:115], a[224:227], v[224:227], v[112:115]\n"
"s_add_u32 s61, %[m0b], s60\n"
"s_add_u32 s60, s60, 12288\n"
"s_cmp_eq_u32 s60, 61440\n"
"s_cselect_b32 s60, 0, s60\n"
"v_add_u32 v236, s60, %[rdK]\n"
"v_mfma_f32_16x16x32_bf16 v[116:119], a[224:227], v[228:231], v[116:119]\n"
"v_xor_b32 v237, 64, v236\n"
"v_mfma_f32_16x16x32_bf16 v[120:123], a[224:227], v[232:235], v[120:123]\n"
"s_waitcnt vmcnt(20) lgkmcnt(0)\n"
"s_barrier\n"
"ds_read_b128 a[216:219], v236 offset:0\n"
"v_mfma_f32_16x16x32_bf16 v[124:127], a[228:231], v[224:227], v[124:127]\n"
"ds_read_b128 a[220:223], v237 offset:0\n"
"v_mfma_f32_16x16x32_bf16 v[128:131], a[228:231], v[228:231], v[128:131]\n"
"v_mfma_f32_16x16x32_bf16 v[132:135], a[228:231], v[232:235], v[132:135]\n"
"v_mfma_f32_16x16x32_bf16 v[136:139], a[232:235], v[224:227], v[136:139]\n"
"ds_read_b128 a[224:227], v236 offset:128\n"
"v_mfma_f32_16x16x32_bf16 v[140:143], a[232:235], v[228:231], v[140:143]\n"
"v_mfma_f32_16x16x32_bf16 v[144:147], a[232:235], v[232:235], v[144:147]\n"
"buffer_load_dwordx4 %[dKV2], %[rK], s68 offen offset:2048 nt lds\n"
"v_mfma_f32_16x16x32_bf16 v[148:151], a[236:239], v[224:227], v[148:151]\n"
"ds_read_b128 a[228:231], v237 offset:128\n"
"v_mfma_f32_16x16x32_bf16 v[152:155], a[236:239], v[228:231], v[152:155]\n"
"v_mfma_f32_16x16x32_bf16 v[156:159], a[236:239], v[232:235], v[156:159]\n"
"v_pk_mul_f32 v[16:17], v[16:17], s[78:79] op_sel_hi:[1,0]\n"
"v_pk_mul_f32 v[18:19], v[18:19], s[78:79] op_sel_hi:[1,0]\n"
"v_pk_mul_f32 v[20:21], v[20:21], s[78:79] op_sel_hi:[1,0]\n"
"v_pk_mul_f32 v[22:23], v[22:23], s[78:79] op_sel_hi:[1,0]\n"
"v_pk_mul_f32 v[24:25], v[24:25], s[78:79] op_sel_hi:[1,0]\n"
"v_pk_mul_f32 v[26:27], v[26:27], s[78:79] op_sel_hi:[1,0]\n"
"v_pk_mul_f32 v[28:29], v[28:29], s[78:79] op_sel_hi:[1,0]\n"
"v_pk_mul_f32 v[30:31], v[30:31], s[78:79] op_sel_hi:[1,0]\n"
"v_pk_mul_f32 v[32:33], v[32:33], s[78:79] op_sel_hi:[1,0]\n"
"v_pk_mul_f32 v[34:35], v[34:35], s[78:79] op_sel_hi:[1,0]\n"
"v_pk_mul_f32 v[36:37], v[36:37], s[78:79] op_sel_hi:[1,0]\n"
"v_pk_mul_f32 v[38:39], v[38:39], s[78:79] op_sel_hi:[1,0]\n"
"v_pk_mul_f32 v[40:41], v[40:41], s[78:79] op_sel_hi:[1,0]\n"
"v_pk_mul_f32 v[42:43], v[42:43], s[78:79] op_sel_hi:[1,0]\n"
"v_pk_mul_f32 v[44:45], v[44:45], s[78:79] op_sel_hi:[1,0]\n"
"v_pk_mul_f32 v[46:47], v[46:47], s[78:79] op_sel_hi:[1,0]\n"
"v_pk_mul_f32 v[48:49], v[48:49], s[78:79] op_sel_hi:[1,0]\n"
"v_pk_mul_f32 v[50:51], v[50:51], s[78:79] op_sel_hi:[1,0]\n"
"v_pk_mul_f32 v[52:53], v[52:53], s[78:79] op_sel_hi:[1,0]\n"
"v_pk_mul_f32 v[54:55], v[54:55], s[78:79] op_sel_hi:[1,0]\n"
"v_pk_mul_f32 v[56:57], v[56:57], s[78:79] op_sel_hi:[1,0]\n"
"v_pk_mul_f32 v[58:59], v[58:59], s[78:79] op_sel_hi:[1,0]\n"
"v_pk_mul_f32 v[60:61], v[60:61], s[78:79] op_sel_hi:[1,0]\n"
"v_pk_mul_f32 v[62:63], v[62:63], s[78:79] op_sel_hi:[1,0]\n"
"v_pk_mul_f32 v[64:65], v[64:65], s[78:79] op_sel_hi:[1,0]\n"
"v_pk_mul_f32 v[66:67], v[66:67], s[78:79] op_sel_hi:[1,0]\n"
"v_pk_mul_f32 v[68:69], v[68:69], s[78:79] op_sel_hi:[1,0]\n"
"v_pk_mul_f32 v[70:71], v[70:71], s[78:79] op_sel_hi:[1,0]\n"
"v_pk_mul_f32 v[72:73], v[72:73], s[78:79] op_sel_hi:[1,0]\n"
"v_pk_mul_f32 v[74:75], v[74:75], s[78:79] op_sel_hi:[1,0]\n"
"v_pk_mul_f32 v[76:77], v[76:77], s[78:79] op_sel_hi:[1,0]\n"
"v_pk_mul_f32 v[78:79], v[78:79], s[78:79] op_sel_hi:[1,0]\n"
"v_pk_mul_f32 v[80:81], v[80:81], s[78:79] op_sel_hi:[1,0]\n"
"v_pk_mul_f32 v[82:83], v[82:83], s[78:79] op_sel_hi:[1,0]\n"
"v_pk_mul_f32 v[84:85], v[84:85], s[78:79] op_sel_hi:[1,0]\n"
"v_pk_mul_f32 v[86:87], v[86:87], s[78:79] op_sel_hi:[1,0]\n"
"v_pk_mul_f32 v[88:89], v[88:89], s[78:79] op_sel_hi:[1,0]\n"
"v_pk_mul_f32 v[90:91], v[90:91], s[78:79] op_sel_hi:[1,0]\n"
"v_pk_mul_f32 v[92:93], v[92:93], s[78:79] op_sel_hi:[1,0]\n"
"v_pk_mul_f32 v[94:95], v[94:95], s[78:79] op_sel_hi:[1,0]\n"
"v_pk_mul_f32 v[96:97], v[96:97], s[78:79] op_sel_hi:[1,0]\n"
"v_pk_mul_f32 v[98:99], v[98:99], s[78:79] op_sel_hi:[1,0]\n"
"v_pk_mul_f32 v[100:101], v[100:101], s[78:79] op_sel_hi:[1,0]\n"
"v_pk_mul_f32 v[102:103], v[102:103], s[78:79] op_sel_hi:[1,0]\n"
"v_pk_mul_f32 v[104:105], v[104:105], s[78:79] op_sel_hi:[1,0]\n"
"v_pk_mul_f32 v[106:107], v[106:107], s[78:79] op_sel_hi:[1,0]\n"
"v_pk_mul_f32 v[108:109], v[108:109], s[78:79] op_sel_hi:[1,0]\n"
"v_pk_mul_f32 v[110:111], v[110:111], s[78:79] op_sel_hi:[1,0]\n"
"v_pk_mul_f32 v[112:113], v[112:113], s[78:79] op_sel_hi:[1,0]\n"
"v_pk_mul_f32 v[114:115], v[114:115], s[78:79] op_sel_hi:[1,0]\n"
"v_pk_mul_f32 v[116:117], v[116:117], s[78:79] op_sel_hi:[1,0]\n"
"v_pk_mul_f32 v[118:119], v[118:119], s[78:79] op_sel_hi:[1,0]\n"
"v_pk_mul_f32 v[120:121], v[120:121], s[78:79] op_sel_hi:[1,0]\n"
"v_pk_mul_f32 v[122:123], v[122:123], s[78:79] op_sel_hi:[1,0]\n"
"v_pk_mul_f32 v[124:125], v[124:125], s[78:79] op_sel_hi:[1,0]\n"
"v_pk_mul_f32 v[126:127], v[126:127], s[78:79] op_sel_hi:[1,0]\n"
"v_pk_mul_f32 v[128:129], v[128:129], s[78:79] op_sel_hi:[1,0]\n"
"v_pk_mul_f32 v[130:131], v[130:131], s[78:79] op_sel_hi:[1,0]\n"
"v_pk_mul_f32 v[132:133], v[132:133], s[78:79] op_sel_hi:[1,0]\n"
"v_pk_mul_f32 v[134:135], v[134:135], s[78:79] op_sel_hi:[1,0]\n"
"v_pk_mul_f32 v[136:137], v[136:137], s[78:79] op_sel_hi:[1,0]\n"
"v_pk_mul_f32 v[138:139], v[138:139], s[78:79] op_sel_hi:[1,0]\n"
"v_pk_mul_f32 v[140:141], v[140:141], s[78:79] op_sel_hi:[1,0]\n"
"v_pk_mul_f32 v[142:143], v[142:143], s[78:79] op_sel_hi:[1,0]\n"
"v_pk_mul_f32 v[144:145], v[144:145], s[78:79] op_sel_hi:[1,0]\n"
"v_pk_mul_f32 v[146:147], v[146:147], s[78:79] op_sel_hi:[1,0]\n"
"v_pk_mul_f32 v[148:149], v[148:149], s[78:79] op_sel_hi:[1,0]\n"
"v_pk_mul_f32 v[150:151], v[150:151], s[78:79] op_sel_hi:[1,0]\n"
"v_pk_mul_f32 v[152:153], v[152:153], s[78:79] op_sel_hi:[1,0]\n"
"v_pk_mul_f32 v[154:155], v[154:155], s[78:79] op_sel_hi:[1,0]\n"
"v_pk_mul_f32 v[156:157], v[156:157], s[78:79] op_sel_hi:[1,0]\n"
"v_pk_mul_f32 v[158:159], v[158:159], s[78:79] op_sel_hi:[1,0]\n"
"v_cvt_pk_bf16_f32 v16, v16, v17\n"
"v_cvt_pk_bf16_f32 v17, v18, v19\n"
"v_cvt_pk_bf16_f32 v20, v20, v21\n"
"v_cvt_pk_bf16_f32 v21, v22, v23\n"
"v_cvt_pk_bf16_f32 v24, v24, v25\n"
"v_cvt_pk_bf16_f32 v25, v26, v27\n"
"v_cvt_pk_bf16_f32 v28, v28, v29\n"
"v_cvt_pk_bf16_f32 v29, v30, v31\n"
"v_cvt_pk_bf16_f32 v32, v32, v33\n"
"v_cvt_pk_bf16_f32 v33, v34, v35\n"
"v_cvt_pk_bf16_f32 v36, v36, v37\n"
"v_cvt_pk_bf16_f32 v37, v38, v39\n"
"v_cvt_pk_bf16_f32 v40, v40, v41\n"
"v_cvt_pk_bf16_f32 v41, v42, v43\n"
"v_cvt_pk_bf16_f32 v44, v44, v45\n"
"v_cvt_pk_bf16_f32 v45, v46, v47\n"
"v_cvt_pk_bf16_f32 v48, v48, v49\n"
"v_cvt_pk_bf16_f32 v49, v50, v51\n"
"v_cvt_pk_bf16_f32 v52, v52, v53\n"
"v_cvt_pk_bf16_f32 v53, v54, v55\n"
"v_cvt_pk_bf16_f32 v56, v56, v57\n"
"v_cvt_pk_bf16_f32 v57, v58, v59\n"
"v_cvt_pk_bf16_f32 v60, v60, v61\n"
"v_cvt_pk_bf16_f32 v61, v62, v63\n"
"v_cvt_pk_bf16_f32 v64, v64, v65\n"
"v_cvt_pk_bf16_f32 v65, v66, v67\n"
"v_cvt_pk_bf16_f32 v68, v68, v69\n"
"v_cvt_pk_bf16_f32 v69, v70, v71\n"
"v_cvt_pk_bf16_f32 v72, v72, v73\n"
"v_cvt_pk_bf16_f32 v73, v74, v75\n"
"v_cvt_pk_bf16_f32 v76, v76, v77\n"
"v_cvt_pk_bf16_f32 v77, v78, v79\n"
"v_cvt_pk_bf16_f32 v80, v80, v81\n"
"v_cvt_pk_bf16_f32 v81, v82, v83\n"
"v_cvt_pk_bf16_f32 v84, v84, v85\n"
"v_cvt_pk_bf16_f32 v85, v86, v87\n"
"v_cvt_pk_bf16_f32 v88, v88, v89\n"
"v_cvt_pk_bf16_f32 v89, v90, v91\n"
"v_cvt_pk_bf16_f32 v92, v92, v93\n"
"v_cvt_pk_bf16_f32 v93, v94, v95\n"
"v_cvt_pk_bf16_f32 v96, v96, v97\n"
"v_cvt_pk_bf16_f32 v97, v98, v99\n"
"v_cvt_pk_bf16_f32 v100, v100, v101\n"
"v_cvt_pk_bf16_f32 v101, v102, v103\n"
"v_cvt_pk_bf16_f32 v104, v104, v105\n"
"v_cvt_pk_bf16_f32 v105, v106, v107\n"
"v_cvt_pk_bf16_f32 v108, v108, v109\n"
"v_cvt_pk_bf16_f32 v109, v110, v111\n"
"v_cvt_pk_bf16_f32 v112, v112, v113\n"
"v_cvt_pk_bf16_f32 v113, v114, v115\n"
"v_cvt_pk_bf16_f32 v116, v116, v117\n"
"v_cvt_pk_bf16_f32 v117, v118, v119\n"
"v_cvt_pk_bf16_f32 v120, v120, v121\n"
"v_cvt_pk_bf16_f32 v121, v122, v123\n"
"v_cvt_pk_bf16_f32 v124, v124, v125\n"
"v_cvt_pk_bf16_f32 v125, v126, v127\n"
"v_cvt_pk_bf16_f32 v128, v128, v129\n"
"v_cvt_pk_bf16_f32 v129, v130, v131\n"
"v_cvt_pk_bf16_f32 v132, v132, v133\n"
"v_cvt_pk_bf16_f32 v133, v134, v135\n"
"v_cvt_pk_bf16_f32 v136, v136, v137\n"
"v_cvt_pk_bf16_f32 v137, v138, v139\n"
"v_cvt_pk_bf16_f32 v140, v140, v141\n"
"v_cvt_pk_bf16_f32 v141, v142, v143\n"
"v_cvt_pk_bf16_f32 v144, v144, v145\n"
"v_cvt_pk_bf16_f32 v145, v146, v147\n"
"v_cvt_pk_bf16_f32 v148, v148, v149\n"
"v_cvt_pk_bf16_f32 v149, v150, v151\n"
"v_cvt_pk_bf16_f32 v152, v152, v153\n"
"v_cvt_pk_bf16_f32 v153, v154, v155\n"
"v_cvt_pk_bf16_f32 v156, v156, v157\n"
"v_cvt_pk_bf16_f32 v157, v158, v159\n"
"s_mov_b64 exec, s[80:81]\n"
"s_nop 0\n"
"v_swap_b32 v16, v28\n"
"v_swap_b32 v17, v29\n"
"v_swap_b32 v20, v32\n"
"v_swap_b32 v21, v33\n"
"v_swap_b32 v24, v36\n"
"v_swap_b32 v25, v37\n"
"v_swap_b32 v40, v52\n"
"v_swap_b32 v41, v53\n"
"v_swap_b32 v44, v56\n"
"v_swap_b32 v45, v57\n"
"v_swap_b32 v48, v60\n"
"v_swap_b32 v49, v61\n"
"v_swap_b32 v64, v76\n"
"v_swap_b32 v65, v77\n"
"v_swap_b32 v68, v80\n"
"v_swap_b32 v69, v81\n"
"v_swap_b32 v72, v84\n"
"v_swap_b32 v73, v85\n"
"v_swap_b32 v88, v100\n"
"v_swap_b32 v89, v101\n"
"v_swap_b32 v92, v104\n"
"v_swap_b32 v93, v105\n"
"v_swap_b32 v96, v108\n"
"v_swap_b32 v97, v109\n"
"v_swap_b32 v112, v124\n"
"v_swap_b32 v113, v125\n"
"v_swap_b32 v116, v128\n"
"v_swap_b32 v117, v129\n"
"v_swap_b32 v120, v132\n"
"v_swap_b32 v121, v133\n"
"v_swap_b32 v136, v148\n"
"v_swap_b32 v137, v149\n"
"v_swap_b32 v140, v152\n"
"v_swap_b32 v141, v153\n"
"v_swap_b32 v144, v156\n"
"v_swap_b32 v145, v157\n"
"s_mov_b64 exec, -1\n"
"s_nop 0\n"
"v_accvgpr_write_b32 a144, v16\n"
"v_accvgpr_write_b32 a145, v17\n"
"v_accvgpr_write_b32 a148, v20\n"
"v_accvgpr_write_b32 a149, v21\n"
"v_accvgpr_write_b32 a152, v24\n"
"v_accvgpr_write_b32 a153, v25\n"
"v_accvgpr_write_b32 a146, v28\n"
"v_accvgpr_write_b32 a147, v29\n"
"v_accvgpr_write_b32 a150, v32\n"
"v_accvgpr_write_b32 a151, v33\n"
"v_accvgpr_write_b32 a154, v36\n"
"v_accvgpr_write_b32 a155, v37\n"
"v_accvgpr_write_b32 a156, v40\n"
"v_accvgpr_write_b32 a157, v41\n"
"v_accvgpr_write_b32 a160, v44\n"
"v_accvgpr_write_b32 a161, v45\n"
"v_accvgpr_write_b32 a164, v48\n"
"v_accvgpr_write_b32 a165, v49\n"
"v_accvgpr_write_b32 a158, v52\n"
"v_accvgpr_write_b32 a159, v53\n"
"v_accvgpr_write_b32 a162, v56\n"
"v_accvgpr_write_b32 a163, v57\n"
"v_accvgpr_write_b32 a166, v60\n"
"v_accvgpr_write_b32 a167, v61\n"
"v_accvgpr_write_b32 a168, v64\n"
"v_accvgpr_write_b32 a169, v65\n"
"v_accvgpr_write_b32 a172, v68\n"
"v_accvgpr_write_b32 a173, v69\n"
"v_accvgpr_write_b32 a176, v72\n"
"v_accvgpr_write_b32 a177, v73\n"
"v_accvgpr_write_b32 a170, v76\n"
"v_accvgpr_write_b32 a171, v77\n"
"v_accvgpr_write_b32 a174, v80\n"
"v_accvgpr_write_b32 a175, v81\n"
"v_accvgpr_write_b32 a178, v84\n"
"v_accvgpr_write_b32 a179, v85\n"
"v_accvgpr_write_b32 a180, v88\n"
"v_accvgpr_write_b32 a181, v89\n"
"v_accvgpr_write_b32 a184, v92\n"
"v_accvgpr_write_b32 a185, v93\n"
"v_accvgpr_write_b32 a188, v96\n"
"v_accvgpr_write_b32 a189, v97\n"
"v_accvgpr_write_b32 a182, v100\n"
"v_accvgpr_write_b32 a183, v101\n"
"v_accvgpr_write_b32 a186, v104\n"
"v_accvgpr_write_b32 a187, v105\n"
"v_accvgpr_write_b32 a190, v108\n"
"v_accvgpr_write_b32 a191, v109\n"
"v_accvgpr_write_b32 a192, v112\n"
"v_accvgpr_write_b32 a193, v113\n"
"v_accvgpr_write_b32 a196, v116\n"
"v_accvgpr_write_b32 a197, v117\n"
"v_accvgpr_write_b32 a200, v120\n"
"v_accvgpr_write_b32 a201, v121\n"
"v_accvgpr_write_b32 a194, v124\n"
"v_accvgpr_write_b32 a195, v125\n"
"v_accvgpr_write_b32 a198, v128\n"
"v_accvgpr_write_b32 a199, v129\n"
"v_accvgpr_write_b32 a202, v132\n"
"v_accvgpr_write_b32 a203, v133\n"
"v_accvgpr_write_b32 a204, v136\n"
"v_accvgpr_write_b32 a205, v137\n"
"v_accvgpr_write_b32 a208, v140\n"
"v_accvgpr_write_b32 a209, v141\n"
"v_accvgpr_write_b32 a212, v144\n"
"v_accvgpr_write_b32 a213, v145\n"
"v_accvgpr_write_b32 a206, v148\n"
"v_accvgpr_write_b32 a207, v149\n"
"v_accvgpr_write_b32 a210, v152\n"
"v_accvgpr_write_b32 a211, v153\n"
"v_accvgpr_write_b32 a214, v156\n"
"v_accvgpr_write_b32 a215, v157\n"
"s_waitcnt lgkmcnt(3)\n"
"v_mfma_f32_16x16x32_bf16 v[160:163], a[216:219], a[144:147], v[196:199]\n"
"ds_read_b128 a[232:235], v236 offset:256\n"
"v_mfma_f32_16x16x32_bf16 v[164:167], a[216:219], a[148:151], v[200:203]\n"
"v_mfma_f32_16x16x32_bf16 v[168:171], a[216:219], a[152:155], v[204:207]\n"
"s_mov_b32 m0, s61\n"
"s_add_u32 s68, s66, 196608\n"
"buffer_load_dwordx4 %[dW0], %[rWq], s66 offen offset:0 lds\n"
"s_waitcnt lgkmcnt(3)\n"
"v_mfma_f32_16x16x32_bf16 v[160:163], a[220:223], a[156:159], v[160:163]\n"
"ds_read_b128 a[236:239], v237 offset:256\n"
"v_mfma_f32_16x16x32_bf16 v[164:167], a[220:223], a[160:163], v[164:167]\n"
"v_mfma_f32_16x16x32_bf16 v[168:171], a[220:223], a[164:167], v[168:171]\n"
"s_waitcnt lgkmcnt(3)\n"
"v_mfma_f32_16x16x32_bf16 v[160:163], a[224:227], a[168:171], v[160:163]\n"
"ds_read_b128 a[216:219], v236 offset:6144\n"
"v_mfma_f32_16x16x32_bf16 v[164:167], a[224:227], a[172:175], v[164:167]\n"
"v_mfma_f32_16x16x32_bf16 v[168:171], a[224:227], a[176:179], v[168:171]\n"
"buffer_load_dwordx4 %[dW1], %[rWq], s66 offen offset:1024 lds\n"
"s_waitcnt lgkmcnt(3)\n"
"v_mfma_f32_16x16x32_bf16 v[160:163], a[228:231], a[180:183], v[160:163]\n"
"ds_read_b128 a[220:223], v237 offset:6144\n"
"v_mfma_f32_16x16x32_bf16 v[164:167], a[228:231], a[184:187], v[164:167]\n"
"v_mfma_f32_16x16x32_bf16 v[168:171], a[228:231], a[188:191], v[168:171]\n"
"s_waitcnt lgkmcnt(3)\n"
"v_mfma_f32_16x16x32_bf16 v[160:163], a[232:235], a[192:195], v[160:163]\n"
"ds_read_b128 a[224:227], v236 offset:6272\n"
"v_mfma_f32_16x16x32_bf16 v[164:167], a[232:235], a[196:199], v[164:167]\n"
"v_mfma_f32_16x16x32_bf16 v[168:171], a[232:235], a[200:203], v[168:171]\n"
"buffer_load_dwordx4 %[dW2], %[rWq], s66 offen offset:2048 lds\n"
"s_add_u32 m0, s61, 3072\n"
"s_waitcnt lgkmcnt(3)\n"
"v_mfma_f32_16x16x32_bf16 v[160:163], a[236:239], a[204:207], v[160:163]\n"
"ds_read_b128 a[228:231], v237 offset:6272\n"
"v_mfma_f32_16x16x32_bf16 v[164:167], a[236:239], a[208:211], v[164:167]\n"
"v_mfma_f32_16x16x32_bf16 v[168:171], a[236:239], a[212:215], v[168:171]\n"
"s_waitcnt lgkmcnt(3)\n"
"v_mfma_f32_16x16x32_bf16 v[172:175], a[216:219], a[144:147], v[196:199]\n"
"ds_read_b128 a[232:235], v236 offset:6400\n"
"v_mfma_f32_16x16x32_bf16 v[176:179], a[216:219], a[148:151], v[200:203]\n"
"v_mfma_f32_16x16x32_bf16 v[180:183], a[216:219], a[152:155], v[204:207]\n"
"s_add_u32 s66, s66, 393216\n"
"buffer_load_dwordx4 %[dW0], %[rWq], s68 offen offset:0 lds\n"
"s_waitcnt lgkmcnt(3)\n"
"v_mfma_f32_16x16x32_bf16 v[172:175], a[220:223], a[156:159], v[172:175]\n"
"ds_read_b128 a[236:239], v237 offset:6400\n"
"v_mfma_f32_16x16x32_bf16 v[176:179], a[220:223], a[160:163], v[176:179]\n"
"v_mfma_f32_16x16x32_bf16 v[180:183], a[220:223], a[164:167], v[180:183]\n"
"buffer_load_dwordx4 %[dW1], %[rWq], s68 offen offset:1024 lds\n"
"s_waitcnt lgkmcnt(3)\n"
"v_mfma_f32_16x16x32_bf16 v[172:175], a[224:227], a[168:171], v[172:175]\n"
"s_add_u32 s61, %[m0b], s60\n"
"s_add_u32 s60, s60, 12288\n"
"s_cmp_eq_u32 s60, 61440\n"
"s_cselect_b32 s60, 0, s60\n"
"v_add_u32 v236, s60, %[rdW]\n"
"v_mfma_f32_16x16x32_bf16 v[176:179], a[224:227], a[172:175], v[176:179]\n"
"v_xor_b32 v237, 8, v236\n"
"v_xor_b32 v238, 64, v236\n"
"v_xor_b32 v239, 72, v236\n"
"v_add_u32 v240, s62, %[rdX]\n"
"s_add_u32 s62, s62, 12288\n"
"s_cmp_eq_u32 s62, 36864\n"
"s_cselect_b32 s62, 0, s62\n"
"v_mfma_f32_16x16x32_bf16 v[180:183], a[224:227], a[176:179], v[180:183]\n"
"s_waitcnt vmcnt(17) lgkmcnt(0)\n"
"s_barrier\n"
"ds_read_b64_tr_b16 a[216:217], v236 offset:0\n"
"ds_read_b64_tr_b16 a[218:219], v236 offset:6144\n"
"v_mfma_f32_16x16x32_bf16 v[172:175], a[228:231], a[180:183], v[172:175]\n"
"ds_read_b128 a[240:243], v240 offset:0\n"
"ds_read_b64_tr_b16 a[220:221], v237 offset:0\n"
"v_mfma_f32_16x16x32_bf16 v[176:179], a[228:231], a[184:187], v[176:179]\n"
"ds_read_b64_tr_b16 a[222:223], v237 offset:6144\n"
"ds_read_b128 a[244:247], v240 offset:2048\n"
"v_mfma_f32_16x16x32_bf16 v[180:183], a[228:231], a[188:191], v[180:183]\n"
"ds_read_b128 a[248:251], v240 offset:4096\n"
"v_mfma_f32_16x16x32_bf16 v[172:175], a[232:235], a[192:195], v[172:175]\n"
"ds_read_b64_tr_b16 a[224:225], v238 offset:0\n"
"v_mfma_f32_16x16x32_bf16 v[176:179], a[232:235], a[196:199], v[176:179]\n"
"ds_read_b64_tr_b16 a[226:227], v238 offset:6144\n"
"v_mfma_f32_16x16x32_bf16 v[180:183], a[232:235], a[200:203], v[180:183]\n"
"buffer_load_dwordx4 %[dW2], %[rWq], s68 offen offset:2048 lds\n"
"v_mfma_f32_16x16x32_bf16 v[172:175], a[236:239], a[204:207], v[172:175]\n"
"ds_read_b64_tr_b16 a[228:229], v239 offset:0\n"
"v_mfma_f32_16x16x32_bf16 v[176:179], a[236:239], a[208:211], v[176:179]\n"
"ds_read_b64_tr_b16 a[230:231], v239 offset:6144\n"
"v_mfma_f32_16x16x32_bf16 v[180:183], a[236:239], a[212:215], v[180:183]\n"
"s_waitcnt lgkmcnt(8)\n"
"v_mfma_f32_16x16x32_bf16 v[16:19], a[216:219], a[240:243], 0\n"
"ds_read_b64_tr_b16 a[232:233], v236 offset:128\n"
"s_waitcnt lgkmcnt(6)\n"
"v_mfma_f32_16x16x32_bf16 v[20:23], a[216:219], a[244:247], 0\n"
"ds_read_b64_tr_b16 a[234:235], v236 offset:6272\n"
"s_waitcnt lgkmcnt(6)\n"
"v_mfma_f32_16x16x32_bf16 v[24:27], a[216:219], a[248:251], 0\n"
"s_mov_b32 m0, s61\n"
"s_mul_i32 s66, s56, 6144\n"
"s_add_u32 s66, s66, s57\n"
"s_add_u32 s68, s66, 196608\n"
"buffer_load_dwordx4 %[dW0], %[rWq], s66 offen offset:0 lds\n"
"v_mfma_f32_16x16x32_bf16 v[28:31], a[220:223], a[240:243], 0\n"
"ds_read_b64_tr_b16 a[236:237], v237 offset:128\n"
"v_mfma_f32_16x16x32_bf16 v[32:35], a[220:223], a[244:247], 0\n"
"ds_read_b64_tr_b16 a[238:239], v237 offset:6272\n"
"v_max3_f32 v242, v160, v161, v162\n"
"v_max3_f32 v242, v242, v163, v164\n"
"v_max3_f32 v242, v242, v165, v166\n"
"v_mfma_f32_16x16x32_bf16 v[36:39], a[220:223], a[248:251], 0\n"
"buffer_load_dwordx4 %[dW1], %[rWq], s66 offen offset:1024 lds\n"
"v_max3_f32 v242, v242, v167, v168\n"
"v_max3_f32 v242, v242, v169, v170\n"
"s_waitcnt lgkmcnt(6)\n"
"v_mfma_f32_16x16x32_bf16 v[40:43], a[224:227], a[240:243], 0\n"
"ds_read_b64_tr_b16 a[216:217], v238 offset:128\n"
"v_max_f32 v242, v242, v171\n"
"v_max3_f32 v243, v172, v173, v174\n"
"v_mfma_f32_16x16x32_bf16 v[44:47], a[224:227], a[244:247], 0\n"
"ds_read_b64_tr_b16 a[218:219], v238 offset:6272\n"
"v_max3_f32 v243, v243, v175, v176\n"
"v_max3_f32 v243, v243, v177, v178\n"
"v_max3_f32 v243, v243, v179, v180\n"
"v_mfma_f32_16x16x32_bf16 v[48:51], a[224:227], a[248:251], 0\n"
"v_max3_f32 v243, v243, v181, v182\n"
"v_max3_f32 v243, v243, v183, v242\n"
"v_cmp_lt_f32 vcc, s70, v243\n"
"s_waitcnt lgkmcnt(6)\n"
"v_mfma_f32_16x16x32_bf16 v[52:55], a[228:231], a[240:243], 0\n"
"ds_read_b64_tr_b16 a[220:221], v239 offset:128\n"
"s_nop 1\n"
"s_cbranch_vccz 1\n"
"s_call_b64 s[76:77], Lslow_%=\n"
"v_mfma_f32_16x16x32_bf16 v[56:59], a[228:231], a[244:247], 0\n"
"ds_read_b64_tr_b16 a[222:223], v239 offset:6272\n"
"v_exp_f32 v160, v160\n"
"v_exp_f32 v161, v161\n"
"v_mfma_f32_16x16x32_bf16 v[60:63], a[228:231], a[248:251], 0\n"
"buffer_load_dwordx4 %[dW2], %[rWq], s66 offen offset:2048 lds\n"
"s_add_u32 m0, s61, 3072\n"
"s_waitcnt lgkmcnt(6)\n"
"v_mfma_f32_16x16x32_bf16 v[64:67], a[232:235], a[240:243], 0\n"
"ds_read_b64_tr_b16 a[224:225], v236 offset:256\n"
"v_exp_f32 v162, v162\n"
"v_exp_f32 v163, v163\n"
"v_mfma_f32_16x16x32_bf16 v[68:71], a[232:235], a[244:247], 0\n"
"ds_read_b64_tr_b16 a[226:227], v236 offset:6400\n"
"v_exp_f32 v172, v172\n"
"v_mfma_f32_16x16x32_bf16 v[72:75], a[232:235], a[248:251], 0\n"
"s_add_u32 s66, s66, 393216\n"
"buffer_load_dwordx4 %[dW0], %[rWq], s68 offen offset:0 lds\n"
"v_exp_f32 v173, v173\n"
"s_waitcnt lgkmcnt(6)\n"
"v_mfma_f32_16x16x32_bf16 v[76:79], a[236:239], a[240:243], 0\n"
"ds_read_b64_tr_b16 a[228:229], v237 offset:256\n"
"v_exp_f32 v174, v174\n"
"v_mfma_f32_16x16x32_bf16 v[80:83], a[236:239], a[244:247], 0\n"
"ds_read_b64_tr_b16 a[230:231], v237 offset:6400\n"
"v_exp_f32 v175, v175\n"
"v_cvt_pk_bf16_f32 v184, v160, v161\n"
"v_mfma_f32_16x16x32_bf16 v[84:87], a[236:239], a[248:251], 0\n"
"v_cvt_pk_bf16_f32 v185, v162, v163\n"
"v_exp_f32 v164, v164\n"
"v_exp_f32 v165, v165\n"
"s_waitcnt lgkmcnt(6)\n"
"v_mfma_f32_16x16x32_bf16 v[88:91], a[216:219], a[240:243], 0\n"
"ds_read_b64_tr_b16 a[232:233], v238 offset:256\n"
"v_exp_f32 v166, v166\n"
"v_mfma_f32_16x16x32_bf16 v[92:95], a[216:219], a[244:247], 0\n"
"ds_read_b64_tr_b16 a[234:235], v238 offset:6400\n"
"v_exp_f32 v167, v167\n"
"v_cvt_pk_bf16_f32 v186, v172, v173\n"
"v_mfma_f32_16x16x32_bf16 v[96:99], a[216:219], a[248:251], 0\n"
"buffer_load_dwordx4 %[dW1], %[rWq], s68 offen offset:1024 lds\n"
"v_cvt_pk_bf16_f32 v187, v174, v175\n"
"v_exp_f32 v176, v176\n"
"s_waitcnt lgkmcnt(6)\n"
"v_mfma_f32_16x16x32_bf16 v[100:103], a[220:223], a[240:243], 0\n"
"ds_read_b64_tr_b16 a[236:237], v239 offset:256\n"
"v_exp_f32 v177, v177\n"
"v_mfma_f32_16x16x32_bf16 v[104:107], a[220:223], a[244:247], 0\n"
"ds_read_b64_tr_b16 a[238:239], v239 offset:6400\n"
"v_exp_f32 v178, v178\n"
"v_exp_f32 v179, v179\n"
"v_mfma_f32_16x16x32_bf16 v[108:111], a[220:223], a[248:251], 0\n"
"buffer_load_dwordx4 %[dW2], %[rWq], s68 offen offset:2048 lds\n"
"v_cvt_pk_bf16_f32 v188, v164, v165\n"
"s_waitcnt lgkmcnt(6)\n"
"v_mfma_f32_16x16x32_bf16 v[112:115], a[224:227], a[240:243], 0\n"
"s_add_u32 s61, %[m0b], s60\n"
"s_add_u32 s60, s60, 12288\n"
"s_cmp_eq_u32 s60, 61440\n"
"s_cselect_b32 s60, 0, s60\n"
"v_add_u32 v236, s60, %[rdV]\n"
"v_mfma_f32_16x16x32_bf16 v[116:119], a[224:227], a[244:247], 0\n"
"v_xor_b32 v237, 32, v236\n"
"v_xor_b32 v238, 64, v236\n"
"v_xor_b32 v239, 96, v236\n"
"v_mfma_f32_16x16x32_bf16 v[120:123], a[224:227], a[248:251], 0\n"
"v_cvt_pk_bf16_f32 v189, v166, v167\n"
"s_waitcnt vmcnt(18) lgkmcnt(0)\n"
"s_barrier\n"
"ds_read_b64_tr_b16 a[216:217], v236 offset:0\n"
"ds_read_b64_tr_b16 a[218:219], v236 offset:6144\n"
"v_mfma_f32_16x16x32_bf16 v[124:127], a[228:231], a[240:243], 0\n"
"ds_read_b64_tr_b16 a[220:221], v237 offset:0\n"
"v_exp_f32 v168, v168\n"
"v_exp_f32 v169, v169\n"
"v_mfma_f32_16x16x32_bf16 v[128:131], a[228:231], a[244:247], 0\n"
"ds_read_b64_tr_b16 a[222:223], v237 offset:6144\n"
"v_exp_f32 v170, v170\n"
"v_mfma_f32_16x16x32_bf16 v[132:135], a[228:231], a[248:251], 0\n"
"v_exp_f32 v171, v171\n"
"v_cvt_pk_bf16_f32 v190, v176, v177\n"
"v_cvt_pk_bf16_f32 v191, v178, v179\n"
"v_mfma_f32_16x16x32_bf16 v[136:139], a[232:235], a[240:243], 0\n"
"ds_read_b64_tr_b16 a[224:225], v238 offset:0\n"
"v_exp_f32 v180, v180\n"
"v_exp_f32 v181, v181\n"
"v_mfma_f32_16x16x32_bf16 v[140:143], a[232:235], a[244:247], 0\n"
"ds_read_b64_tr_b16 a[226:227], v238 offset:6144\n"
"v_exp_f32 v182, v182\n"
"v_mfma_f32_16x16x32_bf16 v[144:147], a[232:235], a[248:251], 0\n"
"s_add_u32 m0, %[m0x], s63\n"
"s_add_u32 s67, %[x0], s56\n"
"s_add_u32 s75, s67, 9216\n"
"buffer_load_dwordx4 %[dX], %[rX], s67 offen offset:0 lds\n"
"v_mfma_f32_16x16x32_bf16 v[148:151], a[236:239], a[240:243], 0\n"
"ds_read_b64_tr_b16 a[228:229], v239 offset:0\n"
"v_exp_f32 v183, v183\n"
"v_mfma_f32_16x16x32_bf16 v[152:155], a[236:239], a[244:247], 0\n"
"ds_read_b64_tr_b16 a[230:231], v239 offset:6144\n"
"v_cvt_pk_bf16_f32 v192, v168, v169\n"
"v_cvt_pk_bf16_f32 v193, v170, v171\n"
"v_cvt_pk_bf16_f32 v194, v180, v181\n"
"v_mfma_f32_16x16x32_bf16 v[156:159], a[236:239], a[248:251], 0\n"
"buffer_load_dwordx4 %[dX], %[rX], s75 offen offset:1024 lds\n"
"s_add_u32 s75, s67, 18432\n"
"v_cvt_pk_bf16_f32 v195, v182, v183\n"
"s_waitcnt lgkmcnt(6)\n"
"v_mfma_f32_16x16x32_bf16 a[0:3], a[216:219], v[184:187], 0\n"
"ds_read_b64_tr_b16 a[232:233], v236 offset:128\n"
"v_mfma_f32_16x16x32_bf16 a[4:7], a[216:219], v[188:191], 0\n"
"ds_read_b64_tr_b16 a[234:235], v236 offset:6272\n"
"v_mfma_f32_16x16x32_bf16 a[8:11], a[216:219], v[192:195], 0\n"
"buffer_load_dwordx4 %[dX], %[rX], s75 offen offset:2048 lds\n"
"s_add_u32 s56, s56, 128\n"
"s_cmp_eq_u32 s56, 1280\n"
"s_cselect_b32 s56, 0, s56\n"
"s_add_u32 s63, s63, 12288\n"
"s_cmp_eq_u32 s63, 36864\n"
"s_cselect_b32 s63, 0, s63\n"
"s_waitcnt lgkmcnt(6)\n"
"v_mfma_f32_16x16x32_bf16 a[12:15], a[220:223], v[184:187], 0\n"
"ds_read_b64_tr_b16 a[236:237], v237 offset:128\n"
"v_mfma_f32_16x16x32_bf16 a[16:19], a[220:223], v[188:191], 0\n"
"ds_read_b64_tr_b16 a[238:239], v237 offset:6272\n"
"v_mfma_f32_16x16x32_bf16 a[20:23], a[220:223], v[192:195], 0\n"
"s_mov_b32 m0, s61\n"
"s_add_u32 s68, s65, 3072\n"
"buffer_load_dwordx4 %[dKV0], %[rV], s65 offen offset:0 nt lds\n"
"s_waitcnt lgkmcnt(6)\n"
"v_mfma_f32_16x16x32_bf16 a[24:27], a[224:227], v[184:187], 0\n"
"ds_read_b64_tr_b16 a[216:217], v238 offset:128\n"
"v_mfma_f32_16x16x32_bf16 a[28:31], a[224:227], v[188:191], 0\n"
"ds_read_b64_tr_b16 a[218:219], v238 offset:6272\n"
"v_mfma_f32_16x16x32_bf16 a[32:35], a[224:227], v[192:195], 0\n"
"s_waitcnt lgkmcnt(6)\n"
"v_mfma_f32_16x16x32_bf16 a[36:39], a[228:231], v[184:187], 0\n"
"ds_read_b64_tr_b16 a[220:221], v239 offset:128\n"
"v_mfma_f32_16x16x32_bf16 a[40:43], a[228:231], v[188:191], 0\n"
"ds_read_b64_tr_b16 a[222:223], v239 offset:6272\n"
"v_mfma_f32_16x16x32_bf16 a[44:47], a[228:231], v[192:195], 0\n"
"buffer_load_dwordx4 %[dKV1], %[rV], s65 offen offset:1024 nt lds\n"
"s_waitcnt lgkmcnt(6)\n"
"v_mfma_f32_16x16x32_bf16 a[48:51], a[232:235], v[184:187], 0\n"
"ds_read_b64_tr_b16 a[224:225], v236 offset:256\n"
"v_mfma_f32_16x16x32_bf16 a[52:55], a[232:235], v[188:191], 0\n"
"ds_read_b64_tr_b16 a[226:227], v236 offset:6400\n"
"v_mfma_f32_16x16x32_bf16 a[56:59], a[232:235], v[192:195], 0\n"
"s_waitcnt lgkmcnt(6)\n"
"v_mfma_f32_16x16x32_bf16 a[60:63], a[236:239], v[184:187], 0\n"
"ds_read_b64_tr_b16 a[228:229], v237 offset:256\n"
"v_mfma_f32_16x16x32_bf16 a[64:67], a[236:239], v[188:191], 0\n"
"ds_read_b64_tr_b16 a[230:231], v237 offset:6400\n"
"v_mfma_f32_16x16x32_bf16 a[68:71], a[236:239], v[192:195], 0\n"
"buffer_load_dwordx4 %[dKV2], %[rV], s65 offen offset:2048 nt lds\n"
"s_add_u32 m0, s61, 3072\n"
"s_waitcnt lgkmcnt(6)\n"
"v_mfma_f32_16x16x32_bf16 a[72:75], a[216:219], v[184:187], 0\n"
"ds_read_b64_tr_b16 a[232:233], v238 offset:256\n"
"v_mfma_f32_16x16x32_bf16 a[76:79], a[216:219], v[188:191], 0\n"
"ds_read_b64_tr_b16 a[234:235], v238 offset:6400\n"
"v_mfma_f32_16x16x32_bf16 a[80:83], a[216:219], v[192:195], 0\n"
"s_add_u32 s65, s65, 12288\n"
"buffer_load_dwordx4 %[dKV0], %[rV], s68 offen offset:0 nt lds\n"
"s_waitcnt lgkmcnt(6)\n"
"v_mfma_f32_16x16x32_bf16 a[84:87], a[220:223], v[184:187], 0\n"
"ds_read_b64_tr_b16 a[236:237], v239 offset:256\n"
"v_mfma_f32_16x16x32_bf16 a[88:91], a[220:223], v[188:191], 0\n"
"ds_read_b64_tr_b16 a[238:239], v239 offset:6400\n"
"v_mfma_f32_16x16x32_bf16 a[92:95], a[220:223], v[192:195], 0\n"
"buffer_load_dwordx4 %[dKV1], %[rV], s68 offen offset:1024 nt lds\n"
"s_waitcnt lgkmcnt(6)\n"
"v_mfma_f32_16x16x32_bf16 a[96:99], a[224:227], v[184:187], 0\n"
"s_add_u32 s61, %[m0b], s60\n"
"s_add_u32 s60, s60, 12288\n"
"s_cmp_eq_u32 s60, 61440\n"
"s_cselect_b32 s60, 0, s60\n"
"v_add_u32 v236, s60, %[rdK]\n"
"v_mfma_f32_16x16x32_bf16 a[100:103], a[224:227], v[188:191], 0\n"
"v_xor_b32 v237, 64, v236\n"
"v_mfma_f32_16x16x32_bf16 a[104:107], a[224:227], v[192:195], 0\n"
"s_waitcnt vmcnt(20) lgkmcnt(0)\n"
"s_barrier\n"
"ds_read_b128 a[216:219], v236 offset:0\n"
"v_mfma_f32_16x16x32_bf16 a[108:111], a[228:231], v[184:187], 0\n"
"ds_read_b128 a[220:223], v237 offset:0\n"
"v_mfma_f32_16x16x32_bf16 a[112:115], a[228:231], v[188:191], 0\n"
"v_mfma_f32_16x16x32_bf16 a[116:119], a[228:231], v[192:195], 0\n"
"v_mfma_f32_16x16x32_bf16 a[120:123], a[232:235], v[184:187], 0\n"
"ds_read_b128 a[224:227], v236 offset:128\n"
"v_mfma_f32_16x16x32_bf16 a[124:127], a[232:235], v[188:191], 0\n"
"v_mfma_f32_16x16x32_bf16 a[128:131], a[232:235], v[192:195], 0\n"
"buffer_load_dwordx4 %[dKV2], %[rV], s68 offen offset:2048 nt lds\n"
"v_mfma_f32_16x16x32_bf16 a[132:135], a[236:239], v[184:187], 0\n"
"ds_read_b128 a[228:231], v237 offset:128\n"
"v_mfma_f32_16x16x32_bf16 a[136:139], a[236:239], v[188:191], 0\n"
"v_mfma_f32_16x16x32_bf16 a[140:143], a[236:239], v[192:195], 0\n"
"v_mfma_f32_16x16x32_bf16 v[208:211], v[220:223], v[184:187], 0\n"
"v_mfma_f32_16x16x32_bf16 v[212:215], v[220:223], v[188:191], 0\n"
"v_mfma_f32_16x16x32_bf16 v[216:219], v[220:223], v[192:195], 0\n"
"s_waitcnt lgkmcnt(3)\n"
"v_mfma_f32_16x16x32_bf16 v[160:163], a[216:219], a[144:147], v[196:199]\n"
"ds_read_b128 a[232:235], v236 offset:256\n"
"v_mfma_f32_16x16x32_bf16 v[164:167], a[216:219], a[148:151], v[200:203]\n"
"v_mfma_f32_16x16x32_bf16 v[168:171], a[216:219], a[152:155], v[204:207]\n"
"s_mov_b32 m0, s61\n"
"s_add_u32 s68, s64, 3072\n"
"buffer_load_dwordx4 %[dKV0], %[rK], s64 offen offset:0 nt lds\n"
"s_waitcnt lgkmcnt(3)\n"
"v_mfma_f32_16x16x32_bf16 v[160:163], a[220:223], a[156:159], v[160:163]\n"
"ds_read_b128 a[236:239], v237 offset:256\n"
"v_mfma_f32_16x16x32_bf16 v[164:167], a[220:223], a[160:163], v[164:167]\n"
"v_mfma_f32_16x16x32_bf16 v[168:171], a[220:223], a[164:167], v[168:171]\n"
"s_waitcnt lgkmcnt(3)\n"
"v_mfma_f32_16x16x32_bf16 v[160:163], a[224:227], a[168:171], v[160:163]\n"
"ds_read_b128 a[216:219], v236 offset:6144\n"
"v_mfma_f32_16x16x32_bf16 v[164:167], a[224:227], a[172:175], v[164:167]\n"
"v_mfma_f32_16x16x32_bf16 v[168:171], a[224:227], a[176:179], v[168:171]\n"
"buffer_load_dwordx4 %[dKV1], %[rK], s64 offen offset:1024 nt lds\n"
"s_waitcnt lgkmcnt(3)\n"
"v_mfma_f32_16x16x32_bf16 v[160:163], a[228:231], a[180:183], v[160:163]\n"
"ds_read_b128 a[220:223], v237 offset:6144\n"
"v_mfma_f32_16x16x32_bf16 v[164:167], a[228:231], a[184:187], v[164:167]\n"
"v_mfma_f32_16x16x32_bf16 v[168:171], a[228:231], a[188:191], v[168:171]\n"
"s_waitcnt lgkmcnt(3)\n"
"v_mfma_f32_16x16x32_bf16 v[160:163], a[232:235], a[192:195], v[160:163]\n"
"ds_read_b128 a[224:227], v236 offset:6272\n"
"v_mfma_f32_16x16x32_bf16 v[164:167], a[232:235], a[196:199], v[164:167]\n"
"v_mfma_f32_16x16x32_bf16 v[168:171], a[232:235], a[200:203], v[168:171]\n"
"buffer_load_dwordx4 %[dKV2], %[rK], s64 offen offset:2048 nt lds\n"
"s_add_u32 m0, s61, 3072\n"
"s_waitcnt lgkmcnt(3)\n"
"v_mfma_f32_16x16x32_bf16 v[160:163], a[236:239], a[204:207], v[160:163]\n"
"ds_read_b128 a[228:231], v237 offset:6272\n"
"v_mfma_f32_16x16x32_bf16 v[164:167], a[236:239], a[208:211], v[164:167]\n"
"v_mfma_f32_16x16x32_bf16 v[168:171], a[236:239], a[212:215], v[168:171]\n"
"s_waitcnt lgkmcnt(3)\n"
"v_mfma_f32_16x16x32_bf16 v[172:175], a[216:219], a[144:147], v[196:199]\n"
"ds_read_b128 a[232:235], v236 offset:6400\n"
"v_mfma_f32_16x16x32_bf16 v[176:179], a[216:219], a[148:151], v[200:203]\n"
"v_mfma_f32_16x16x32_bf16 v[180:183], a[216:219], a[152:155], v[204:207]\n"
"s_add_u32 s64, s64, 12288\n"
"buffer_load_dwordx4 %[dKV0], %[rK], s68 offen offset:0 nt lds\n"
"s_waitcnt lgkmcnt(3)\n"
"v_mfma_f32_16x16x32_bf16 v[172:175], a[220:223], a[156:159], v[172:175]\n"
"ds_read_b128 a[236:239], v237 offset:6400\n"
"v_mfma_f32_16x16x32_bf16 v[176:179], a[220:223], a[160:163], v[176:179]\n"
"v_mfma_f32_16x16x32_bf16 v[180:183], a[220:223], a[164:167], v[180:183]\n"
"buffer_load_dwordx4 %[dKV1], %[rK], s68 offen offset:1024 nt lds\n"
"s_waitcnt lgkmcnt(3)\n"
"v_mfma_f32_16x16x32_bf16 v[172:175], a[224:227], a[168:171], v[172:175]\n"
"s_add_u32 s61, %[m0b], s60\n"
"s_add_u32 s60, s60, 12288\n"
"s_cmp_eq_u32 s60, 61440\n"
"s_cselect_b32 s60, 0, s60\n"
"v_add_u32 v236, s60, %[rdW]\n"
"v_mfma_f32_16x16x32_bf16 v[176:179], a[224:227], a[172:175], v[176:179]\n"
"v_xor_b32 v237, 8, v236\n"
"v_xor_b32 v238, 64, v236\n"
"v_xor_b32 v239, 72, v236\n"
"v_xor_b32 v240, 64, v240\n"
"v_mfma_f32_16x16x32_bf16 v[180:183], a[224:227], a[176:179], v[180:183]\n"
"s_waitcnt vmcnt(20) lgkmcnt(0)\n"
"s_barrier\n"
"ds_read_b64_tr_b16 a[216:217], v236 offset:0\n"
"ds_read_b64_tr_b16 a[218:219], v236 offset:6144\n"
"v_mfma_f32_16x16x32_bf16 v[172:175], a[228:231], a[180:183], v[172:175]\n"
"ds_read_b128 v[224:227], v240 offset:0\n"
"ds_read_b64_tr_b16 a[220:221], v237 offset:0\n"
"v_mfma_f32_16x16x32_bf16 v[176:179], a[228:231], a[184:187], v[176:179]\n"
"ds_read_b64_tr_b16 a[222:223], v237 offset:6144\n"
"ds_read_b128 v[228:231], v240 offset:2048\n"
"v_mfma_f32_16x16x32_bf16 v[180:183], a[228:231], a[188:191], v[180:183]\n"
"ds_read_b128 v[232:235], v240 offset:4096\n"
"v_mfma_f32_16x16x32_bf16 v[172:175], a[232:235], a[192:195], v[172:175]\n"
"ds_read_b64_tr_b16 a[224:225], v238 offset:0\n"
"v_mfma_f32_16x16x32_bf16 v[176:179], a[232:235], a[196:199], v[176:179]\n"
"ds_read_b64_tr_b16 a[226:227], v238 offset:6144\n"
"v_mfma_f32_16x16x32_bf16 v[180:183], a[232:235], a[200:203], v[180:183]\n"
"buffer_load_dwordx4 %[dKV2], %[rK], s68 offen offset:2048 nt lds\n"
"v_mfma_f32_16x16x32_bf16 v[172:175], a[236:239], a[204:207], v[172:175]\n"
"ds_read_b64_tr_b16 a[228:229], v239 offset:0\n"
"v_mfma_f32_16x16x32_bf16 v[176:179], a[236:239], a[208:211], v[176:179]\n"
"ds_read_b64_tr_b16 a[230:231], v239 offset:6144\n"
"v_mfma_f32_16x16x32_bf16 v[180:183], a[236:239], a[212:215], v[180:183]\n"
"s_waitcnt lgkmcnt(8)\n"
"v_mfma_f32_16x16x32_bf16 v[16:19], a[216:219], v[224:227], v[16:19]\n"
"ds_read_b64_tr_b16 a[232:233], v236 offset:128\n"
"s_waitcnt lgkmcnt(6)\n"
"v_mfma_f32_16x16x32_bf16 v[20:23], a[216:219], v[228:231], v[20:23]\n"
"ds_read_b64_tr_b16 a[234:235], v236 offset:6272\n"
"s_waitcnt lgkmcnt(6)\n"
"v_mfma_f32_16x16x32_bf16 v[24:27], a[216:219], v[232:235], v[24:27]\n"
"s_mov_b32 m0, s61\n"
"s_add_u32 s68, s66, 196608\n"
"buffer_load_dwordx4 %[dW0], %[rWq], s66 offen offset:0 lds\n"
"v_mfma_f32_16x16x32_bf16 v[28:31], a[220:223], v[224:227], v[28:31]\n"
"ds_read_b64_tr_b16 a[236:237], v237 offset:128\n"
"v_max3_f32 v242, v160, v161, v162\n"
"v_max3_f32 v242, v242, v163, v164\n"
"v_mfma_f32_16x16x32_bf16 v[32:35], a[220:223], v[228:231], v[32:35]\n"
"ds_read_b64_tr_b16 a[238:239], v237 offset:6272\n"
"v_max3_f32 v242, v242, v165, v166\n"
"v_max3_f32 v242, v242, v167, v168\n"
"v_mfma_f32_16x16x32_bf16 v[36:39], a[220:223], v[232:235], v[36:39]\n"
"v_max3_f32 v242, v242, v169, v170\n"
"v_max_f32 v242, v242, v171\n"
"v_max3_f32 v243, v172, v173, v174\n"
"v_max3_f32 v243, v243, v175, v176\n"
"s_waitcnt lgkmcnt(6)\n"
"v_mfma_f32_16x16x32_bf16 v[40:43], a[224:227], v[224:227], v[40:43]\n"
"ds_read_b64_tr_b16 a[216:217], v238 offset:128\n"
"v_max3_f32 v243, v243, v177, v178\n"
"v_max3_f32 v243, v243, v179, v180\n"
"v_mfma_f32_16x16x32_bf16 v[44:47], a[224:227], v[228:231], v[44:47]\n"
"ds_read_b64_tr_b16 a[218:219], v238 offset:6272\n"
"v_max3_f32 v243, v243, v181, v182\n"
"v_max3_f32 v243, v243, v183, v242\n"
"v_mfma_f32_16x16x32_bf16 v[48:51], a[224:227], v[232:235], v[48:51]\n"
"buffer_load_dwordx4 %[dW1], %[rWq], s66 offen offset:1024 lds\n"
"v_cmp_lt_f32 vcc, s70, v243\n"
"s_waitcnt lgkmcnt(6)\n"
"v_mfma_f32_16x16x32_bf16 v[52:55], a[228:231], v[224:227], v[52:55]\n"
"ds_read_b64_tr_b16 a[220:221], v239 offset:128\n"
"s_nop 1\n"
"s_cbranch_vccz 1\n"
"s_call_b64 s[76:77], Lslow_%=\n"
"v_mfma_f32_16x16x32_bf16 v[56:59], a[228:231], v[228:231], v[56:59]\n"
"ds_read_b64_tr_b16 a[222:223], v239 offset:6272\n"
"v_exp_f32 v160, v160\n"
"v_mfma_f32_16x16x32_bf16 v[60:63], a[228:231], v[232:235], v[60:63]\n"
"v_exp_f32 v161, v161\n"
"v_exp_f32 v162, v162\n"
"s_waitcnt lgkmcnt(6)\n"
"v_mfma_f32_16x16x32_bf16 v[64:67], a[232:235], v[224:227], v[64:67]\n"
"ds_read_b64_tr_b16 a[224:225], v236 offset:256\n"
"v_exp_f32 v163, v163\n"
"v_exp_f32 v172, v172\n"
"v_mfma_f32_16x16x32_bf16 v[68:71], a[232:235], v[228:231], v[68:71]\n"
"ds_read_b64_tr_b16 a[226:227], v236 offset:6400\n"
"v_exp_f32 v173, v173\n"
"v_mfma_f32_16x16x32_bf16 v[72:75], a[232:235], v[232:235], v[72:75]\n"
"buffer_load_dwordx4 %[dW2], %[rWq], s66 offen offset:2048 lds\n"
"s_add_u32 m0, s61, 3072\n"
"v_exp_f32 v174, v174\n"
"s_waitcnt lgkmcnt(6)\n"
"v_mfma_f32_16x16x32_bf16 v[76:79], a[236:239], v[224:227], v[76:79]\n"
"ds_read_b64_tr_b16 a[228:229], v237 offset:256\n"
"v_exp_f32 v175, v175\n"
"v_cvt_pk_bf16_f32 v184, v160, v161\n"
"v_mfma_f32_16x16x32_bf16 v[80:83], a[236:239], v[228:231], v[80:83]\n"
"ds_read_b64_tr_b16 a[230:231], v237 offset:6400\n"
"v_cvt_pk_bf16_f32 v185, v162, v163\n"
"v_exp_f32 v164, v164\n"
"v_mfma_f32_16x16x32_bf16 v[84:87], a[236:239], v[232:235], v[84:87]\n"
"v_exp_f32 v165, v165\n"
"v_exp_f32 v166, v166\n"
"s_waitcnt lgkmcnt(6)\n"
"v_mfma_f32_16x16x32_bf16 v[88:91], a[216:219], v[224:227], v[88:91]\n"
"ds_read_b64_tr_b16 a[232:233], v238 offset:256\n"
"v_exp_f32 v167, v167\n"
"v_cvt_pk_bf16_f32 v186, v172, v173\n"
"v_mfma_f32_16x16x32_bf16 v[92:95], a[216:219], v[228:231], v[92:95]\n"
"ds_read_b64_tr_b16 a[234:235], v238 offset:6400\n"
"v_cvt_pk_bf16_f32 v187, v174, v175\n"
"v_exp_f32 v176, v176\n"
"v_mfma_f32_16x16x32_bf16 v[96:99], a[216:219], v[232:235], v[96:99]\n"
"s_add_u32 s66, s66, 393216\n"
"buffer_load_dwordx4 %[dW0], %[rWq], s68 offen offset:0 lds\n"
"s_waitcnt lgkmcnt(6)\n"
"v_mfma_f32_16x16x32_bf16 v[100:103], a[220:223], v[224:227], v[100:103]\n"
"ds_read_b64_tr_b16 a[236:237], v239 offset:256\n"
"v_exp_f32 v177, v177\n"
"v_exp_f32 v178, v178\n"
"v_mfma_f32_16x16x32_bf16 v[104:107], a[220:223], v[228:231], v[104:107]\n"
"ds_read_b64_tr_b16 a[238:239], v239 offset:6400\n"
"v_exp_f32 v179, v179\n"
"v_cvt_pk_bf16_f32 v188, v164, v165\n"
"v_mfma_f32_16x16x32_bf16 v[108:111], a[220:223], v[232:235], v[108:111]\n"
"buffer_load_dwordx4 %[dW1], %[rWq], s68 offen offset:1024 lds\n"
"v_cvt_pk_bf16_f32 v189, v166, v167\n"
"s_waitcnt lgkmcnt(6)\n"
"v_mfma_f32_16x16x32_bf16 v[112:115], a[224:227], v[224:227], v[112:115]\n"
"s_add_u32 s61, %[m0b], s60\n"
"s_add_u32 s60, s60, 12288\n"
"s_cmp_eq_u32 s60, 61440\n"
"s_cselect_b32 s60, 0, s60\n"
"v_add_u32 v236, s60, %[rdW]\n"
"v_mfma_f32_16x16x32_bf16 v[116:119], a[224:227], v[228:231], v[116:119]\n"
"v_xor_b32 v237, 8, v236\n"
"v_xor_b32 v238, 64, v236\n"
"v_xor_b32 v239, 72, v236\n"
"v_add_u32 v240, s62, %[rdX]\n"
"s_add_u32 s62, s62, 12288\n"
"s_cmp_eq_u32 s62, 36864\n"
"s_cselect_b32 s62, 0, s62\n"
"v_mfma_f32_16x16x32_bf16 v[120:123], a[224:227], v[232:235], v[120:123]\n"
"s_waitcnt vmcnt(17) lgkmcnt(0)\n"
"s_barrier\n"
"ds_read_b64_tr_b16 a[216:217], v236 offset:0\n"
"ds_read_b64_tr_b16 a[218:219], v236 offset:6144\n"
"v_mfma_f32_16x16x32_bf16 v[124:127], a[228:231], v[224:227], v[124:127]\n"
"ds_read_b128 a[240:243], v240 offset:0\n"
"ds_read_b64_tr_b16 a[220:221], v237 offset:0\n"
"v_mfma_f32_16x16x32_bf16 v[128:131], a[228:231], v[228:231], v[128:131]\n"
"ds_read_b64_tr_b16 a[222:223], v237 offset:6144\n"
"ds_read_b128 a[244:247], v240 offset:2048\n"
"v_exp_f32 v168, v168\n"
"v_mfma_f32_16x16x32_bf16 v[132:135], a[228:231], v[232:235], v[132:135]\n"
"ds_read_b128 a[248:251], v240 offset:4096\n"
"v_exp_f32 v169, v169\n"
"v_mfma_f32_16x16x32_bf16 v[136:139], a[232:235], v[224:227], v[136:139]\n"
"ds_read_b64_tr_b16 a[224:225], v238 offset:0\n"
"v_exp_f32 v170, v170\n"
"v_exp_f32 v171, v171\n"
"v_mfma_f32_16x16x32_bf16 v[140:143], a[232:235], v[228:231], v[140:143]\n"
"ds_read_b64_tr_b16 a[226:227], v238 offset:6144\n"
"v_cvt_pk_bf16_f32 v190, v176, v177\n"
"v_cvt_pk_bf16_f32 v191, v178, v179\n"
"v_mfma_f32_16x16x32_bf16 v[144:147], a[232:235], v[232:235], v[144:147]\n"
"buffer_load_dwordx4 %[dW2], %[rWq], s68 offen offset:2048 lds\n"
"v_exp_f32 v180, v180\n"
"v_mfma_f32_16x16x32_bf16 v[148:151], a[236:239], v[224:227], v[148:151]\n"
"ds_read_b64_tr_b16 a[228:229], v239 offset:0\n"
"v_exp_f32 v181, v181\n"
"v_exp_f32 v182, v182\n"
"v_mfma_f32_16x16x32_bf16 v[152:155], a[236:239], v[228:231], v[152:155]\n"
"ds_read_b64_tr_b16 a[230:231], v239 offset:6144\n"
"v_exp_f32 v183, v183\n"
"v_mfma_f32_16x16x32_bf16 v[156:159], a[236:239], v[232:235], v[156:159]\n"
"v_cvt_pk_bf16_f32 v192, v168, v169\n"
"v_cvt_pk_bf16_f32 v193, v170, v171\n"
"v_cvt_pk_bf16_f32 v194, v180, v181\n"
"v_cvt_pk_bf16_f32 v195, v182, v183\n"
"s_waitcnt lgkmcnt(8)\n"
"v_mfma_f32_16x16x32_bf16 v[16:19], a[216:219], a[240:243], v[16:19]\n"
"ds_read_b64_tr_b16 a[232:233], v236 offset:128\n"
"s_waitcnt lgkmcnt(6)\n"
"v_mfma_f32_16x16x32_bf16 v[20:23], a[216:219], a[244:247], v[20:23]\n"
"ds_read_b64_tr_b16 a[234:235], v236 offset:6272\n"
"s_waitcnt lgkmcnt(6)\n"
"v_mfma_f32_16x16x32_bf16 v[24:27], a[216:219], a[248:251], v[24:27]\n"
"s_mov_b32 m0, s61\n"
"s_mul_i32 s66, s56, 6144\n"
"s_add_u32 s66, s66, s57\n"
"s_add_u32 s68, s66, 196608\n"
"buffer_load_dwordx4 %[dW0], %[rWq], s66 offen offset:0 lds\n"
"v_mfma_f32_16x16x32_bf16 v[28:31], a[220:223], a[240:243], v[28:31]\n"
"ds_read_b64_tr_b16 a[236:237], v237 offset:128\n"
"v_mfma_f32_16x16x32_bf16 v[32:35], a[220:223], a[244:247], v[32:35]\n"
"ds_read_b64_tr_b16 a[238:239], v237 offset:6272\n"
"v_mfma_f32_16x16x32_bf16 v[36:39], a[220:223], a[248:251], v[36:39]\n"
"buffer_load_dwordx4 %[dW1], %[rWq], s66 offen offset:1024 lds\n"
"s_waitcnt lgkmcnt(6)\n"
"v_mfma_f32_16x16x32_bf16 v[40:43], a[224:227], a[240:243], v[40:43]\n"
"ds_read_b64_tr_b16 a[216:217], v238 offset:128\n"
"v_mfma_f32_16x16x32_bf16 v[44:47], a[224:227], a[244:247], v[44:47]\n"
"ds_read_b64_tr_b16 a[218:219], v238 offset:6272\n"
"v_mfma_f32_16x16x32_bf16 v[48:51], a[224:227], a[248:251], v[48:51]\n"
"s_waitcnt lgkmcnt(6)\n"
"v_mfma_f32_16x16x32_bf16 v[52:55], a[228:231], a[240:243], v[52:55]\n"
"ds_read_b64_tr_b16 a[220:221], v239 offset:128\n"
"v_mfma_f32_16x16x32_bf16 v[56:59], a[228:231], a[244:247], v[56:59]\n"
"ds_read_b64_tr_b16 a[222:223], v239 offset:6272\n"
"v_mfma_f32_16x16x32_bf16 v[60:63], a[228:231], a[248:251], v[60:63]\n"
"buffer_load_dwordx4 %[dW2], %[rWq], s66 offen offset:2048 lds\n"
"s_add_u32 m0, s61, 3072\n"
"s_waitcnt lgkmcnt(6)\n"
"v_mfma_f32_16x16x32_bf16 v[64:67], a[232:235], a[240:243], v[64:67]\n"
"ds_read_b64_tr_b16 a[224:225], v236 offset:256\n"
"v_mfma_f32_16x16x32_bf16 v[68:71], a[232:235], a[244:247], v[68:71]\n"
"ds_read_b64_tr_b16 a[226:227], v236 offset:6400\n"
"v_mfma_f32_16x16x32_bf16 v[72:75], a[232:235], a[248:251], v[72:75]\n"
"s_add_u32 s66, s66, 393216\n"
"buffer_load_dwordx4 %[dW0], %[rWq], s68 offen offset:0 lds\n"
"s_waitcnt lgkmcnt(6)\n"
"v_mfma_f32_16x16x32_bf16 v[76:79], a[236:239], a[240:243], v[76:79]\n"
"ds_read_b64_tr_b16 a[228:229], v237 offset:256\n"
"v_mfma_f32_16x16x32_bf16 v[80:83], a[236:239], a[244:247], v[80:83]\n"
"ds_read_b64_tr_b16 a[230:231], v237 offset:6400\n"
"v_mfma_f32_16x16x32_bf16 v[84:87], a[236:239], a[248:251], v[84:87]\n"
"s_waitcnt lgkmcnt(6)\n"
"v_mfma_f32_16x16x32_bf16 v[88:91], a[216:219], a[240:243], v[88:91]\n"
"ds_read_b64_tr_b16 a[232:233], v238 offset:256\n"
"v_mfma_f32_16x16x32_bf16 v[92:95], a[216:219], a[244:247], v[92:95]\n"
"ds_read_b64_tr_b16 a[234:235], v238 offset:6400\n"
"v_mfma_f32_16x16x32_bf16 v[96:99], a[216:219], a[248:251], v[96:99]\n"
"buffer_load_dwordx4 %[dW1], %[rWq], s68 offen offset:1024 lds\n"
"s_waitcnt lgkmcnt(6)\n"
"v_mfma_f32_16x16x32_bf16 v[100:103], a[220:223], a[240:243], v[100:103]\n"
"ds_read_b64_tr_b16 a[236:237], v239 offset:256\n"
"v_mfma_f32_16x16x32_bf16 v[104:107], a[220:223], a[244:247], v[104:107]\n"
"ds_read_b64_tr_b16 a[238:239], v239 offset:6400\n"
"v_mfma_f32_16x16x32_bf16 v[108:111], a[220:223], a[248:251], v[108:111]\n"
"buffer_load_dwordx4 %[dW2], %[rWq], s68 offen offset:2048 lds\n"
"s_waitcnt lgkmcnt(6)\n"
"v_mfma_f32_16x16x32_bf16 v[112:115], a[224:227], a[240:243], v[112:115]\n"
"s_add_u32 s61, %[m0b], s60\n"
"s_add_u32 s60, s60, 12288\n"
"s_cmp_eq_u32 s60, 61440\n"
"s_cselect_b32 s60, 0, s60\n"
"v_add_u32 v236, s60, %[rdV]\n"
"v_mfma_f32_16x16x32_bf16 v[116:119], a[224:227], a[244:247], v[116:119]\n"
"v_xor_b32 v237, 32, v236\n"
"v_xor_b32 v238, 64, v236\n"
"v_xor_b32 v239, 96, v236\n"
"v_mfma_f32_16x16x32_bf16 v[120:123], a[224:227], a[248:251], v[120:123]\n"
"s_waitcnt vmcnt(18) lgkmcnt(0)\n"
"s_barrier\n"
"ds_read_b64_tr_b16 a[216:217], v236 offset:0\n"
"ds_read_b64_tr_b16 a[218:219], v236 offset:6144\n"
"v_mfma_f32_16x16x32_bf16 v[124:127], a[228:231], a[240:243], v[124:127]\n"
"ds_read_b64_tr_b16 a[220:221], v237 offset:0\n"
"v_mfma_f32_16x16x32_bf16 v[128:131], a[228:231], a[244:247], v[128:131]\n"
"ds_read_b64_tr_b16 a[222:223], v237 offset:6144\n"
"v_mfma_f32_16x16x32_bf16 v[132:135], a[228:231], a[248:251], v[132:135]\n"
"v_mfma_f32_16x16x32_bf16 v[136:139], a[232:235], a[240:243], v[136:139]\n"
"ds_read_b64_tr_b16 a[224:225], v238 offset:0\n"
"v_mfma_f32_16x16x32_bf16 v[140:143], a[232:235], a[244:247], v[140:143]\n"
"ds_read_b64_tr_b16 a[226:227], v238 offset:6144\n"
"v_mfma_f32_16x16x32_bf16 v[144:147], a[232:235], a[248:251], v[144:147]\n"
"s_add_u32 m0, %[m0x], s63\n"
"s_add_u32 s67, %[x0], s56\n"
"s_add_u32 s75, s67, 9216\n"
"buffer_load_dwordx4 %[dX], %[rX], s67 offen offset:0 lds\n"
"v_mfma_f32_16x16x32_bf16 v[148:151], a[236:239], a[240:243], v[148:151]\n"
"ds_read_b64_tr_b16 a[228:229], v239 offset:0\n"
"v_mfma_f32_16x16x32_bf16 v[152:155], a[236:239], a[244:247], v[152:155]\n"
"ds_read_b64_tr_b16 a[230:231], v239 offset:6144\n"
"v_mfma_f32_16x16x32_bf16 v[156:159], a[236:239], a[248:251], v[156:159]\n"
"buffer_load_dwordx4 %[dX], %[rX], s75 offen offset:1024 lds\n"
"s_add_u32 s75, s67, 18432\n"
"s_waitcnt lgkmcnt(6)\n"
"v_mfma_f32_16x16x32_bf16 a[0:3], a[216:219], v[184:187], a[0:3]\n"
"ds_read_b64_tr_b16 a[232:233], v236 offset:128\n"
"v_mfma_f32_16x16x32_bf16 a[4:7], a[216:219], v[188:191], a[4:7]\n"
"ds_read_b64_tr_b16 a[234:235], v236 offset:6272\n"
"v_mfma_f32_16x16x32_bf16 a[8:11], a[216:219], v[192:195], a[8:11]\n"
"buffer_load_dwordx4 %[dX], %[rX], s75 offen offset:2048 lds\n"
"s_add_u32 s56, s56, 128\n"
"s_cmp_eq_u32 s56, 1280\n"
"s_cselect_b32 s56, 0, s56\n"
"s_add_u32 s63, s63, 12288\n"
"s_cmp_eq_u32 s63, 36864\n"
"s_cselect_b32 s63, 0, s63\n"
"s_waitcnt lgkmcnt(6)\n"
"v_mfma_f32_16x16x32_bf16 a[12:15], a[220:223], v[184:187], a[12:15]\n"
"ds_read_b64_tr_b16 a[236:237], v237 offset:128\n"
"v_mfma_f32_16x16x32_bf16 a[16:19], a[220:223], v[188:191], a[16:19]\n"
"ds_read_b64_tr_b16 a[238:239], v237 offset:6272\n"
"v_mfma_f32_16x16x32_bf16 a[20:23], a[220:223], v[192:195], a[20:23]\n"
"s_mov_b32 m0, s61\n"
"s_add_u32 s68, s65, 3072\n"
"buffer_load_dwordx4 %[dKV0], %[rV], s65 offen offset:0 nt lds\n"
"s_waitcnt lgkmcnt(6)\n"
"v_mfma_f32_16x16x32_bf16 a[24:27], a[224:227], v[184:187], a[24:27]\n"
"ds_read_b64_tr_b16 a[216:217], v238 offset:128\n"
"v_mfma_f32_16x16x32_bf16 a[28:31], a[224:227], v[188:191], a[28:31]\n"
"ds_read_b64_tr_b16 a[218:219], v238 offset:6272\n"
"v_mfma_f32_16x16x32_bf16 a[32:35], a[224:227], v[192:195], a[32:35]\n"
"s_waitcnt lgkmcnt(6)\n"
"v_mfma_f32_16x16x32_bf16 a[36:39], a[228:231], v[184:187], a[36:39]\n"
"ds_read_b64_tr_b16 a[220:221], v239 offset:128\n"
"v_mfma_f32_16x16x32_bf16 a[40:43], a[228:231], v[188:191], a[40:43]\n"
"ds_read_b64_tr_b16 a[222:223], v239 offset:6272\n"
"v_mfma_f32_16x16x32_bf16 a[44:47], a[228:231], v[192:195], a[44:47]\n"
"buffer_load_dwordx4 %[dKV1], %[rV], s65 offen offset:1024 nt lds\n"
"s_waitcnt lgkmcnt(6)\n"
"v_mfma_f32_16x16x32_bf16 a[48:51], a[232:235], v[184:187], a[48:51]\n"
"ds_read_b64_tr_b16 a[224:225], v236 offset:256\n"
"v_mfma_f32_16x16x32_bf16 a[52:55], a[232:235], v[188:191], a[52:55]\n"
"ds_read_b64_tr_b16 a[226:227], v236 offset:6400\n"
"v_mfma_f32_16x16x32_bf16 a[56:59], a[232:235], v[192:195], a[56:59]\n"
"s_waitcnt lgkmcnt(6)\n"
"v_mfma_f32_16x16x32_bf16 a[60:63], a[236:239], v[184:187], a[60:63]\n"
"ds_read_b64_tr_b16 a[228:229], v237 offset:256\n"
"v_mfma_f32_16x16x32_bf16 a[64:67], a[236:239], v[188:191], a[64:67]\n"
"ds_read_b64_tr_b16 a[230:231], v237 offset:6400\n"
"v_mfma_f32_16x16x32_bf16 a[68:71], a[236:239], v[192:195], a[68:71]\n"
"buffer_load_dwordx4 %[dKV2], %[rV], s65 offen offset:2048 nt lds\n"
"s_add_u32 m0, s61, 3072\n"
"s_waitcnt lgkmcnt(6)\n"
"v_mfma_f32_16x16x32_bf16 a[72:75], a[216:219], v[184:187], a[72:75]\n"
"ds_read_b64_tr_b16 a[232:233], v238 offset:256\n"
"v_mfma_f32_16x16x32_bf16 a[76:79], a[216:219], v[188:191], a[76:79]\n"
"ds_read_b64_tr_b16 a[234:235], v238 offset:6400\n"
"v_mfma_f32_16x16x32_bf16 a[80:83], a[216:219], v[192:195], a[80:83]\n"
"s_add_u32 s65, s65, 12288\n"
"buffer_load_dwordx4 %[dKV0], %[rV], s68 offen offset:0 nt lds\n"
"s_waitcnt lgkmcnt(6)\n"
"v_mfma_f32_16x16x32_bf16 a[84:87], a[220:223], v[184:187], a[84:87]\n"
"ds_read_b64_tr_b16 a[236:237], v239 offset:256\n"
"v_mfma_f32_16x16x32_bf16 a[88:91], a[220:223], v[188:191], a[88:91]\n"
"ds_read_b64_tr_b16 a[238:239], v239 offset:6400\n"
"v_mfma_f32_16x16x32_bf16 a[92:95], a[220:223], v[192:195], a[92:95]\n"
"buffer_load_dwordx4 %[dKV1], %[rV], s68 offen offset:1024 nt lds\n"
"s_waitcnt lgkmcnt(6)\n"
"v_mfma_f32_16x16x32_bf16 a[96:99], a[224:227], v[184:187], a[96:99]\n"
"s_add_u32 s61, %[m0b], s60\n"
"s_add_u32 s60, s60, 12288\n"
"s_cmp_eq_u32 s60, 61440\n"
"s_cselect_b32 s60, 0, s60\n"
"v_add_u32 v236, s60, %[rdK]\n"
"v_mfma_f32_16x16x32_bf16 a[100:103], a[224:227], v[188:191], a[100:103]\n"
"v_xor_b32 v237, 64, v236\n"
"v_mfma_f32_16x16x32_bf16 a[104:107], a[224:227], v[192:195], a[104:107]\n"
"s_waitcnt vmcnt(20) lgkmcnt(0)\n"
"s_barrier\n"
"ds_read_b128 a[216:219], v236 offset:0\n"
"v_mfma_f32_16x16x32_bf16 a[108:111], a[228:231], v[184:187], a[108:111]\n"
"ds_read_b128 a[220:223], v237 offset:0\n"
"v_mfma_f32_16x16x32_bf16 a[112:115], a[228:231], v[188:191], a[112:115]\n"
"v_mfma_f32_16x16x32_bf16 a[116:119], a[228:231], v[192:195], a[116:119]\n"
"v_mfma_f32_16x16x32_bf16 a[120:123], a[232:235], v[184:187], a[120:123]\n"
"ds_read_b128 a[224:227], v236 offset:128\n"
"v_mfma_f32_16x16x32_bf16 a[124:127], a[232:235], v[188:191], a[124:127]\n"
"v_mfma_f32_16x16x32_bf16 a[128:131], a[232:235], v[192:195], a[128:131]\n"
"buffer_load_dwordx4 %[dKV2], %[rV], s68 offen offset:2048 nt lds\n"
"v_mfma_f32_16x16x32_bf16 a[132:135], a[236:239], v[184:187], a[132:135]\n"
"ds_read_b128 a[228:231], v237 offset:128\n"
"v_mfma_f32_16x16x32_bf16 a[136:139], a[236:239], v[188:191], a[136:139]\n"
"v_mfma_f32_16x16x32_bf16 a[140:143], a[236:239], v[192:195], a[140:143]\n"
"v_mfma_f32_16x16x32_bf16 v[208:211], v[220:223], v[184:187], v[208:211]\n"
"v_mfma_f32_16x16x32_bf16 v[212:215], v[220:223], v[188:191], v[212:215]\n"
"v_mfma_f32_16x16x32_bf16 v[216:219], v[220:223], v[192:195], v[216:219]\n"
"s_waitcnt lgkmcnt(3)\n"
"v_mfma_f32_16x16x32_bf16 v[160:163], a[216:219], a[144:147], v[196:199]\n"
"ds_read_b128 a[232:235], v236 offset:256\n"
"v_mfma_f32_16x16x32_bf16 v[164:167], a[216:219], a[148:151], v[200:203]\n"
"v_mfma_f32_16x16x32_bf16 v[168:171], a[216:219], a[152:155], v[204:207]\n"
"s_mov_b32 m0, s61\n"
"s_add_u32 s68, s64, 3072\n"
"buffer_load_dwordx4 %[dKV0], %[rK], s64 offen offset:0 nt lds\n"
"s_waitcnt lgkmcnt(3)\n"
"v_mfma_f32_16x16x32_bf16 v[160:163], a[220:223], a[156:159], v[160:163]\n"
"ds_read_b128 a[236:239], v237 offset:256\n"
"v_mfma_f32_16x16x32_bf16 v[164:167], a[220:223], a[160:163], v[164:167]\n"
"v_mfma_f32_16x16x32_bf16 v[168:171], a[220:223], a[164:167], v[168:171]\n"
"s_waitcnt lgkmcnt(3)\n"
"v_mfma_f32_16x16x32_bf16 v[160:163], a[224:227], a[168:171], v[160:163]\n"
"ds_read_b128 a[216:219], v236 offset:6144\n"
"v_mfma_f32_16x16x32_bf16 v[164:167], a[224:227], a[172:175], v[164:167]\n"
"v_mfma_f32_16x16x32_bf16 v[168:171], a[224:227], a[176:179], v[168:171]\n"
"buffer_load_dwordx4 %[dKV1], %[rK], s64 offen offset:1024 nt lds\n"
"s_waitcnt lgkmcnt(3)\n"
"v_mfma_f32_16x16x32_bf16 v[160:163], a[228:231], a[180:183], v[160:163]\n"
"ds_read_b128 a[220:223], v237 offset:6144\n"
"v_mfma_f32_16x16x32_bf16 v[164:167], a[228:231], a[184:187], v[164:167]\n"
"v_mfma_f32_16x16x32_bf16 v[168:171], a[228:231], a[188:191], v[168:171]\n"
"s_waitcnt lgkmcnt(3)\n"
"v_mfma_f32_16x16x32_bf16 v[160:163], a[232:235], a[192:195], v[160:163]\n"
"ds_read_b128 a[224:227], v236 offset:6272\n"
"v_mfma_f32_16x16x32_bf16 v[164:167], a[232:235], a[196:199], v[164:167]\n"
"v_mfma_f32_16x16x32_bf16 v[168:171], a[232:235], a[200:203], v[168:171]\n"
"buffer_load_dwordx4 %[dKV2], %[rK], s64 offen offset:2048 nt lds\n"
"s_add_u32 m0, s61, 3072\n"
"s_waitcnt lgkmcnt(3)\n"
"v_mfma_f32_16x16x32_bf16 v[160:163], a[236:239], a[204:207], v[160:163]\n"
"ds_read_b128 a[228:231], v237 offset:6272\n"
"v_mfma_f32_16x16x32_bf16 v[164:167], a[236:239], a[208:211], v[164:167]\n"
"v_mfma_f32_16x16x32_bf16 v[168:171], a[236:239], a[212:215], v[168:171]\n"
"s_waitcnt lgkmcnt(3)\n"
"v_mfma_f32_16x16x32_bf16 v[172:175], a[216:219], a[144:147], v[196:199]\n"
"ds_read_b128 a[232:235], v236 offset:6400\n"
"v_mfma_f32_16x16x32_bf16 v[176:179], a[216:219], a[148:151], v[200:203]\n"
"v_mfma_f32_16x16x32_bf16 v[180:183], a[216:219], a[152:155], v[204:207]\n"
"s_add_u32 s64, s64, 12288\n"
"buffer_load_dwordx4 %[dKV0], %[rK], s68 offen offset:0 nt lds\n"
"s_waitcnt lgkmcnt(3)\n"
"v_mfma_f32_16x16x32_bf16 v[172:175], a[220:223], a[156:159], v[172:175]\n"
"ds_read_b128 a[236:239], v237 offset:6400\n"
"v_mfma_f32_16x16x32_bf16 v[176:179], a[220:223], a[160:163], v[176:179]\n"
"v_mfma_f32_16x16x32_bf16 v[180:183], a[220:223], a[164:167], v[180:183]\n"
"buffer_load_dwordx4 %[dKV1], %[rK], s68 offen offset:1024 nt lds\n"
"s_waitcnt lgkmcnt(3)\n"
"v_mfma_f32_16x16x32_bf16 v[172:175], a[224:227], a[168:171], v[172:175]\n"
"s_add_u32 s61, %[m0b], s60\n"
"s_add_u32 s60, s60, 12288\n"
"s_cmp_eq_u32 s60, 61440\n"
"s_cselect_b32 s60, 0, s60\n"
"v_add_u32 v236, s60, %[rdW]\n"
"v_mfma_f32_16x16x32_bf16 v[176:179], a[224:227], a[172:175], v[176:179]\n"
"v_xor_b32 v237, 8, v236\n"
"v_xor_b32 v238, 64, v236\n"
"v_xor_b32 v239, 72, v236\n"
"v_xor_b32 v240, 64, v240\n"
"v_mfma_f32_16x16x32_bf16 v[180:183], a[224:227], a[176:179], v[180:183]\n"
"s_waitcnt vmcnt(20) lgkmcnt(0)\n"
"s_barrier\n"
"ds_read_b64_tr_b16 a[216:217], v236 offset:0\n"
"ds_read_b64_tr_b16 a[218:219], v236 offset:6144\n"
"v_mfma_f32_16x16x32_bf16 v[172:175], a[228:231], a[180:183], v[172:175]\n"
"ds_read_b128 v[224:227], v240 offset:0\n"
"ds_read_b64_tr_b16 a[220:221], v237 offset:0\n"
"v_mfma_f32_16x16x32_bf16 v[176:179], a[228:231], a[184:187], v[176:179]\n"
"ds_read_b64_tr_b16 a[222:223], v237 offset:6144\n"
"ds_read_b128 v[228:231], v240 offset:2048\n"
"v_mfma_f32_16x16x32_bf16 v[180:183], a[228:231], a[188:191], v[180:183]\n"
"ds_read_b128 v[232:235], v240 offset:4096\n"
"v_mfma_f32_16x16x32_bf16 v[172:175], a[232:235], a[192:195], v[172:175]\n"
"ds_read_b64_tr_b16 a[224:225], v238 offset:0\n"
"v_mfma_f32_16x16x32_bf16 v[176:179], a[232:235], a[196:199], v[176:179]\n"
"ds_read_b64_tr_b16 a[226:227], v238 offset:6144\n"
"v_mfma_f32_16x16x32_bf16 v[180:183], a[232:235], a[200:203], v[180:183]\n"
"buffer_load_dwordx4 %[dKV2], %[rK], s68 offen offset:2048 nt lds\n"
"v_mfma_f32_16x16x32_bf16 v[172:175], a[236:239], a[204:207], v[172:175]\n"
"ds_read_b64_tr_b16 a[228:229], v239 offset:0\n"
"v_mfma_f32_16x16x32_bf16 v[176:179], a[236:239], a[208:211], v[176:179]\n"
"ds_read_b64_tr_b16 a[230:231], v239 offset:6144\n"
"v_mfma_f32_16x16x32_bf16 v[180:183], a[236:239], a[212:215], v[180:183]\n"
"s_waitcnt lgkmcnt(8)\n"
"v_mfma_f32_16x16x32_bf16 v[16:19], a[216:219], v[224:227], v[16:19]\n"
"ds_read_b64_tr_b16 a[232:233], v236 offset:128\n"
"s_waitcnt lgkmcnt(6)\n"
"v_mfma_f32_16x16x32_bf16 v[20:23], a[216:219], v[228:231], v[20:23]\n"
"ds_read_b64_tr_b16 a[234:235], v236 offset:6272\n"
"s_waitcnt lgkmcnt(6)\n"
"v_mfma_f32_16x16x32_bf16 v[24:27], a[216:219], v[232:235], v[24:27]\n"
"s_mov_b32 m0, s61\n"
"s_add_u32 s68, s66, 196608\n"
"buffer_load_dwordx4 %[dW0], %[rWq], s66 offen offset:0 lds\n"
"v_mfma_f32_16x16x32_bf16 v[28:31], a[220:223], v[224:227], v[28:31]\n"
"ds_read_b64_tr_b16 a[236:237], v237 offset:128\n"
"v_max3_f32 v242, v160, v161, v162\n"
"v_max3_f32 v242, v242, v163, v164\n"
"v_mfma_f32_16x16x32_bf16 v[32:35], a[220:223], v[228:231], v[32:35]\n"
"ds_read_b64_tr_b16 a[238:239], v237 offset:6272\n"
"v_max3_f32 v242, v242, v165, v166\n"
"v_max3_f32 v242, v242, v167, v168\n"
"v_mfma_f32_16x16x32_bf16 v[36:39], a[220:223], v[232:235], v[36:39]\n"
"v_max3_f32 v242, v242, v169, v170\n"
"v_max_f32 v242, v242, v171\n"
"v_max3_f32 v243, v172, v173, v174\n"
"v_max3_f32 v243, v243, v175, v176\n"
"s_waitcnt lgkmcnt(6)\n"
"v_mfma_f32_16x16x32_bf16 v[40:43], a[224:227], v[224:227], v[40:43]\n"
"ds_read_b64_tr_b16 a[216:217], v238 offset:128\n"
"v_max3_f32 v243, v243, v177, v178\n"
"v_max3_f32 v243, v243, v179, v180\n"
"v_mfma_f32_16x16x32_bf16 v[44:47], a[224:227], v[228:231], v[44:47]\n"
"ds_read_b64_tr_b16 a[218:219], v238 offset:6272\n"
"v_max3_f32 v243, v243, v181, v182\n"
"v_max3_f32 v243, v243, v183, v242\n"
"v_mfma_f32_16x16x32_bf16 v[48:51], a[224:227], v[232:235], v[48:51]\n"
"buffer_load_dwordx4 %[dW1], %[rWq], s66 offen offset:1024 lds\n"
"v_cmp_lt_f32 vcc, s70, v243\n"
"s_waitcnt lgkmcnt(6)\n"
"v_mfma_f32_16x16x32_bf16 v[52:55], a[228:231], v[224:227], v[52:55]\n"
"ds_read_b64_tr_b16 a[220:221], v239 offset:128\n"
"s_nop 1\n"
"s_cbranch_vccz 1\n"
"s_call_b64 s[76:77], Lslow_%=\n"
"v_mfma_f32_16x16x32_bf16 v[56:59], a[228:231], v[228:231], v[56:59]\n"
"ds_read_b64_tr_b16 a[222:223], v239 offset:6272\n"
"v_exp_f32 v160, v160\n"
"v_mfma_f32_16x16x32_bf16 v[60:63], a[228:231], v[232:235], v[60:63]\n"
"v_exp_f32 v161, v161\n"
"v_exp_f32 v162, v162\n"
"s_waitcnt lgkmcnt(6)\n"
"v_mfma_f32_16x16x32_bf16 v[64:67], a[232:235], v[224:227], v[64:67]\n"
"ds_read_b64_tr_b16 a[224:225], v236 offset:256\n"
"v_exp_f32 v163, v163\n"
"v_exp_f32 v172, v172\n"
"v_mfma_f32_16x16x32_bf16 v[68:71], a[232:235], v[228:231], v[68:71]\n"
"ds_read_b64_tr_b16 a[226:227], v236 offset:6400\n"
"v_exp_f32 v173, v173\n"
"v_mfma_f32_16x16x32_bf16 v[72:75], a[232:235], v[232:235], v[72:75]\n"
"buffer_load_dwordx4 %[dW2], %[rWq], s66 offen offset:2048 lds\n"
"s_add_u32 m0, s61, 3072\n"
"v_exp_f32 v174, v174\n"
"s_waitcnt lgkmcnt(6)\n"
"v_mfma_f32_16x16x32_bf16 v[76:79], a[236:239], v[224:227], v[76:79]\n"
"ds_read_b64_tr_b16 a[228:229], v237 offset:256\n"
"v_exp_f32 v175, v175\n"
"v_cvt_pk_bf16_f32 v184, v160, v161\n"
"v_mfma_f32_16x16x32_bf16 v[80:83], a[236:239], v[228:231], v[80:83]\n"
"ds_read_b64_tr_b16 a[230:231], v237 offset:6400\n"
"v_cvt_pk_bf16_f32 v185, v162, v163\n"
"v_exp_f32 v164, v164\n"
"v_mfma_f32_16x16x32_bf16 v[84:87], a[236:239], v[232:235], v[84:87]\n"
"v_exp_f32 v165, v165\n"
"v_exp_f32 v166, v166\n"
"s_waitcnt lgkmcnt(6)\n"
"v_mfma_f32_16x16x32_bf16 v[88:91], a[216:219], v[224:227], v[88:91]\n"
"ds_read_b64_tr_b16 a[232:233], v238 offset:256\n"
"v_exp_f32 v167, v167\n"
"v_cvt_pk_bf16_f32 v186, v172, v173\n"
"v_mfma_f32_16x16x32_bf16 v[92:95], a[216:219], v[228:231], v[92:95]\n"
"ds_read_b64_tr_b16 a[234:235], v238 offset:6400\n"
"v_cvt_pk_bf16_f32 v187, v174, v175\n"
"v_exp_f32 v176, v176\n"
"v_mfma_f32_16x16x32_bf16 v[96:99], a[216:219], v[232:235], v[96:99]\n"
"s_add_u32 s66, s66, 393216\n"
"buffer_load_dwordx4 %[dW0], %[rWq], s68 offen offset:0 lds\n"
"s_waitcnt lgkmcnt(6)\n"
"v_mfma_f32_16x16x32_bf16 v[100:103], a[220:223], v[224:227], v[100:103]\n"
"ds_read_b64_tr_b16 a[236:237], v239 offset:256\n"
"v_exp_f32 v177, v177\n"
"v_exp_f32 v178, v178\n"
"v_mfma_f32_16x16x32_bf16 v[104:107], a[220:223], v[228:231], v[104:107]\n"
"ds_read_b64_tr_b16 a[238:239], v239 offset:6400\n"
"v_exp_f32 v179, v179\n"
"v_cvt_pk_bf16_f32 v188, v164, v165\n"
"v_mfma_f32_16x16x32_bf16 v[108:111], a[220:223], v[232:235], v[108:111]\n"
"buffer_load_dwordx4 %[dW1], %[rWq], s68 offen offset:1024 lds\n"
"v_cvt_pk_bf16_f32 v189, v166, v167\n"
"s_waitcnt lgkmcnt(6)\n"
"v_mfma_f32_16x16x32_bf16 v[112:115], a[224:227], v[224:227], v[112:115]\n"
"s_add_u32 s61, %[m0b], s60\n"
"s_add_u32 s60, s60, 12288\n"
"s_cmp_eq_u32 s60, 61440\n"
"s_cselect_b32 s60, 0, s60\n"
"v_add_u32 v236, s60, %[rdW]\n"
"v_mfma_f32_16x16x32_bf16 v[116:119], a[224:227], v[228:231], v[116:119]\n"
"v_xor_b32 v237, 8, v236\n"
"v_xor_b32 v238, 64, v236\n"
"v_xor_b32 v239, 72, v236\n"
"v_add_u32 v240, s62, %[rdX]\n"
"s_add_u32 s62, s62, 12288\n"
"s_cmp_eq_u32 s62, 36864\n"
"s_cselect_b32 s62, 0, s62\n"
"v_mfma_f32_16x16x32_bf16 v[120:123], a[224:227], v[232:235], v[120:123]\n"
"s_waitcnt vmcnt(17) lgkmcnt(0)\n"
"s_barrier\n"
"ds_read_b64_tr_b16 a[216:217], v236 offset:0\n"
"ds_read_b64_tr_b16 a[218:219], v236 offset:6144\n"
"v_mfma_f32_16x16x32_bf16 v[124:127], a[228:231], v[224:227], v[124:127]\n"
"ds_read_b128 a[240:243], v240 offset:0\n"
"ds_read_b64_tr_b16 a[220:221], v237 offset:0\n"
"v_mfma_f32_16x16x32_bf16 v[128:131], a[228:231], v[228:231], v[128:131]\n"
"ds_read_b64_tr_b16 a[222:223], v237 offset:6144\n"
"ds_read_b128 a[244:247], v240 offset:2048\n"
"v_exp_f32 v168, v168\n"
"v_mfma_f32_16x16x32_bf16 v[132:135], a[228:231], v[232:235], v[132:135]\n"
"ds_read_b128 a[248:251], v240 offset:4096\n"
"v_exp_f32 v169, v169\n"
"v_mfma_f32_16x16x32_bf16 v[136:139], a[232:235], v[224:227], v[136:139]\n"
"ds_read_b64_tr_b16 a[224:225], v238 offset:0\n"
"v_exp_f32 v170, v170\n"
"v_exp_f32 v171, v171\n"
"v_mfma_f32_16x16x32_bf16 v[140:143], a[232:235], v[228:231], v[140:143]\n"
"ds_read_b64_tr_b16 a[226:227], v238 offset:6144\n"
"v_cvt_pk_bf16_f32 v190, v176, v177\n"
"v_cvt_pk_bf16_f32 v191, v178, v179\n"
"v_mfma_f32_16x16x32_bf16 v[144:147], a[232:235], v[232:235], v[144:147]\n"
"buffer_load_dwordx4 %[dW2], %[rWq], s68 offen offset:2048 lds\n"
"v_exp_f32 v180, v180\n"
"v_mfma_f32_16x16x32_bf16 v[148:151], a[236:239], v[224:227], v[148:151]\n"
"ds_read_b64_tr_b16 a[228:229], v239 offset:0\n"
"v_exp_f32 v181, v181\n"
"v_exp_f32 v182, v182\n"
"v_mfma_f32_16x16x32_bf16 v[152:155], a[236:239], v[228:231], v[152:155]\n"
"ds_read_b64_tr_b16 a[230:231], v239 offset:6144\n"
"v_exp_f32 v183, v183\n"
"v_mfma_f32_16x16x32_bf16 v[156:159], a[236:239], v[232:235], v[156:159]\n"
"v_cvt_pk_bf16_f32 v192, v168, v169\n"
"v_cvt_pk_bf16_f32 v193, v170, v171\n"
"v_cvt_pk_bf16_f32 v194, v180, v181\n"
"v_cvt_pk_bf16_f32 v195, v182, v183\n"
"s_waitcnt lgkmcnt(8)\n"
"v_mfma_f32_16x16x32_bf16 v[16:19], a[216:219], a[240:243], v[16:19]\n"
"ds_read_b64_tr_b16 a[232:233], v236 offset:128\n"
"s_waitcnt lgkmcnt(6)\n"
"v_mfma_f32_16x16x32_bf16 v[20:23], a[216:219], a[244:247], v[20:23]\n"
"ds_read_b64_tr_b16 a[234:235], v236 offset:6272\n"
"s_waitcnt lgkmcnt(6)\n"
"v_mfma_f32_16x16x32_bf16 v[24:27], a[216:219], a[248:251], v[24:27]\n"
"s_mov_b32 m0, s61\n"
"s_add_u32 s68, s65, 3072\n"
"buffer_load_dwordx4 %[dKV0], %[rV], s65 offen offset:0 nt lds\n"
"v_mfma_f32_16x16x32_bf16 v[28:31], a[220:223], a[240:243], v[28:31]\n"
"ds_read_b64_tr_b16 a[236:237], v237 offset:128\n"
"v_mfma_f32_16x16x32_bf16 v[32:35], a[220:223], a[244:247], v[32:35]\n"
"ds_read_b64_tr_b16 a[238:239], v237 offset:6272\n"
"v_mfma_f32_16x16x32_bf16 v[36:39], a[220:223], a[248:251], v[36:39]\n"
"s_waitcnt lgkmcnt(6)\n"
"v_mfma_f32_16x16x32_bf16 v[40:43], a[224:227], a[240:243], v[40:43]\n"
"ds_read_b64_tr_b16 a[216:217], v238 offset:128\n"
"v_mfma_f32_16x16x32_bf16 v[44:47], a[224:227], a[244:247], v[44:47]\n"
"ds_read_b64_tr_b16 a[218:219], v238 offset:6272\n"
"v_mfma_f32_16x16x32_bf16 v[48:51], a[224:227], a[248:251], v[48:51]\n"
"buffer_load_dwordx4 %[dKV1], %[rV], s65 offen offset:1024 nt lds\n"
"s_waitcnt lgkmcnt(6)\n"
"v_mfma_f32_16x16x32_bf16 v[52:55], a[228:231], a[240:243], v[52:55]\n"
"ds_read_b64_tr_b16 a[220:221], v239 offset:128\n"
"v_mfma_f32_16x16x32_bf16 v[56:59], a[228:231], a[244:247], v[56:59]\n"
"ds_read_b64_tr_b16 a[222:223], v239 offset:6272\n"
"v_mfma_f32_16x16x32_bf16 v[60:63], a[228:231], a[248:251], v[60:63]\n"
"s_waitcnt lgkmcnt(6)\n"
"v_mfma_f32_16x16x32_bf16 v[64:67], a[232:235], a[240:243], v[64:67]\n"
"ds_read_b64_tr_b16 a[224:225], v236 offset:256\n"
"v_mfma_f32_16x16x32_bf16 v[68:71], a[232:235], a[244:247], v[68:71]\n"
"ds_read_b64_tr_b16 a[226:227], v236 offset:6400\n"
"v_mfma_f32_16x16x32_bf16 v[72:75], a[232:235], a[248:251], v[72:75]\n"
"buffer_load_dwordx4 %[dKV2], %[rV], s65 offen offset:2048 nt lds\n"
"s_add_u32 m0, s61, 3072\n"
"s_waitcnt lgkmcnt(6)\n"
"v_mfma_f32_16x16x32_bf16 v[76:79], a[236:239], a[240:243], v[76:79]\n"
"ds_read_b64_tr_b16 a[228:229], v237 offset:256\n"
"v_mfma_f32_16x16x32_bf16 v[80:83], a[236:239], a[244:247], v[80:83]\n"
"ds_read_b64_tr_b16 a[230:231], v237 offset:6400\n"
"v_mfma_f32_16x16x32_bf16 v[84:87], a[236:239], a[248:251], v[84:87]\n"
"s_waitcnt lgkmcnt(6)\n"
"v_mfma_f32_16x16x32_bf16 v[88:91], a[216:219], a[240:243], v[88:91]\n"
"ds_read_b64_tr_b16 a[232:233], v238 offset:256\n"
"v_mfma_f32_16x16x32_bf16 v[92:95], a[216:219], a[244:247], v[92:95]\n"
"ds_read_b64_tr_b16 a[234:235], v238 offset:6400\n"
"v_mfma_f32_16x16x32_bf16 v[96:99], a[216:219], a[248:251], v[96:99]\n"
"s_add_u32 s65, s65, 12288\n"
"buffer_load_dwordx4 %[dKV0], %[rV], s68 offen offset:0 nt lds\n"
"s_waitcnt lgkmcnt(6)\n"
"v_mfma_f32_16x16x32_bf16 v[100:103], a[220:223], a[240:243], v[100:103]\n"
"ds_read_b64_tr_b16 a[236:237], v239 offset:256\n"
"v_mfma_f32_16x16x32_bf16 v[104:107], a[220:223], a[244:247], v[104:107]\n"
"ds_read_b64_tr_b16 a[238:239], v239 offset:6400\n"
"v_mfma_f32_16x16x32_bf16 v[108:111], a[220:223], a[248:251], v[108:111]\n"
"buffer_load_dwordx4 %[dKV1], %[rV], s68 offen offset:1024 nt lds\n"
"s_waitcnt lgkmcnt(6)\n"
"v_mfma_f32_16x16x32_bf16 v[112:115], a[224:227], a[240:243], v[112:115]\n"
"s_add_u32 s61, %[m0b], s60\n"
"s_add_u32 s60, s60, 12288\n"
"s_cmp_eq_u32 s60, 61440\n"
"s_cselect_b32 s60, 0, s60\n"
"v_add_u32 v236, s60, %[rdV]\n"
"v_mfma_f32_16x16x32_bf16 v[116:119], a[224:227], a[244:247], v[116:119]\n"
"v_xor_b32 v237, 32, v236\n"
"v_xor_b32 v238, 64, v236\n"
"v_xor_b32 v239, 96, v236\n"
"v_mfma_f32_16x16x32_bf16 v[120:123], a[224:227], a[248:251], v[120:123]\n"
"s_waitcnt vmcnt(17) lgkmcnt(0)\n"
"s_barrier\n"
"ds_read_b64_tr_b16 a[216:217], v236 offset:0\n"
"ds_read_b64_tr_b16 a[218:219], v236 offset:6144\n"
"v_mfma_f32_16x16x32_bf16 v[124:127], a[228:231], a[240:243], v[124:127]\n"
"ds_read_b64_tr_b16 a[220:221], v237 offset:0\n"
"v_mfma_f32_16x16x32_bf16 v[128:131], a[228:231], a[244:247], v[128:131]\n"
"ds_read_b64_tr_b16 a[222:223], v237 offset:6144\n"
"v_mfma_f32_16x16x32_bf16 v[132:135], a[228:231], a[248:251], v[132:135]\n"
"v_mfma_f32_16x16x32_bf16 v[136:139], a[232:235], a[240:243], v[136:139]\n"
"ds_read_b64_tr_b16 a[224:225], v238 offset:0\n"
"v_mfma_f32_16x16x32_bf16 v[140:143], a[232:235], a[244:247], v[140:143]\n"
"ds_read_b64_tr_b16 a[226:227], v238 offset:6144\n"
"v_mfma_f32_16x16x32_bf16 v[144:147], a[232:235], a[248:251], v[144:147]\n"
"buffer_load_dwordx4 %[dKV2], %[rV], s68 offen offset:2048 nt lds\n"
"v_mfma_f32_16x16x32_bf16 v[148:151], a[236:239], a[240:243], v[148:151]\n"
"ds_read_b64_tr_b16 a[228:229], v239 offset:0\n"
"v_mfma_f32_16x16x32_bf16 v[152:155], a[236:239], a[244:247], v[152:155]\n"
"ds_read_b64_tr_b16 a[230:231], v239 offset:6144\n"
"v_mfma_f32_16x16x32_bf16 v[156:159], a[236:239], a[248:251], v[156:159]\n"
"s_waitcnt lgkmcnt(6)\n"
"v_mfma_f32_16x16x32_bf16 a[0:3], a[216:219], v[184:187], a[0:3]\n"
"ds_read_b64_tr_b16 a[232:233], v236 offset:128\n"
"v_mfma_f32_16x16x32_bf16 a[4:7], a[216:219], v[188:191], a[4:7]\n"
"ds_read_b64_tr_b16 a[234:235], v236 offset:6272\n"
"v_mfma_f32_16x16x32_bf16 a[8:11], a[216:219], v[192:195], a[8:11]\n"
"s_mov_b32 m0, s61\n"
"s_add_u32 s68, s64, 3072\n"
"buffer_load_dwordx4 %[dKV0], %[rK], s64 offen offset:0 nt lds\n"
"s_waitcnt lgkmcnt(6)\n"
"v_mfma_f32_16x16x32_bf16 a[12:15], a[220:223], v[184:187], a[12:15]\n"
"ds_read_b64_tr_b16 a[236:237], v237 offset:128\n"
"v_mfma_f32_16x16x32_bf16 a[16:19], a[220:223], v[188:191], a[16:19]\n"
"ds_read_b64_tr_b16 a[238:239], v237 offset:6272\n"
"v_mfma_f32_16x16x32_bf16 a[20:23], a[220:223], v[192:195], a[20:23]\n"
"s_waitcnt lgkmcnt(6)\n"
"v_mfma_f32_16x16x32_bf16 a[24:27], a[224:227], v[184:187], a[24:27]\n"
"ds_read_b64_tr_b16 a[216:217], v238 offset:128\n"
"v_mfma_f32_16x16x32_bf16 a[28:31], a[224:227], v[188:191], a[28:31]\n"
"ds_read_b64_tr_b16 a[218:219], v238 offset:6272\n"
"v_mfma_f32_16x16x32_bf16 a[32:35], a[224:227], v[192:195], a[32:35]\n"
"buffer_load_dwordx4 %[dKV1], %[rK], s64 offen offset:1024 nt lds\n"
"s_waitcnt lgkmcnt(6)\n"
"v_mfma_f32_16x16x32_bf16 a[36:39], a[228:231], v[184:187], a[36:39]\n"
"ds_read_b64_tr_b16 a[220:221], v239 offset:128\n"
"v_mfma_f32_16x16x32_bf16 a[40:43], a[228:231], v[188:191], a[40:43]\n"
"ds_read_b64_tr_b16 a[222:223], v239 offset:6272\n"
"v_mfma_f32_16x16x32_bf16 a[44:47], a[228:231], v[192:195], a[44:47]\n"
"s_waitcnt lgkmcnt(6)\n"
"v_mfma_f32_16x16x32_bf16 a[48:51], a[232:235], v[184:187], a[48:51]\n"
"ds_read_b64_tr_b16 a[224:225], v236 offset:256\n"
"v_mfma_f32_16x16x32_bf16 a[52:55], a[232:235], v[188:191], a[52:55]\n"
"ds_read_b64_tr_b16 a[226:227], v236 offset:6400\n"
"v_mfma_f32_16x16x32_bf16 a[56:59], a[232:235], v[192:195], a[56:59]\n"
"buffer_load_dwordx4 %[dKV2], %[rK], s64 offen offset:2048 nt lds\n"
"s_add_u32 m0, s61, 3072\n"
"s_waitcnt lgkmcnt(6)\n"
"v_mfma_f32_16x16x32_bf16 a[60:63], a[236:239], v[184:187], a[60:63]\n"
"ds_read_b64_tr_b16 a[228:229], v237 offset:256\n"
"v_mfma_f32_16x16x32_bf16 a[64:67], a[236:239], v[188:191], a[64:67]\n"
"ds_read_b64_tr_b16 a[230:231], v237 offset:6400\n"
"v_mfma_f32_16x16x32_bf16 a[68:71], a[236:239], v[192:195], a[68:71]\n"
"s_waitcnt lgkmcnt(6)\n"
"v_mfma_f32_16x16x32_bf16 a[72:75], a[216:219], v[184:187], a[72:75]\n"
"ds_read_b64_tr_b16 a[232:233], v238 offset:256\n"
"v_mfma_f32_16x16x32_bf16 a[76:79], a[216:219], v[188:191], a[76:79]\n"
"ds_read_b64_tr_b16 a[234:235], v238 offset:6400\n"
"v_mfma_f32_16x16x32_bf16 a[80:83], a[216:219], v[192:195], a[80:83]\n"
"s_add_u32 s64, s64, 12288\n"
"buffer_load_dwordx4 %[dKV0], %[rK], s68 offen offset:0 nt lds\n"
"s_waitcnt lgkmcnt(6)\n"
"v_mfma_f32_16x16x32_bf16 a[84:87], a[220:223], v[184:187], a[84:87]\n"
"ds_read_b64_tr_b16 a[236:237], v239 offset:256\n"
"v_mfma_f32_16x16x32_bf16 a[88:91], a[220:223], v[188:191], a[88:91]\n"
"ds_read_b64_tr_b16 a[238:239], v239 offset:6400\n"
"v_mfma_f32_16x16x32_bf16 a[92:95], a[220:223], v[192:195], a[92:95]\n"
"buffer_load_dwordx4 %[dKV1], %[rK], s68 offen offset:1024 nt lds\n"
"s_waitcnt lgkmcnt(6)\n"
"v_mfma_f32_16x16x32_bf16 a[96:99], a[224:227], v[184:187], a[96:99]\n"
"s_add_u32 s61, %[m0b], s60\n"
"s_add_u32 s60, s60, 12288\n"
"s_cmp_eq_u32 s60, 61440\n"
"s_cselect_b32 s60, 0, s60\n"
"v_add_u32 v236, s60, %[rdK]\n"
"v_mfma_f32_16x16x32_bf16 a[100:103], a[224:227], v[188:191], a[100:103]\n"
"v_xor_b32 v237, 64, v236\n"
"v_mfma_f32_16x16x32_bf16 a[104:107], a[224:227], v[192:195], a[104:107]\n"
"s_waitcnt vmcnt(17) lgkmcnt(0)\n"
"s_barrier\n"
"ds_read_b128 a[216:219], v236 offset:0\n"
"v_mfma_f32_16x16x32_bf16 a[108:111], a[228:231], v[184:187], a[108:111]\n"
"ds_read_b128 a[220:223], v237 offset:0\n"
"v_mfma_f32_16x16x32_bf16 a[112:115], a[228:231], v[188:191], a[112:115]\n"
"v_mfma_f32_16x16x32_bf16 a[116:119], a[228:231], v[192:195], a[116:119]\n"
"v_mfma_f32_16x16x32_bf16 a[120:123], a[232:235], v[184:187], a[120:123]\n"
"ds_read_b128 a[224:227], v236 offset:128\n"
"v_mfma_f32_16x16x32_bf16 a[124:127], a[232:235], v[188:191], a[124:127]\n"
"v_mfma_f32_16x16x32_bf16 a[128:131], a[232:235], v[192:195], a[128:131]\n"
"buffer_load_dwordx4 %[dKV2], %[rK], s68 offen offset:2048 nt lds\n"
"v_mfma_f32_16x16x32_bf16 a[132:135], a[236:239], v[184:187], a[132:135]\n"
"ds_read_b128 a[228:231], v237 offset:128\n"
"v_mfma_f32_16x16x32_bf16 a[136:139], a[236:239], v[188:191], a[136:139]\n"
"v_mfma_f32_16x16x32_bf16 a[140:143], a[236:239], v[192:195], a[140:143]\n"
"v_mfma_f32_16x16x32_bf16 v[208:211], v[220:223], v[184:187], v[208:211]\n"
"v_mfma_f32_16x16x32_bf16 v[212:215], v[220:223], v[188:191], v[212:215]\n"
"v_mfma_f32_16x16x32_bf16 v[216:219], v[220:223], v[192:195], v[216:219]\n"
"s_mov_b32 s69, 5\n"
".p2align 3\n"
"Lb_%=:\n"
"s_waitcnt lgkmcnt(3)\n"
"v_mfma_f32_16x16x32_bf16 v[160:163], a[216:219], a[144:147], v[196:199]\n"
"ds_read_b128 a[232:235], v236 offset:256\n"
"v_mfma_f32_16x16x32_bf16 v[164:167], a[216:219], a[148:151], v[200:203]\n"
"v_mfma_f32_16x16x32_bf16 v[168:171], a[216:219], a[152:155], v[204:207]\n"
"s_mov_b32 m0, s61\n"
"s_mul_i32 s66, s56, 6144\n"
"s_add_u32 s66, s66, s57\n"
"s_add_u32 s68, s66, 196608\n"
"buffer_load_dwordx4 %[dW0], %[rWq], s66 offen offset:0 lds\n"
"s_waitcnt lgkmcnt(3)\n"
"v_mfma_f32_16x16x32_bf16 v[160:163], a[220:223], a[156:159], v[160:163]\n"
"ds_read_b128 a[236:239], v237 offset:256\n"
"v_mfma_f32_16x16x32_bf16 v[164:167], a[220:223], a[160:163], v[164:167]\n"
"v_mfma_f32_16x16x32_bf16 v[168:171], a[220:223], a[164:167], v[168:171]\n"
"buffer_load_dwordx4 %[dW1], %[rWq], s66 offen offset:1024 lds\n"
"s_waitcnt lgkmcnt(3)\n"
"v_mfma_f32_16x16x32_bf16 v[160:163], a[224:227], a[168:171], v[160:163]\n"
"ds_read_b128 a[216:219], v236 offset:6144\n"
"v_mfma_f32_16x16x32_bf16 v[164:167], a[224:227], a[172:175], v[164:167]\n"
"v_mfma_f32_16x16x32_bf16 v[168:171], a[224:227], a[176:179], v[168:171]\n"
"s_waitcnt lgkmcnt(3)\n"
"v_mfma_f32_16x16x32_bf16 v[160:163], a[228:231], a[180:183], v[160:163]\n"
"ds_read_b128 a[220:223], v237 offset:6144\n"
"v_mfma_f32_16x16x32_bf16 v[164:167], a[228:231], a[184:187], v[164:167]\n"
"v_mfma_f32_16x16x32_bf16 v[168:171], a[228:231], a[188:191], v[168:171]\n"
"buffer_load_dwordx4 %[dW2], %[rWq], s66 offen offset:2048 lds\n"
"s_add_u32 m0, s61, 3072\n"
"s_waitcnt lgkmcnt(3)\n"
"v_mfma_f32_16x16x32_bf16 v[160:163], a[232:235], a[192:195], v[160:163]\n"
"ds_read_b128 a[224:227], v236 offset:6272\n"
"v_mfma_f32_16x16x32_bf16 v[164:167], a[232:235], a[196:199], v[164:167]\n"
"v_mfma_f32_16x16x32_bf16 v[168:171], a[232:235], a[200:203], v[168:171]\n"
"s_add_u32 s66, s66, 393216\n"
"buffer_load_dwordx4 %[dW0], %[rWq], s68 offen offset:0 lds\n"
"s_waitcnt lgkmcnt(3)\n"
"v_mfma_f32_16x16x32_bf16 v[160:163], a[236:239], a[204:207], v[160:163]\n"
"ds_read_b128 a[228:231], v237 offset:6272\n"
"v_mfma_f32_16x16x32_bf16 v[164:167], a[236:239], a[208:211], v[164:167]\n"
"v_mfma_f32_16x16x32_bf16 v[168:171], a[236:239], a[212:215], v[168:171]\n"
"s_waitcnt lgkmcnt(3)\n"
"v_mfma_f32_16x16x32_bf16 v[172:175], a[216:219], a[144:147], v[196:199]\n"
"ds_read_b128 a[232:235], v236 offset:6400\n"
"v_mfma_f32_16x16x32_bf16 v[176:179], a[216:219], a[148:151], v[200:203]\n"
"v_mfma_f32_16x16x32_bf16 v[180:183], a[216:219], a[152:155], v[204:207]\n"
"buffer_load_dwordx4 %[dW1], %[rWq], s68 offen offset:1024 lds\n"
"s_waitcnt lgkmcnt(3)\n"
"v_mfma_f32_16x16x32_bf16 v[172:175], a[220:223], a[156:159], v[172:175]\n"
"ds_read_b128 a[236:239], v237 offset:6400\n"
"v_mfma_f32_16x16x32_bf16 v[176:179], a[220:223], a[160:163], v[176:179]\n"
"v_mfma_f32_16x16x32_bf16 v[180:183], a[220:223], a[164:167], v[180:183]\n"
"buffer_load_dwordx4 %[dW2], %[rWq], s68 offen offset:2048 lds\n"
"s_waitcnt lgkmcnt(3)\n"
"v_mfma_f32_16x16x32_bf16 v[172:175], a[224:227], a[168:171], v[172:175]\n"
"s_add_u32 s61, %[m0b], s60\n"
"s_add_u32 s60, s60, 12288\n"
"s_cmp_eq_u32 s60, 61440\n"
"s_cselect_b32 s60, 0, s60\n"
"v_add_u32 v236, s60, %[rdW]\n"
"v_mfma_f32_16x16x32_bf16 v[176:179], a[224:227], a[172:175], v[176:179]\n"
"v_xor_b32 v237, 8, v236\n"
"v_xor_b32 v238, 64, v236\n"
"v_xor_b32 v239, 72, v236\n"
"v_xor_b32 v240, 64, v240\n"
"v_mfma_f32_16x16x32_bf16 v[180:183], a[224:227], a[176:179], v[180:183]\n"
"s_waitcnt vmcnt(18) lgkmcnt(0)\n"
"s_barrier\n"
"ds_read_b64_tr_b16 a[216:217], v236 offset:0\n"
"ds_read_b64_tr_b16 a[218:219], v236 offset:6144\n"
"v_mfma_f32_16x16x32_bf16 v[172:175], a[228:231], a[180:183], v[172:175]\n"
"ds_read_b128 v[224:227], v240 offset:0\n"
"ds_read_b64_tr_b16 a[220:221], v237 offset:0\n"
"v_mfma_f32_16x16x32_bf16 v[176:179], a[228:231], a[184:187], v[176:179]\n"
"ds_read_b64_tr_b16 a[222:223], v237 offset:6144\n"
"ds_read_b128 v[228:231], v240 offset:2048\n"
"v_mfma_f32_16x16x32_bf16 v[180:183], a[228:231], a[188:191], v[180:183]\n"
"ds_read_b128 v[232:235], v240 offset:4096\n"
"v_mfma_f32_16x16x32_bf16 v[172:175], a[232:235], a[192:195], v[172:175]\n"
"ds_read_b64_tr_b16 a[224:225], v238 offset:0\n"
"v_mfma_f32_16x16x32_bf16 v[176:179], a[232:235], a[196:199], v[176:179]\n"
"ds_read_b64_tr_b16 a[226:227], v238 offset:6144\n"
"v_mfma_f32_16x16x32_bf16 v[180:183], a[232:235], a[200:203], v[180:183]\n"
"s_add_u32 m0, %[m0x], s63\n"
"s_add_u32 s67, %[x0], s56\n"
"s_add_u32 s75, s67, 9216\n"
"buffer_load_dwordx4 %[dX], %[rX], s67 offen offset:0 lds\n"
"v_mfma_f32_16x16x32_bf16 v[172:175], a[236:239], a[204:207], v[172:175]\n"
"ds_read_b64_tr_b16 a[228:229], v239 offset:0\n"
"v_mfma_f32_16x16x32_bf16 v[176:179], a[236:239], a[208:211], v[176:179]\n"
"ds_read_b64_tr_b16 a[230:231], v239 offset:6144\n"
"v_mfma_f32_16x16x32_bf16 v[180:183], a[236:239], a[212:215], v[180:183]\n"
"buffer_load_dwordx4 %[dX], %[rX], s75 offen offset:1024 lds\n"
"s_add_u32 s75, s67, 18432\n"
"s_waitcnt lgkmcnt(8)\n"
"v_mfma_f32_16x16x32_bf16 v[16:19], a[216:219], v[224:227], v[16:19]\n"
"ds_read_b64_tr_b16 a[232:233], v236 offset:128\n"
"s_waitcnt lgkmcnt(6)\n"
"v_mfma_f32_16x16x32_bf16 v[20:23], a[216:219], v[228:231], v[20:23]\n"
"ds_read_b64_tr_b16 a[234:235], v236 offset:6272\n"
"s_waitcnt lgkmcnt(6)\n"
"v_mfma_f32_16x16x32_bf16 v[24:27], a[216:219], v[232:235], v[24:27]\n"
"buffer_load_dwordx4 %[dX], %[rX], s75 offen offset:2048 lds\n"
"s_add_u32 s56, s56, 128\n"
"s_cmp_eq_u32 s56, 1280\n"
"s_cselect_b32 s56, 0, s56\n"
"s_add_u32 s63, s63, 12288\n"
"s_cmp_eq_u32 s63, 36864\n"
"s_cselect_b32 s63, 0, s63\n"
"v_mfma_f32_16x16x32_bf16 v[28:31], a[220:223], v[224:227], v[28:31]\n"
"ds_read_b64_tr_b16 a[236:237], v237 offset:128\n"
"v_mfma_f32_16x16x32_bf16 v[32:35], a[220:223], v[228:231], v[32:35]\n"
"ds_read_b64_tr_b16 a[238:239], v237 offset:6272\n"
"v_max3_f32 v242, v160, v161, v162\n"
"v_max3_f32 v242, v242, v163, v164\n"
"v_mfma_f32_16x16x32_bf16 v[36:39], a[220:223], v[232:235], v[36:39]\n"
"s_mov_b32 m0, s61\n"
"s_add_u32 s68, s65, 3072\n"
"buffer_load_dwordx4 %[dKV0], %[rV], s65 offen offset:0 nt lds\n"
"v_max3_f32 v242, v242, v165, v166\n"
"s_waitcnt lgkmcnt(6)\n"
"v_mfma_f32_16x16x32_bf16 v[40:43], a[224:227], v[224:227], v[40:43]\n"
"ds_read_b64_tr_b16 a[216:217], v238 offset:128\n"
"v_max3_f32 v242, v242, v167, v168\n"
"v_max3_f32 v242, v242, v169, v170\n"
"v_max_f32 v242, v242, v171\n"
"v_mfma_f32_16x16x32_bf16 v[44:47], a[224:227], v[228:231], v[44:47]\n"
"ds_read_b64_tr_b16 a[218:219], v238 offset:6272\n"
"v_max3_f32 v243, v172, v173, v174\n"
"v_max3_f32 v243, v243, v175, v176\n"
"v_max3_f32 v243, v243, v177, v178\n"
"v_mfma_f32_16x16x32_bf16 v[48:51], a[224:227], v[232:235], v[48:51]\n"
"v_max3_f32 v243, v243, v179, v180\n"
"v_max3_f32 v243, v243, v181, v182\n"
"v_max3_f32 v243, v243, v183, v242\n"
"v_cmp_lt_f32 vcc, s70, v243\n"
"s_waitcnt lgkmcnt(6)\n"
"v_mfma_f32_16x16x32_bf16 v[52:55], a[228:231], v[224:227], v[52:55]\n"
"ds_read_b64_tr_b16 a[220:221], v239 offset:128\n"
"s_nop 1\n"
"s_cbranch_vccz 1\n"
"s_call_b64 s[76:77], Lslow_%=\n"
"v_mfma_f32_16x16x32_bf16 v[56:59], a[228:231], v[228:231], v[56:59]\n"
"ds_read_b64_tr_b16 a[222:223], v239 offset:6272\n"
"v_exp_f32 v160, v160\n"
"v_mfma_f32_16x16x32_bf16 v[60:63], a[228:231], v[232:235], v[60:63]\n"
"buffer_load_dwordx4 %[dKV1], %[rV], s65 offen offset:1024 nt lds\n"
"v_exp_f32 v161, v161\n"
"s_waitcnt lgkmcnt(6)\n"
"v_mfma_f32_16x16x32_bf16 v[64:67], a[232:235], v[224:227], v[64:67]\n"
"ds_read_b64_tr_b16 a[224:225], v236 offset:256\n"
"v_exp_f32 v162, v162\n"
"v_exp_f32 v163, v163\n"
"v_mfma_f32_16x16x32_bf16 v[68:71], a[232:235], v[228:231], v[68:71]\n"
"ds_read_b64_tr_b16 a[226:227], v236 offset:6400\n"
"v_exp_f32 v172, v172\n"
"v_mfma_f32_16x16x32_bf16 v[72:75], a[232:235], v[232:235], v[72:75]\n"
"v_exp_f32 v173, v173\n"
"v_exp_f32 v174, v174\n"
"s_waitcnt lgkmcnt(6)\n"
"v_mfma_f32_16x16x32_bf16 v[76:79], a[236:239], v[224:227], v[76:79]\n"
"ds_read_b64_tr_b16 a[228:229], v237 offset:256\n"
"v_exp_f32 v175, v175\n"
"v_cvt_pk_bf16_f32 v184, v160, v161\n"
"v_mfma_f32_16x16x32_bf16 v[80:83], a[236:239], v[228:231], v[80:83]\n"
"ds_read_b64_tr_b16 a[230:231], v237 offset:6400\n"
"v_cvt_pk_bf16_f32 v185, v162, v163\n"
"v_exp_f32 v164, v164\n"
"v_mfma_f32_16x16x32_bf16 v[84:87], a[236:239], v[232:235], v[84:87]\n"
"buffer_load_dwordx4 %[dKV2], %[rV], s65 offen offset:2048 nt lds\n"
"s_add_u32 m0, s61, 3072\n"
"s_waitcnt lgkmcnt(6)\n"
"v_mfma_f32_16x16x32_bf16 v[88:91], a[216:219], v[224:227], v[88:91]\n"
"ds_read_b64_tr_b16 a[232:233], v238 offset:256\n"
"v_exp_f32 v165, v165\n"
"v_mfma_f32_16x16x32_bf16 v[92:95], a[216:219], v[228:231], v[92:95]\n"
"ds_read_b64_tr_b16 a[234:235], v238 offset:6400\n"
"v_exp_f32 v166, v166\n"
"v_exp_f32 v167, v167\n"
"v_mfma_f32_16x16x32_bf16 v[96:99], a[216:219], v[232:235], v[96:99]\n"
"s_add_u32 s65, s65, 12288\n"
"buffer_load_dwordx4 %[dKV0], %[rV], s68 offen offset:0 nt lds\n"
"s_waitcnt lgkmcnt(6)\n"
"v_mfma_f32_16x16x32_bf16 v[100:103], a[220:223], v[224:227], v[100:103]\n"
"ds_read_b64_tr_b16 a[236:237], v239 offset:256\n"
"v_cvt_pk_bf16_f32 v186, v172, v173\n"
"v_cvt_pk_bf16_f32 v187, v174, v175\n"
"v_exp_f32 v176, v176\n"
"v_mfma_f32_16x16x32_bf16 v[104:107], a[220:223], v[228:231], v[104:107]\n"
"ds_read_b64_tr_b16 a[238:239], v239 offset:6400\n"
"v_exp_f32 v177, v177\n"
"v_mfma_f32_16x16x32_bf16 v[108:111], a[220:223], v[232:235], v[108:111]\n"
"buffer_load_dwordx4 %[dKV1], %[rV], s68 offen offset:1024 nt lds\n"
"v_exp_f32 v178, v178\n"
"s_waitcnt lgkmcnt(6)\n"
"v_mfma_f32_16x16x32_bf16 v[112:115], a[224:227], v[224:227], v[112:115]\n"
"s_add_u32 s61, %[m0b], s60\n"
"s_add_u32 s60, s60, 12288\n"
"s_cmp_eq_u32 s60, 61440\n"
"s_cselect_b32 s60, 0, s60\n"
"v_add_u32 v236, s60, %[rdV]\n"
"v_mfma_f32_16x16x32_bf16 v[116:119], a[224:227], v[228:231], v[116:119]\n"
"v_xor_b32 v237, 32, v236\n"
"v_xor_b32 v238, 64, v236\n"
"v_xor_b32 v239, 96, v236\n"
"v_mfma_f32_16x16x32_bf16 v[120:123], a[224:227], v[232:235], v[120:123]\n"
"s_waitcnt vmcnt(20) lgkmcnt(0)\n"
"s_barrier\n"
"ds_read_b64_tr_b16 a[216:217], v236 offset:0\n"
"ds_read_b64_tr_b16 a[218:219], v236 offset:6144\n"
"v_mfma_f32_16x16x32_bf16 v[124:127], a[228:231], v[224:227], v[124:127]\n"
"ds_read_b64_tr_b16 a[220:221], v237 offset:0\n"
"v_exp_f32 v179, v179\n"
"v_cvt_pk_bf16_f32 v188, v164, v165\n"
"v_mfma_f32_16x16x32_bf16 v[128:131], a[228:231], v[228:231], v[128:131]\n"
"ds_read_b64_tr_b16 a[222:223], v237 offset:6144\n"
"v_cvt_pk_bf16_f32 v189, v166, v167\n"
"v_exp_f32 v168, v168\n"
"v_mfma_f32_16x16x32_bf16 v[132:135], a[228:231], v[232:235], v[132:135]\n"
"v_exp_f32 v169, v169\n"
"v_exp_f32 v170, v170\n"
"v_mfma_f32_16x16x32_bf16 v[136:139], a[232:235], v[224:227], v[136:139]\n"
"ds_read_b64_tr_b16 a[224:225], v238 offset:0\n"
"v_exp_f32 v171, v171\n"
"v_mfma_f32_16x16x32_bf16 v[140:143], a[232:235], v[228:231], v[140:143]\n"
"ds_read_b64_tr_b16 a[226:227], v238 offset:6144\n"
"v_cvt_pk_bf16_f32 v190, v176, v177\n"
"v_cvt_pk_bf16_f32 v191, v178, v179\n"
"v_mfma_f32_16x16x32_bf16 v[144:147], a[232:235], v[232:235], v[144:147]\n"
"buffer_load_dwordx4 %[dKV2], %[rV], s68 offen offset:2048 nt lds\n"
"v_exp_f32 v180, v180\n"
"v_mfma_f32_16x16x32_bf16 v[148:151], a[236:239], v[224:227], v[148:151]\n"
"ds_read_b64_tr_b16 a[228:229], v239 offset:0\n"
"v_exp_f32 v181, v181\n"
"v_exp_f32 v182, v182\n"
"v_mfma_f32_16x16x32_bf16 v[152:155], a[236:239], v[228:231], v[152:155]\n"
"ds_read_b64_tr_b16 a[230:231], v239 offset:6144\n"
"v_exp_f32 v183, v183\n"
"v_mfma_f32_16x16x32_bf16 v[156:159], a[236:239], v[232:235], v[156:159]\n"
"v_cvt_pk_bf16_f32 v192, v168, v169\n"
"v_cvt_pk_bf16_f32 v193, v170, v171\n"
"v_cvt_pk_bf16_f32 v194, v180, v181\n"
"v_cvt_pk_bf16_f32 v195, v182, v183\n"
"s_waitcnt lgkmcnt(6)\n"
"v_mfma_f32_16x16x32_bf16 a[0:3], a[216:219], v[184:187], a[0:3]\n"
"ds_read_b64_tr_b16 a[232:233], v236 offset:128\n"
"v_mfma_f32_16x16x32_bf16 a[4:7], a[216:219], v[188:191], a[4:7]\n"
"ds_read_b64_tr_b16 a[234:235], v236 offset:6272\n"
"v_mfma_f32_16x16x32_bf16 a[8:11], a[216:219], v[192:195], a[8:11]\n"
"s_mov_b32 m0, s61\n"
"s_add_u32 s68, s64, 3072\n"
"buffer_load_dwordx4 %[dKV0], %[rK], s64 offen offset:0 nt lds\n"
"s_waitcnt lgkmcnt(6)\n"
"v_mfma_f32_16x16x32_bf16 a[12:15], a[220:223], v[184:187], a[12:15]\n"
"ds_read_b64_tr_b16 a[236:237], v237 offset:128\n"
"v_mfma_f32_16x16x32_bf16 a[16:19], a[220:223], v[188:191], a[16:19]\n"
"ds_read_b64_tr_b16 a[238:239], v237 offset:6272\n"
"v_mfma_f32_16x16x32_bf16 a[20:23], a[220:223], v[192:195], a[20:23]\n"
"s_waitcnt lgkmcnt(6)\n"
"v_mfma_f32_16x16x32_bf16 a[24:27], a[224:227], v[184:187], a[24:27]\n"
"ds_read_b64_tr_b16 a[216:217], v238 offset:128\n"
"v_mfma_f32_16x16x32_bf16 a[28:31], a[224:227], v[188:191], a[28:31]\n"
"ds_read_b64_tr_b16 a[218:219], v238 offset:6272\n"
"v_mfma_f32_16x16x32_bf16 a[32:35], a[224:227], v[192:195], a[32:35]\n"
"buffer_load_dwordx4 %[dKV1], %[rK], s64 offen offset:1024 nt lds\n"
"s_waitcnt lgkmcnt(6)\n"
"v_mfma_f32_16x16x32_bf16 a[36:39], a[228:231], v[184:187], a[36:39]\n"
"ds_read_b64_tr_b16 a[220:221], v239 offset:128\n"
"v_mfma_f32_16x16x32_bf16 a[40:43], a[228:231], v[188:191], a[40:43]\n"
"ds_read_b64_tr_b16 a[222:223], v239 offset:6272\n"
"v_mfma_f32_16x16x32_bf16 a[44:47], a[228:231], v[192:195], a[44:47]\n"
"s_waitcnt lgkmcnt(6)\n"
"v_mfma_f32_16x16x32_bf16 a[48:51], a[232:235], v[184:187], a[48:51]\n"
"ds_read_b64_tr_b16 a[224:225], v236 offset:256\n"
"v_mfma_f32_16x16x32_bf16 a[52:55], a[232:235], v[188:191], a[52:55]\n"
"ds_read_b64_tr_b16 a[226:227], v236 offset:6400\n"
"v_mfma_f32_16x16x32_bf16 a[56:59], a[232:235], v[192:195], a[56:59]\n"
"buffer_load_dwordx4 %[dKV2], %[rK], s64 offen offset:2048 nt lds\n"
"s_add_u32 m0, s61, 3072\n"
"s_waitcnt lgkmcnt(6)\n"
"v_mfma_f32_16x16x32_bf16 a[60:63], a[236:239], v[184:187], a[60:63]\n"
"ds_read_b64_tr_b16 a[228:229], v237 offset:256\n"
"v_mfma_f32_16x16x32_bf16 a[64:67], a[236:239], v[188:191], a[64:67]\n"
"ds_read_b64_tr_b16 a[230:231], v237 offset:6400\n"
"v_mfma_f32_16x16x32_bf16 a[68:71], a[236:239], v[192:195], a[68:71]\n"
"s_waitcnt lgkmcnt(6)\n"
"v_mfma_f32_16x16x32_bf16 a[72:75], a[216:219], v[184:187], a[72:75]\n"
"ds_read_b64_tr_b16 a[232:233], v238 offset:256\n"
"v_mfma_f32_16x16x32_bf16 a[76:79], a[216:219], v[188:191], a[76:79]\n"
"ds_read_b64_tr_b16 a[234:235], v238 offset:6400\n"
"v_mfma_f32_16x16x32_bf16 a[80:83], a[216:219], v[192:195], a[80:83]\n"
"s_add_u32 s64, s64, 12288\n"
"buffer_load_dwordx4 %[dKV0], %[rK], s68 offen offset:0 nt lds\n"
"s_waitcnt lgkmcnt(6)\n"
"v_mfma_f32_16x16x32_bf16 a[84:87], a[220:223], v[184:187], a[84:87]\n"
"ds_read_b64_tr_b16 a[236:237], v239 offset:256\n"
"v_mfma_f32_16x16x32_bf16 a[88:91], a[220:223], v[188:191], a[88:91]\n"
"ds_read_b64_tr_b16 a[238:239], v239 offset:6400\n"
"v_mfma_f32_16x16x32_bf16 a[92:95], a[220:223], v[192:195], a[92:95]\n"
"buffer_load_dwordx4 %[dKV1], %[rK], s68 offen offset:1024 nt lds\n"
"s_waitcnt lgkmcnt(6)\n"
"v_mfma_f32_16x16x32_bf16 a[96:99], a[224:227], v[184:187], a[96:99]\n"
"s_add_u32 s61, %[m0b], s60\n"
"s_add_u32 s60, s60, 12288\n"
"s_cmp_eq_u32 s60, 61440\n"
"s_cselect_b32 s60, 0, s60\n"
"v_add_u32 v236, s60, %[rdK]\n"
"v_mfma_f32_16x16x32_bf16 a[100:103], a[224:227], v[188:191], a[100:103]\n"
"v_xor_b32 v237, 64, v236\n"
"v_mfma_f32_16x16x32_bf16 a[104:107], a[224:227], v[192:195], a[104:107]\n"
"s_waitcnt vmcnt(20) lgkmcnt(0)\n"
"s_barrier\n"
"ds_read_b128 a[216:219], v236 offset:0\n"
"v_mfma_f32_16x16x32_bf16 a[108:111], a[228:231], v[184:187], a[108:111]\n"
"ds_read_b128 a[220:223], v237 offset:0\n"
"v_mfma_f32_16x16x32_bf16 a[112:115], a[228:231], v[188:191], a[112:115]\n"
"v_mfma_f32_16x16x32_bf16 a[116:119], a[228:231], v[192:195], a[116:119]\n"
"v_mfma_f32_16x16x32_bf16 a[120:123], a[232:235], v[184:187], a[120:123]\n"
"ds_read_b128 a[224:227], v236 offset:128\n"
"v_mfma_f32_16x16x32_bf16 a[124:127], a[232:235], v[188:191], a[124:127]\n"
"v_mfma_f32_16x16x32_bf16 a[128:131], a[232:235], v[192:195], a[128:131]\n"
"buffer_load_dwordx4 %[dKV2], %[rK], s68 offen offset:2048 nt lds\n"
"v_mfma_f32_16x16x32_bf16 a[132:135], a[236:239], v[184:187], a[132:135]\n"
"ds_read_b128 a[228:231], v237 offset:128\n"
"v_mfma_f32_16x16x32_bf16 a[136:139], a[236:239], v[188:191], a[136:139]\n"
"v_mfma_f32_16x16x32_bf16 a[140:143], a[236:239], v[192:195], a[140:143]\n"
"v_mfma_f32_16x16x32_bf16 v[208:211], v[220:223], v[184:187], v[208:211]\n"
"v_mfma_f32_16x16x32_bf16 v[212:215], v[220:223], v[188:191], v[212:215]\n"
"v_mfma_f32_16x16x32_bf16 v[216:219], v[220:223], v[192:195], v[216:219]\n"
"s_waitcnt lgkmcnt(3)\n"
"v_mfma_f32_16x16x32_bf16 v[160:163], a[216:219], a[144:147], v[196:199]\n"
"ds_read_b128 a[232:235], v236 offset:256\n"
"v_mfma_f32_16x16x32_bf16 v[164:167], a[216:219], a[148:151], v[200:203]\n"
"v_mfma_f32_16x16x32_bf16 v[168:171], a[216:219], a[152:155], v[204:207]\n"
"s_mov_b32 m0, s61\n"
"s_add_u32 s68, s66, 196608\n"
"buffer_load_dwordx4 %[dW0], %[rWq], s66 offen offset:0 lds\n"
"s_waitcnt lgkmcnt(3)\n"
"v_mfma_f32_16x16x32_bf16 v[160:163], a[220:223], a[156:159], v[160:163]\n"
"ds_read_b128 a[236:239], v237 offset:256\n"
"v_mfma_f32_16x16x32_bf16 v[164:167], a[220:223], a[160:163], v[164:167]\n"
"v_mfma_f32_16x16x32_bf16 v[168:171], a[220:223], a[164:167], v[168:171]\n"
"s_waitcnt lgkmcnt(3)\n"
"v_mfma_f32_16x16x32_bf16 v[160:163], a[224:227], a[168:171], v[160:163]\n"
"ds_read_b128 a[216:219], v236 offset:6144\n"
"v_mfma_f32_16x16x32_bf16 v[164:167], a[224:227], a[172:175], v[164:167]\n"
"v_mfma_f32_16x16x32_bf16 v[168:171], a[224:227], a[176:179], v[168:171]\n"
"buffer_load_dwordx4 %[dW1], %[rWq], s66 offen offset:1024 lds\n"
"s_waitcnt lgkmcnt(3)\n"
"v_mfma_f32_16x16x32_bf16 v[160:163], a[228:231], a[180:183], v[160:163]\n"
"ds_read_b128 a[220:223], v237 offset:6144\n"
"v_mfma_f32_16x16x32_bf16 v[164:167], a[228:231], a[184:187], v[164:167]\n"
"v_mfma_f32_16x16x32_bf16 v[168:171], a[228:231], a[188:191], v[168:171]\n"
"s_waitcnt lgkmcnt(3)\n"
"v_mfma_f32_16x16x32_bf16 v[160:163], a[232:235], a[192:195], v[160:163]\n"
"ds_read_b128 a[224:227], v236 offset:6272\n"
"v_mfma_f32_16x16x32_bf16 v[164:167], a[232:235], a[196:199], v[164:167]\n"
"v_mfma_f32_16x16x32_bf16 v[168:171], a[232:235], a[200:203], v[168:171]\n"
"buffer_load_dwordx4 %[dW2], %[rWq], s66 offen offset:2048 lds\n"
"s_add_u32 m0, s61, 3072\n"
"s_waitcnt lgkmcnt(3)\n"
"v_mfma_f32_16x16x32_bf16 v[160:163], a[236:239], a[204:207], v[160:163]\n"
"ds_read_b128 a[228:231], v237 offset:6272\n"
"v_mfma_f32_16x16x32_bf16 v[164:167], a[236:239], a[208:211], v[164:167]\n"
"v_mfma_f32_16x16x32_bf16 v[168:171], a[236:239], a[212:215], v[168:171]\n"
"s_waitcnt lgkmcnt(3)\n"
"v_mfma_f32_16x16x32_bf16 v[172:175], a[216:219], a[144:147], v[196:199]\n"
"ds_read_b128 a[232:235], v236 offset:6400\n"
"v_mfma_f32_16x16x32_bf16 v[176:179], a[216:219], a[148:151], v[200:203]\n"
"v_mfma_f32_16x16x32_bf16 v[180:183], a[216:219], a[152:155], v[204:207]\n"
"s_add_u32 s66, s66, 393216\n"
"buffer_load_dwordx4 %[dW0], %[rWq], s68 offen offset:0 lds\n"
"s_waitcnt lgkmcnt(3)\n"
"v_mfma_f32_16x16x32_bf16 v[172:175], a[220:223], a[156:159], v[172:175]\n"
"ds_read_b128 a[236:239], v237 offset:6400\n"
"v_mfma_f32_16x16x32_bf16 v[176:179], a[220:223], a[160:163], v[176:179]\n"
"v_mfma_f32_16x16x32_bf16 v[180:183], a[220:223], a[164:167], v[180:183]\n"
"buffer_load_dwordx4 %[dW1], %[rWq], s68 offen offset:1024 lds\n"
"s_waitcnt lgkmcnt(3)\n"
"v_mfma_f32_16x16x32_bf16 v[172:175], a[224:227], a[168:171], v[172:175]\n"
"s_add_u32 s61, %[m0b], s60\n"
"s_add_u32 s60, s60, 12288\n"
"s_cmp_eq_u32 s60, 61440\n"
"s_cselect_b32 s60, 0, s60\n"
"v_add_u32 v236, s60, %[rdW]\n"
"v_mfma_f32_16x16x32_bf16 v[176:179], a[224:227], a[172:175], v[176:179]\n"
"v_xor_b32 v237, 8, v236\n"
"v_xor_b32 v238, 64, v236\n"
"v_xor_b32 v239, 72, v236\n"
"v_add_u32 v240, s62, %[rdX]\n"
"s_add_u32 s62, s62, 12288\n"
"s_cmp_eq_u32 s62, 36864\n"
"s_cselect_b32 s62, 0, s62\n"
"v_mfma_f32_16x16x32_bf16 v[180:183], a[224:227], a[176:179], v[180:183]\n"
"s_waitcnt vmcnt(17) lgkmcnt(0)\n"
"s_barrier\n"
"ds_read_b64_tr_b16 a[216:217], v236 offset:0\n"
"ds_read_b64_tr_b16 a[218:219], v236 offset:6144\n"
"v_mfma_f32_16x16x32_bf16 v[172:175], a[228:231], a[180:183], v[172:175]\n"
"ds_read_b128 a[240:243], v240 offset:0\n"
"ds_read_b64_tr_b16 a[220:221], v237 offset:0\n"
"v_mfma_f32_16x16x32_bf16 v[176:179], a[228:231], a[184:187], v[176:179]\n"
"ds_read_b64_tr_b16 a[222:223], v237 offset:6144\n"
"ds_read_b128 a[244:247], v240 offset:2048\n"
"v_mfma_f32_16x16x32_bf16 v[180:183], a[228:231], a[188:191], v[180:183]\n"
"ds_read_b128 a[248:251], v240 offset:4096\n"
"v_mfma_f32_16x16x32_bf16 v[172:175], a[232:235], a[192:195], v[172:175]\n"
"ds_read_b64_tr_b16 a[224:225], v238 offset:0\n"
"v_mfma_f32_16x16x32_bf16 v[176:179], a[232:235], a[196:199], v[176:179]\n"
"ds_read_b64_tr_b16 a[226:227], v238 offset:6144\n"
"v_mfma_f32_16x16x32_bf16 v[180:183], a[232:235], a[200:203], v[180:183]\n"
"buffer_load_dwordx4 %[dW2], %[rWq], s68 offen offset:2048 lds\n"
"v_mfma_f32_16x16x32_bf16 v[172:175], a[236:239], a[204:207], v[172:175]\n"
"ds_read_b64_tr_b16 a[228:229], v239 offset:0\n"
"v_mfma_f32_16x16x32_bf16 v[176:179], a[236:239], a[208:211], v[176:179]\n"
"ds_read_b64_tr_b16 a[230:231], v239 offset:6144\n"
"v_mfma_f32_16x16x32_bf16 v[180:183], a[236:239], a[212:215], v[180:183]\n"
"s_waitcnt lgkmcnt(8)\n"
"v_mfma_f32_16x16x32_bf16 v[16:19], a[216:219], a[240:243], v[16:19]\n"
"ds_read_b64_tr_b16 a[232:233], v236 offset:128\n"
"s_waitcnt lgkmcnt(6)\n"
"v_mfma_f32_16x16x32_bf16 v[20:23], a[216:219], a[244:247], v[20:23]\n"
"ds_read_b64_tr_b16 a[234:235], v236 offset:6272\n"
"s_waitcnt lgkmcnt(6)\n"
"v_mfma_f32_16x16x32_bf16 v[24:27], a[216:219], a[248:251], v[24:27]\n"
"s_mov_b32 m0, s61\n"
"s_add_u32 s68, s65, 3072\n"
"buffer_load_dwordx4 %[dKV0], %[rV], s65 offen offset:0 nt lds\n"
"v_mfma_f32_16x16x32_bf16 v[28:31], a[220:223], a[240:243], v[28:31]\n"
"ds_read_b64_tr_b16 a[236:237], v237 offset:128\n"
"v_max3_f32 v242, v160, v161, v162\n"
"v_max3_f32 v242, v242, v163, v164\n"
"v_mfma_f32_16x16x32_bf16 v[32:35], a[220:223], a[244:247], v[32:35]\n"
"ds_read_b64_tr_b16 a[238:239], v237 offset:6272\n"
"v_max3_f32 v242, v242, v165, v166\n"
"v_max3_f32 v242, v242, v167, v168\n"
"v_mfma_f32_16x16x32_bf16 v[36:39], a[220:223], a[248:251], v[36:39]\n"
"v_max3_f32 v242, v242, v169, v170\n"
"v_max_f32 v242, v242, v171\n"
"v_max3_f32 v243, v172, v173, v174\n"
"v_max3_f32 v243, v243, v175, v176\n"
"s_waitcnt lgkmcnt(6)\n"
"v_mfma_f32_16x16x32_bf16 v[40:43], a[224:227], a[240:243], v[40:43]\n"
"ds_read_b64_tr_b16 a[216:217], v238 offset:128\n"
"v_max3_f32 v243, v243, v177, v178\n"
"v_max3_f32 v243, v243, v179, v180\n"
"v_mfma_f32_16x16x32_bf16 v[44:47], a[224:227], a[244:247], v[44:47]\n"
"ds_read_b64_tr_b16 a[218:219], v238 offset:6272\n"
"v_max3_f32 v243, v243, v181, v182\n"
"v_max3_f32 v243, v243, v183, v242\n"
"v_mfma_f32_16x16x32_bf16 v[48:51], a[224:227], a[248:251], v[48:51]\n"
"buffer_load_dwordx4 %[dKV1], %[rV], s65 offen offset:1024 nt lds\n"
"v_cmp_lt_f32 vcc, s70, v243\n"
"s_waitcnt lgkmcnt(6)\n"
"v_mfma_f32_16x16x32_bf16 v[52:55], a[228:231], a[240:243], v[52:55]\n"
"ds_read_b64_tr_b16 a[220:221], v239 offset:128\n"
"s_nop 1\n"
"s_cbranch_vccz 1\n"
"s_call_b64 s[76:77], Lslow_%=\n"
"v_mfma_f32_16x16x32_bf16 v[56:59], a[228:231], a[244:247], v[56:59]\n"
"ds_read_b64_tr_b16 a[222:223], v239 offset:6272\n"
"v_exp_f32 v160, v160\n"
"v_mfma_f32_16x16x32_bf16 v[60:63], a[228:231], a[248:251], v[60:63]\n"
"v_exp_f32 v161, v161\n"
"v_exp_f32 v162, v162\n"
"s_waitcnt lgkmcnt(6)\n"
"v_mfma_f32_16x16x32_bf16 v[64:67], a[232:235], a[240:243], v[64:67]\n"
"ds_read_b64_tr_b16 a[224:225], v236 offset:256\n"
"v_exp_f32 v163, v163\n"
"v_exp_f32 v172, v172\n"
"v_mfma_f32_16x16x32_bf16 v[68:71], a[232:235], a[244:247], v[68:71]\n"
"ds_read_b64_tr_b16 a[226:227], v236 offset:6400\n"
"v_exp_f32 v173, v173\n"
"v_mfma_f32_16x16x32_bf16 v[72:75], a[232:235], a[248:251], v[72:75]\n"
"buffer_load_dwordx4 %[dKV2], %[rV], s65 offen offset:2048 nt lds\n"
"s_add_u32 m0, s61, 3072\n"
"s_waitcnt lgkmcnt(6)\n"
"v_mfma_f32_16x16x32_bf16 v[76:79], a[236:239], a[240:243], v[76:79]\n"
"ds_read_b64_tr_b16 a[228:229], v237 offset:256\n"
"v_exp_f32 v174, v174\n"
"v_exp_f32 v175, v175\n"
"v_mfma_f32_16x16x32_bf16 v[80:83], a[236:239], a[244:247], v[80:83]\n"
"ds_read_b64_tr_b16 a[230:231], v237 offset:6400\n"
"v_cvt_pk_bf16_f32 v184, v160, v161\n"
"v_cvt_pk_bf16_f32 v185, v162, v163\n"
"v_mfma_f32_16x16x32_bf16 v[84:87], a[236:239], a[248:251], v[84:87]\n"
"v_exp_f32 v164, v164\n"
"v_exp_f32 v165, v165\n"
"s_waitcnt lgkmcnt(6)\n"
"v_mfma_f32_16x16x32_bf16 v[88:91], a[216:219], a[240:243], v[88:91]\n"
"ds_read_b64_tr_b16 a[232:233], v238 offset:256\n"
"v_exp_f32 v166, v166\n"
"v_mfma_f32_16x16x32_bf16 v[92:95], a[216:219], a[244:247], v[92:95]\n"
"ds_read_b64_tr_b16 a[234:235], v238 offset:6400\n"
"v_exp_f32 v167, v167\n"
"v_cvt_pk_bf16_f32 v186, v172, v173\n"
"v_mfma_f32_16x16x32_bf16 v[96:99], a[216:219], a[248:251], v[96:99]\n"
"s_add_u32 s65, s65, 12288\n"
"buffer_load_dwordx4 %[dKV0], %[rV], s68 offen offset:0 nt lds\n"
"v_cvt_pk_bf16_f32 v187, v174, v175\n"
"s_waitcnt lgkmcnt(6)\n"
"v_mfma_f32_16x16x32_bf16 v[100:103], a[220:223], a[240:243], v[100:103]\n"
"ds_read_b64_tr_b16 a[236:237], v239 offset:256\n"
"v_exp_f32 v176, v176\n"
"v_mfma_f32_16x16x32_bf16 v[104:107], a[220:223], a[244:247], v[104:107]\n"
"ds_read_b64_tr_b16 a[238:239], v239 offset:6400\n"
"v_exp_f32 v177, v177\n"
"v_exp_f32 v178, v178\n"
"v_mfma_f32_16x16x32_bf16 v[108:111], a[220:223], a[248:251], v[108:111]\n"
"buffer_load_dwordx4 %[dKV1], %[rV], s68 offen offset:1024 nt lds\n"
"s_waitcnt lgkmcnt(6)\n"
"v_mfma_f32_16x16x32_bf16 v[112:115], a[224:227], a[240:243], v[112:115]\n"
"s_add_u32 s61, %[m0b], s60\n"
"s_add_u32 s60, s60, 12288\n"
"s_cmp_eq_u32 s60, 61440\n"
"s_cselect_b32 s60, 0, s60\n"
"v_add_u32 v236, s60, %[rdV]\n"
"v_mfma_f32_16x16x32_bf16 v[116:119], a[224:227], a[244:247], v[116:119]\n"
"v_xor_b32 v237, 32, v236\n"
"v_xor_b32 v238, 64, v236\n"
"v_xor_b32 v239, 96, v236\n"
"v_mfma_f32_16x16x32_bf16 v[120:123], a[224:227], a[248:251], v[120:123]\n"
"s_waitcnt vmcnt(17) lgkmcnt(0)\n"
"s_barrier\n"
"ds_read_b64_tr_b16 a[216:217], v236 offset:0\n"
"ds_read_b64_tr_b16 a[218:219], v236 offset:6144\n"
"v_mfma_f32_16x16x32_bf16 v[124:127], a[228:231], a[240:243], v[124:127]\n"
"ds_read_b64_tr_b16 a[220:221], v237 offset:0\n"
"v_exp_f32 v179, v179\n"
"v_cvt_pk_bf16_f32 v188, v164, v165\n"
"v_mfma_f32_16x16x32_bf16 v[128:131], a[228:231], a[244:247], v[128:131]\n"
"ds_read_b64_tr_b16 a[222:223], v237 offset:6144\n"
"v_cvt_pk_bf16_f32 v189, v166, v167\n"
"v_exp_f32 v168, v168\n"
"v_mfma_f32_16x16x32_bf16 v[132:135], a[228:231], a[248:251], v[132:135]\n"
"v_exp_f32 v169, v169\n"
"v_exp_f32 v170, v170\n"
"v_mfma_f32_16x16x32_bf16 v[136:139], a[232:235], a[240:243], v[136:139]\n"
"ds_read_b64_tr_b16 a[224:225], v238 offset:0\n"
"v_exp_f32 v171, v171\n"
"v_cvt_pk_bf16_f32 v190, v176, v177\n"
"v_mfma_f32_16x16x32_bf16 v[140:143], a[232:235], a[244:247], v[140:143]\n"
"ds_read_b64_tr_b16 a[226:227], v238 offset:6144\n"
"v_cvt_pk_bf16_f32 v191, v178, v179\n"
"v_exp_f32 v180, v180\n"
"v_mfma_f32_16x16x32_bf16 v[144:147], a[232:235], a[248:251], v[144:147]\n"
"buffer_load_dwordx4 %[dKV2], %[rV], s68 offen offset:2048 nt lds\n"
"v_mfma_f32_16x16x32_bf16 v[148:151], a[236:239], a[240:243], v[148:151]\n"
"ds_read_b64_tr_b16 a[228:229], v239 offset:0\n"
"v_exp_f32 v181, v181\n"
"v_exp_f32 v182, v182\n"
"v_mfma_f32_16x16x32_bf16 v[152:155], a[236:239], a[244:247], v[152:155]\n"
"ds_read_b64_tr_b16 a[230:231], v239 offset:6144\n"
"v_exp_f32 v183, v183\n"
"v_mfma_f32_16x16x32_bf16 v[156:159], a[236:239], a[248:251], v[156:159]\n"
"v_cvt_pk_bf16_f32 v192, v168, v169\n"
"v_cvt_pk_bf16_f32 v193, v170, v171\n"
"v_cvt_pk_bf16_f32 v194, v180, v181\n"
"v_cvt_pk_bf16_f32 v195, v182, v183\n"
"s_waitcnt lgkmcnt(6)\n"
"v_mfma_f32_16x16x32_bf16 a[0:3], a[216:219], v[184:187], a[0:3]\n"
"ds_read_b64_tr_b16 a[232:233], v236 offset:128\n"
"v_mfma_f32_16x16x32_bf16 a[4:7], a[216:219], v[188:191], a[4:7]\n"
"ds_read_b64_tr_b16 a[234:235], v236 offset:6272\n"
"v_mfma_f32_16x16x32_bf16 a[8:11], a[216:219], v[192:195], a[8:11]\n"
"s_mov_b32 m0, s61\n"
"s_add_u32 s68, s64, 3072\n"
"buffer_load_dwordx4 %[dKV0], %[rK], s64 offen offset:0 nt lds\n"
"s_waitcnt lgkmcnt(6)\n"
"v_mfma_f32_16x16x32_bf16 a[12:15], a[220:223], v[184:187], a[12:15]\n"
"ds_read_b64_tr_b16 a[236:237], v237 offset:128\n"
"v_mfma_f32_16x16x32_bf16 a[16:19], a[220:223], v[188:191], a[16:19]\n"
"ds_read_b64_tr_b16 a[238:239], v237 offset:6272\n"
"v_mfma_f32_16x16x32_bf16 a[20:23], a[220:223], v[192:195], a[20:23]\n"
"s_waitcnt lgkmcnt(6)\n"
"v_mfma_f32_16x16x32_bf16 a[24:27], a[224:227], v[184:187], a[24:27]\n"
"ds_read_b64_tr_b16 a[216:217], v238 offset:128\n"
"v_mfma_f32_16x16x32_bf16 a[28:31], a[224:227], v[188:191], a[28:31]\n"
"ds_read_b64_tr_b16 a[218:219], v238 offset:6272\n"
"v_mfma_f32_16x16x32_bf16 a[32:35], a[224:227], v[192:195], a[32:35]\n"
"buffer_load_dwordx4 %[dKV1], %[rK], s64 offen offset:1024 nt lds\n"
"s_waitcnt lgkmcnt(6)\n"
"v_mfma_f32_16x16x32_bf16 a[36:39], a[228:231], v[184:187], a[36:39]\n"
"ds_read_b64_tr_b16 a[220:221], v239 offset:128\n"
"v_mfma_f32_16x16x32_bf16 a[40:43], a[228:231], v[188:191], a[40:43]\n"
"ds_read_b64_tr_b16 a[222:223], v239 offset:6272\n"
"v_mfma_f32_16x16x32_bf16 a[44:47], a[228:231], v[192:195], a[44:47]\n"
"s_waitcnt lgkmcnt(6)\n"
"v_mfma_f32_16x16x32_bf16 a[48:51], a[232:235], v[184:187], a[48:51]\n"
"ds_read_b64_tr_b16 a[224:225], v236 offset:256\n"
"v_mfma_f32_16x16x32_bf16 a[52:55], a[232:235], v[188:191], a[52:55]\n"
"ds_read_b64_tr_b16 a[226:227], v236 offset:6400\n"
"v_mfma_f32_16x16x32_bf16 a[56:59], a[232:235], v[192:195], a[56:59]\n"
"buffer_load_dwordx4 %[dKV2], %[rK], s64 offen offset:2048 nt lds\n"
"s_add_u32 m0, s61, 3072\n"
"s_waitcnt lgkmcnt(6)\n"
"v_mfma_f32_16x16x32_bf16 a[60:63], a[236:239], v[184:187], a[60:63]\n"
"ds_read_b64_tr_b16 a[228:229], v237 offset:256\n"
"v_mfma_f32_16x16x32_bf16 a[64:67], a[236:239], v[188:191], a[64:67]\n"
"ds_read_b64_tr_b16 a[230:231], v237 offset:6400\n"
"v_mfma_f32_16x16x32_bf16 a[68:71], a[236:239], v[192:195], a[68:71]\n"
"s_waitcnt lgkmcnt(6)\n"
"v_mfma_f32_16x16x32_bf16 a[72:75], a[216:219], v[184:187], a[72:75]\n"
"ds_read_b64_tr_b16 a[232:233], v238 offset:256\n"
"v_mfma_f32_16x16x32_bf16 a[76:79], a[216:219], v[188:191], a[76:79]\n"
"ds_read_b64_tr_b16 a[234:235], v238 offset:6400\n"
"v_mfma_f32_16x16x32_bf16 a[80:83], a[216:219], v[192:195], a[80:83]\n"
"s_add_u32 s64, s64, 12288\n"
"buffer_load_dwordx4 %[dKV0], %[rK], s68 offen offset:0 nt lds\n"
"s_waitcnt lgkmcnt(6)\n"
"v_mfma_f32_16x16x32_bf16 a[84:87], a[220:223], v[184:187], a[84:87]\n"
"ds_read_b64_tr_b16 a[236:237], v239 offset:256\n"
"v_mfma_f32_16x16x32_bf16 a[88:91], a[220:223], v[188:191], a[88:91]\n"
"ds_read_b64_tr_b16 a[238:239], v239 offset:6400\n"
"v_mfma_f32_16x16x32_bf16 a[92:95], a[220:223], v[192:195], a[92:95]\n"
"buffer_load_dwordx4 %[dKV1], %[rK], s68 offen offset:1024 nt lds\n"
"s_waitcnt lgkmcnt(6)\n"
"v_mfma_f32_16x16x32_bf16 a[96:99], a[224:227], v[184:187], a[96:99]\n"
"s_add_u32 s61, %[m0b], s60\n"
"s_add_u32 s60, s60, 12288\n"
"s_cmp_eq_u32 s60, 61440\n"
"s_cselect_b32 s60, 0, s60\n"
"v_add_u32 v236, s60, %[rdK]\n"
"v_mfma_f32_16x16x32_bf16 a[100:103], a[224:227], v[188:191], a[100:103]\n"
"v_xor_b32 v237, 64, v236\n"
"v_mfma_f32_16x16x32_bf16 a[104:107], a[224:227], v[192:195], a[104:107]\n"
"s_waitcnt vmcnt(17) lgkmcnt(0)\n"
"s_barrier\n"
"ds_read_b128 a[216:219], v236 offset:0\n"
"v_mfma_f32_16x16x32_bf16 a[108:111], a[228:231], v[184:187], a[108:111]\n"
"ds_read_b128 a[220:223], v237 offset:0\n"
"v_mfma_f32_16x16x32_bf16 a[112:115], a[228:231], v[188:191], a[112:115]\n"
"v_mfma_f32_16x16x32_bf16 a[116:119], a[228:231], v[192:195], a[116:119]\n"
"v_mfma_f32_16x16x32_bf16 a[120:123], a[232:235], v[184:187], a[120:123]\n"
"ds_read_b128 a[224:227], v236 offset:128\n"
"v_mfma_f32_16x16x32_bf16 a[124:127], a[232:235], v[188:191], a[124:127]\n"
"v_mfma_f32_16x16x32_bf16 a[128:131], a[232:235], v[192:195], a[128:131]\n"
"buffer_load_dwordx4 %[dKV2], %[rK], s68 offen offset:2048 nt lds\n"
"v_mfma_f32_16x16x32_bf16 a[132:135], a[236:239], v[184:187], a[132:135]\n"
"ds_read_b128 a[228:231], v237 offset:128\n"
"v_mfma_f32_16x16x32_bf16 a[136:139], a[236:239], v[188:191], a[136:139]\n"
"v_mfma_f32_16x16x32_bf16 a[140:143], a[236:239], v[192:195], a[140:143]\n"
"v_mfma_f32_16x16x32_bf16 v[208:211], v[220:223], v[184:187], v[208:211]\n"
"v_mfma_f32_16x16x32_bf16 v[212:215], v[220:223], v[188:191], v[212:215]\n"
"v_mfma_f32_16x16x32_bf16 v[216:219], v[220:223], v[192:195], v[216:219]\n"
"s_sub_u32 s69, s69, 1\n"
"s_cmp_lg_u32 s69, 0\n"
"s_cbranch_scc1 Lb_%=\n"
"s_waitcnt lgkmcnt(3)\n"
"v_mfma_f32_16x16x32_bf16 v[160:163], a[216:219], a[144:147], v[196:199]\n"
"ds_read_b128 a[232:235], v236 offset:256\n"
"v_mfma_f32_16x16x32_bf16 v[164:167], a[216:219], a[148:151], v[200:203]\n"
"v_mfma_f32_16x16x32_bf16 v[168:171], a[216:219], a[152:155], v[204:207]\n"
"s_mov_b32 m0, s61\n"
"s_mul_i32 s66, s56, 6144\n"
"s_add_u32 s66, s66, s57\n"
"s_add_u32 s68, s66, 196608\n"
"buffer_load_dwordx4 %[dW0], %[rWq], s66 offen offset:0 lds\n"
"s_waitcnt lgkmcnt(3)\n"
"v_mfma_f32_16x16x32_bf16 v[160:163], a[220:223], a[156:159], v[160:163]\n"
"ds_read_b128 a[236:239], v237 offset:256\n"
"v_mfma_f32_16x16x32_bf16 v[164:167], a[220:223], a[160:163], v[164:167]\n"
"v_mfma_f32_16x16x32_bf16 v[168:171], a[220:223], a[164:167], v[168:171]\n"
"buffer_load_dwordx4 %[dW1], %[rWq], s66 offen offset:1024 lds\n"
"s_waitcnt lgkmcnt(3)\n"
"v_mfma_f32_16x16x32_bf16 v[160:163], a[224:227], a[168:171], v[160:163]\n"
"ds_read_b128 a[216:219], v236 offset:6144\n"
"v_mfma_f32_16x16x32_bf16 v[164:167], a[224:227], a[172:175], v[164:167]\n"
"v_mfma_f32_16x16x32_bf16 v[168:171], a[224:227], a[176:179], v[168:171]\n"
"s_waitcnt lgkmcnt(3)\n"
"v_mfma_f32_16x16x32_bf16 v[160:163], a[228:231], a[180:183], v[160:163]\n"
"ds_read_b128 a[220:223], v237 offset:6144\n"
"v_mfma_f32_16x16x32_bf16 v[164:167], a[228:231], a[184:187], v[164:167]\n"
"v_mfma_f32_16x16x32_bf16 v[168:171], a[228:231], a[188:191], v[168:171]\n"
"buffer_load_dwordx4 %[dW2], %[rWq], s66 offen offset:2048 lds\n"
"s_add_u32 m0, s61, 3072\n"
"s_waitcnt lgkmcnt(3)\n"
"v_mfma_f32_16x16x32_bf16 v[160:163], a[232:235], a[192:195], v[160:163]\n"
"ds_read_b128 a[224:227], v236 offset:6272\n"
"v_mfma_f32_16x16x32_bf16 v[164:167], a[232:235], a[196:199], v[164:167]\n"
"v_mfma_f32_16x16x32_bf16 v[168:171], a[232:235], a[200:203], v[168:171]\n"
"s_add_u32 s66, s66, 393216\n"
"buffer_load_dwordx4 %[dW0], %[rWq], s68 offen offset:0 lds\n"
"s_waitcnt lgkmcnt(3)\n"
"v_mfma_f32_16x16x32_bf16 v[160:163], a[236:239], a[204:207], v[160:163]\n"
"ds_read_b128 a[228:231], v237 offset:6272\n"
"v_mfma_f32_16x16x32_bf16 v[164:167], a[236:239], a[208:211], v[164:167]\n"
"v_mfma_f32_16x16x32_bf16 v[168:171], a[236:239], a[212:215], v[168:171]\n"
"s_waitcnt lgkmcnt(3)\n"
"v_mfma_f32_16x16x32_bf16 v[172:175], a[216:219], a[144:147], v[196:199]\n"
"ds_read_b128 a[232:235], v236 offset:6400\n"
"v_mfma_f32_16x16x32_bf16 v[176:179], a[216:219], a[148:151], v[200:203]\n"
"v_mfma_f32_16x16x32_bf16 v[180:183], a[216:219], a[152:155], v[204:207]\n"
"buffer_load_dwordx4 %[dW1], %[rWq], s68 offen offset:1024 lds\n"
"s_waitcnt lgkmcnt(3)\n"
"v_mfma_f32_16x16x32_bf16 v[172:175], a[220:223], a[156:159], v[172:175]\n"
"ds_read_b128 a[236:239], v237 offset:6400\n"
"v_mfma_f32_16x16x32_bf16 v[176:179], a[220:223], a[160:163], v[176:179]\n"
"v_mfma_f32_16x16x32_bf16 v[180:183], a[220:223], a[164:167], v[180:183]\n"
"buffer_load_dwordx4 %[dW2], %[rWq], s68 offen offset:2048 lds\n"
"s_waitcnt lgkmcnt(3)\n"
"v_mfma_f32_16x16x32_bf16 v[172:175], a[224:227], a[168:171], v[172:175]\n"
"s_add_u32 s61, %[m0b], s60\n"
"s_add_u32 s60, s60, 12288\n"
"s_cmp_eq_u32 s60, 61440\n"
"s_cselect_b32 s60, 0, s60\n"
"v_add_u32 v236, s60, %[rdW]\n"
"v_mfma_f32_16x16x32_bf16 v[176:179], a[224:227], a[172:175], v[176:179]\n"
"v_xor_b32 v237, 8, v236\n"
"v_xor_b32 v238, 64, v236\n"
"v_xor_b32 v239, 72, v236\n"
"v_xor_b32 v240, 64, v240\n"
"v_mfma_f32_16x16x32_bf16 v[180:183], a[224:227], a[176:179], v[180:183]\n"
"s_waitcnt vmcnt(18) lgkmcnt(0)\n"
"s_barrier\n"
"ds_read_b64_tr_b16 a[216:217], v236 offset:0\n"
"ds_read_b64_tr_b16 a[218:219], v236 offset:6144\n"
"v_mfma_f32_16x16x32_bf16 v[172:175], a[228:231], a[180:183], v[172:175]\n"
"ds_read_b128 v[224:227], v240 offset:0\n"
"ds_read_b64_tr_b16 a[220:221], v237 offset:0\n"
"v_mfma_f32_16x16x32_bf16 v[176:179], a[228:231], a[184:187], v[176:179]\n"
"ds_read_b64_tr_b16 a[222:223], v237 offset:6144\n"
"ds_read_b128 v[228:231], v240 offset:2048\n"
"v_mfma_f32_16x16x32_bf16 v[180:183], a[228:231], a[188:191], v[180:183]\n"
"ds_read_b128 v[232:235], v240 offset:4096\n"
"v_mfma_f32_16x16x32_bf16 v[172:175], a[232:235], a[192:195], v[172:175]\n"
"ds_read_b64_tr_b16 a[224:225], v238 offset:0\n"
"v_mfma_f32_16x16x32_bf16 v[176:179], a[232:235], a[196:199], v[176:179]\n"
"ds_read_b64_tr_b16 a[226:227], v238 offset:6144\n"
"v_mfma_f32_16x16x32_bf16 v[180:183], a[232:235], a[200:203], v[180:183]\n"
"s_add_u32 m0, %[m0x], s63\n"
"s_add_u32 s67, %[x0], s56\n"
"s_add_u32 s75, s67, 9216\n"
"buffer_load_dwordx4 %[dX], %[rX], s67 offen offset:0 lds\n"
"v_mfma_f32_16x16x32_bf16 v[172:175], a[236:239], a[204:207], v[172:175]\n"
"ds_read_b64_tr_b16 a[228:229], v239 offset:0\n"
"v_mfma_f32_16x16x32_bf16 v[176:179], a[236:239], a[208:211], v[176:179]\n"
"ds_read_b64_tr_b16 a[230:231], v239 offset:6144\n"
"v_mfma_f32_16x16x32_bf16 v[180:183], a[236:239], a[212:215], v[180:183]\n"
"buffer_load_dwordx4 %[dX], %[rX], s75 offen offset:1024 lds\n"
"s_add_u32 s75, s67, 18432\n"
"s_waitcnt lgkmcnt(8)\n"
"v_mfma_f32_16x16x32_bf16 v[16:19], a[216:219], v[224:227], v[16:19]\n"
"ds_read_b64_tr_b16 a[232:233], v236 offset:128\n"
"s_waitcnt lgkmcnt(6)\n"
"v_mfma_f32_16x16x32_bf16 v[20:23], a[216:219], v[228:231], v[20:23]\n"
"ds_read_b64_tr_b16 a[234:235], v236 offset:6272\n"
"s_waitcnt lgkmcnt(6)\n"
"v_mfma_f32_16x16x32_bf16 v[24:27], a[216:219], v[232:235], v[24:27]\n"
"buffer_load_dwordx4 %[dX], %[rX], s75 offen offset:2048 lds\n"
"s_add_u32 s56, s56, 128\n"
"s_cmp_eq_u32 s56, 1280\n"
"s_cselect_b32 s56, 0, s56\n"
"s_add_u32 s63, s63, 12288\n"
"s_cmp_eq_u32 s63, 36864\n"
"s_cselect_b32 s63, 0, s63\n"
"v_mfma_f32_16x16x32_bf16 v[28:31], a[220:223], v[224:227], v[28:31]\n"
"ds_read_b64_tr_b16 a[236:237], v237 offset:128\n"
"v_mfma_f32_16x16x32_bf16 v[32:35], a[220:223], v[228:231], v[32:35]\n"
"ds_read_b64_tr_b16 a[238:239], v237 offset:6272\n"
"v_max3_f32 v242, v160, v161, v162\n"
"v_max3_f32 v242, v242, v163, v164\n"
"v_mfma_f32_16x16x32_bf16 v[36:39], a[220:223], v[232:235], v[36:39]\n"
"s_mov_b32 m0, s61\n"
"s_add_u32 s68, s65, 3072\n"
"buffer_load_dwordx4 %[dKV0], %[rV], s65 offen offset:0 nt lds\n"
"v_max3_f32 v242, v242, v165, v166\n"
"s_waitcnt lgkmcnt(6)\n"
"v_mfma_f32_16x16x32_bf16 v[40:43], a[224:227], v[224:227], v[40:43]\n"
"ds_read_b64_tr_b16 a[216:217], v238 offset:128\n"
"v_max3_f32 v242, v242, v167, v168\n"
"v_max3_f32 v242, v242, v169, v170\n"
"v_max_f32 v242, v242, v171\n"
"v_mfma_f32_16x16x32_bf16 v[44:47], a[224:227], v[228:231], v[44:47]\n"
"ds_read_b64_tr_b16 a[218:219], v238 offset:6272\n"
"v_max3_f32 v243, v172, v173, v174\n"
"v_max3_f32 v243, v243, v175, v176\n"
"v_max3_f32 v243, v243, v177, v178\n"
"v_mfma_f32_16x16x32_bf16 v[48:51], a[224:227], v[232:235], v[48:51]\n"
"v_max3_f32 v243, v243, v179, v180\n"
"v_max3_f32 v243, v243, v181, v182\n"
"v_max3_f32 v243, v243, v183, v242\n"
"v_cmp_lt_f32 vcc, s70, v243\n"
"s_waitcnt lgkmcnt(6)\n"
"v_mfma_f32_16x16x32_bf16 v[52:55], a[228:231], v[224:227], v[52:55]\n"
"ds_read_b64_tr_b16 a[220:221], v239 offset:128\n"
"s_nop 1\n"
"s_cbranch_vccz 1\n"
"s_call_b64 s[76:77], Lslow_%=\n"
"v_mfma_f32_16x16x32_bf16 v[56:59], a[228:231], v[228:231], v[56:59]\n"
"ds_read_b64_tr_b16 a[222:223], v239 offset:6272\n"
"v_exp_f32 v160, v160\n"
"v_mfma_f32_16x16x32_bf16 v[60:63], a[228:231], v[232:235], v[60:63]\n"
"buffer_load_dwordx4 %[dKV1], %[rV], s65 offen offset:1024 nt lds\n"
"v_exp_f32 v161, v161\n"
"s_waitcnt lgkmcnt(6)\n"
"v_mfma_f32_16x16x32_bf16 v[64:67], a[232:235], v[224:227], v[64:67]\n"
"ds_read_b64_tr_b16 a[224:225], v236 offset:256\n"
"v_exp_f32 v162, v162\n"
"v_exp_f32 v163, v163\n"
"v_mfma_f32_16x16x32_bf16 v[68:71], a[232:235], v[228:231], v[68:71]\n"
"ds_read_b64_tr_b16 a[226:227], v236 offset:6400\n"
"v_exp_f32 v172, v172\n"
"v_mfma_f32_16x16x32_bf16 v[72:75], a[232:235], v[232:235], v[72:75]\n"
"v_exp_f32 v173, v173\n"
"v_exp_f32 v174, v174\n"
"s_waitcnt lgkmcnt(6)\n"
"v_mfma_f32_16x16x32_bf16 v[76:79], a[236:239], v[224:227], v[76:79]\n"
"ds_read_b64_tr_b16 a[228:229], v237 offset:256\n"
"v_exp_f32 v175, v175\n"
"v_cvt_pk_bf16_f32 v184, v160, v161\n"
"v_mfma_f32_16x16x32_bf16 v[80:83], a[236:239], v[228:231], v[80:83]\n"
"ds_read_b64_tr_b16 a[230:231], v237 offset:6400\n"
"v_cvt_pk_bf16_f32 v185, v162, v163\n"
"v_exp_f32 v164, v164\n"
"v_mfma_f32_16x16x32_bf16 v[84:87], a[236:239], v[232:235], v[84:87]\n"
"buffer_load_dwordx4 %[dKV2], %[rV], s65 offen offset:2048 nt lds\n"
"s_add_u32 m0, s61, 3072\n"
"s_waitcnt lgkmcnt(6)\n"
"v_mfma_f32_16x16x32_bf16 v[88:91], a[216:219], v[224:227], v[88:91]\n"
"ds_read_b64_tr_b16 a[232:233], v238 offset:256\n"
"v_exp_f32 v165, v165\n"
"v_mfma_f32_16x16x32_bf16 v[92:95], a[216:219], v[228:231], v[92:95]\n"
"ds_read_b64_tr_b16 a[234:235], v238 offset:6400\n"
"v_exp_f32 v166, v166\n"
"v_exp_f32 v167, v167\n"
"v_mfma_f32_16x16x32_bf16 v[96:99], a[216:219], v[232:235], v[96:99]\n"
"s_add_u32 s65, s65, 12288\n"
"buffer_load_dwordx4 %[dKV0], %[rV], s68 offen offset:0 nt lds\n"
"s_waitcnt lgkmcnt(6)\n"
"v_mfma_f32_16x16x32_bf16 v[100:103], a[220:223], v[224:227], v[100:103]\n"
"ds_read_b64_tr_b16 a[236:237], v239 offset:256\n"
"v_cvt_pk_bf16_f32 v186, v172, v173\n"
"v_cvt_pk_bf16_f32 v187, v174, v175\n"
"v_exp_f32 v176, v176\n"
"v_mfma_f32_16x16x32_bf16 v[104:107], a[220:223], v[228:231], v[104:107]\n"
"ds_read_b64_tr_b16 a[238:239], v239 offset:6400\n"
"v_exp_f32 v177, v177\n"
"v_mfma_f32_16x16x32_bf16 v[108:111], a[220:223], v[232:235], v[108:111]\n"
"buffer_load_dwordx4 %[dKV1], %[rV], s68 offen offset:1024 nt lds\n"
"v_exp_f32 v178, v178\n"
"s_waitcnt lgkmcnt(6)\n"
"v_mfma_f32_16x16x32_bf16 v[112:115], a[224:227], v[224:227], v[112:115]\n"
"s_add_u32 s61, %[m0b], s60\n"
"s_add_u32 s60, s60, 12288\n"
"s_cmp_eq_u32 s60, 61440\n"
"s_cselect_b32 s60, 0, s60\n"
"v_add_u32 v236, s60, %[rdV]\n"
"v_mfma_f32_16x16x32_bf16 v[116:119], a[224:227], v[228:231], v[116:119]\n"
"v_xor_b32 v237, 32, v236\n"
"v_xor_b32 v238, 64, v236\n"
"v_xor_b32 v239, 96, v236\n"
"v_mfma_f32_16x16x32_bf16 v[120:123], a[224:227], v[232:235], v[120:123]\n"
"s_waitcnt vmcnt(20) lgkmcnt(0)\n"
"s_barrier\n"
"ds_read_b64_tr_b16 a[216:217], v236 offset:0\n"
"ds_read_b64_tr_b16 a[218:219], v236 offset:6144\n"
"v_mfma_f32_16x16x32_bf16 v[124:127], a[228:231], v[224:227], v[124:127]\n"
"ds_read_b64_tr_b16 a[220:221], v237 offset:0\n"
"v_exp_f32 v179, v179\n"
"v_cvt_pk_bf16_f32 v188, v164, v165\n"
"v_mfma_f32_16x16x32_bf16 v[128:131], a[228:231], v[228:231], v[128:131]\n"
"ds_read_b64_tr_b16 a[222:223], v237 offset:6144\n"
"v_cvt_pk_bf16_f32 v189, v166, v167\n"
"v_exp_f32 v168, v168\n"
"v_mfma_f32_16x16x32_bf16 v[132:135], a[228:231], v[232:235], v[132:135]\n"
"v_exp_f32 v169, v169\n"
"v_exp_f32 v170, v170\n"
"v_mfma_f32_16x16x32_bf16 v[136:139], a[232:235], v[224:227], v[136:139]\n"
"ds_read_b64_tr_b16 a[224:225], v238 offset:0\n"
"v_exp_f32 v171, v171\n"
"v_mfma_f32_16x16x32_bf16 v[140:143], a[232:235], v[228:231], v[140:143]\n"
"ds_read_b64_tr_b16 a[226:227], v238 offset:6144\n"
"v_cvt_pk_bf16_f32 v190, v176, v177\n"
"v_cvt_pk_bf16_f32 v191, v178, v179\n"
"v_mfma_f32_16x16x32_bf16 v[144:147], a[232:235], v[232:235], v[144:147]\n"
"buffer_load_dwordx4 %[dKV2], %[rV], s68 offen offset:2048 nt lds\n"
"v_exp_f32 v180, v180\n"
"v_mfma_f32_16x16x32_bf16 v[148:151], a[236:239], v[224:227], v[148:151]\n"
"ds_read_b64_tr_b16 a[228:229], v239 offset:0\n"
"v_exp_f32 v181, v181\n"
"v_exp_f32 v182, v182\n"
"v_mfma_f32_16x16x32_bf16 v[152:155], a[236:239], v[228:231], v[152:155]\n"
"ds_read_b64_tr_b16 a[230:231], v239 offset:6144\n"
"v_exp_f32 v183, v183\n"
"v_mfma_f32_16x16x32_bf16 v[156:159], a[236:239], v[232:235], v[156:159]\n"
"v_cvt_pk_bf16_f32 v192, v168, v169\n"
"v_cvt_pk_bf16_f32 v193, v170, v171\n"
"v_cvt_pk_bf16_f32 v194, v180, v181\n"
"v_cvt_pk_bf16_f32 v195, v182, v183\n"
"s_waitcnt lgkmcnt(6)\n"
"v_mfma_f32_16x16x32_bf16 a[0:3], a[216:219], v[184:187], a[0:3]\n"
"ds_read_b64_tr_b16 a[232:233], v236 offset:128\n"
"v_mfma_f32_16x16x32_bf16 a[4:7], a[216:219], v[188:191], a[4:7]\n"
"ds_read_b64_tr_b16 a[234:235], v236 offset:6272\n"
"v_mfma_f32_16x16x32_bf16 a[8:11], a[216:219], v[192:195], a[8:11]\n"
"s_mov_b32 m0, s61\n"
"s_add_u32 s68, s64, 3072\n"
"buffer_load_dwordx4 %[dKV0], %[rK], s64 offen offset:0 nt lds\n"
"s_waitcnt lgkmcnt(6)\n"
"v_mfma_f32_16x16x32_bf16 a[12:15], a[220:223], v[184:187], a[12:15]\n"
"ds_read_b64_tr_b16 a[236:237], v237 offset:128\n"
"v_mfma_f32_16x16x32_bf16 a[16:19], a[220:223], v[188:191], a[16:19]\n"
"ds_read_b64_tr_b16 a[238:239], v237 offset:6272\n"
"v_mfma_f32_16x16x32_bf16 a[20:23], a[220:223], v[192:195], a[20:23]\n"
"s_waitcnt lgkmcnt(6)\n"
"v_mfma_f32_16x16x32_bf16 a[24:27], a[224:227], v[184:187], a[24:27]\n"
"ds_read_b64_tr_b16 a[216:217], v238 offset:128\n"
"v_mfma_f32_16x16x32_bf16 a[28:31], a[224:227], v[188:191], a[28:31]\n"
"ds_read_b64_tr_b16 a[218:219], v238 offset:6272\n"
"v_mfma_f32_16x16x32_bf16 a[32:35], a[224:227], v[192:195], a[32:35]\n"
"buffer_load_dwordx4 %[dKV1], %[rK], s64 offen offset:1024 nt lds\n"
"s_waitcnt lgkmcnt(6)\n"
"v_mfma_f32_16x16x32_bf16 a[36:39], a[228:231], v[184:187], a[36:39]\n"
"ds_read_b64_tr_b16 a[220:221], v239 offset:128\n"
"v_mfma_f32_16x16x32_bf16 a[40:43], a[228:231], v[188:191], a[40:43]\n"
"ds_read_b64_tr_b16 a[222:223], v239 offset:6272\n"
"v_mfma_f32_16x16x32_bf16 a[44:47], a[228:231], v[192:195], a[44:47]\n"
"s_waitcnt lgkmcnt(6)\n"
"v_mfma_f32_16x16x32_bf16 a[48:51], a[232:235], v[184:187], a[48:51]\n"
"ds_read_b64_tr_b16 a[224:225], v236 offset:256\n"
"v_mfma_f32_16x16x32_bf16 a[52:55], a[232:235], v[188:191], a[52:55]\n"
"ds_read_b64_tr_b16 a[226:227], v236 offset:6400\n"
"v_mfma_f32_16x16x32_bf16 a[56:59], a[232:235], v[192:195], a[56:59]\n"
"buffer_load_dwordx4 %[dKV2], %[rK], s64 offen offset:2048 nt lds\n"
"s_add_u32 m0, s61, 3072\n"
"s_waitcnt lgkmcnt(6)\n"
"v_mfma_f32_16x16x32_bf16 a[60:63], a[236:239], v[184:187], a[60:63]\n"
"ds_read_b64_tr_b16 a[228:229], v237 offset:256\n"
"v_mfma_f32_16x16x32_bf16 a[64:67], a[236:239], v[188:191], a[64:67]\n"
"ds_read_b64_tr_b16 a[230:231], v237 offset:6400\n"
"v_mfma_f32_16x16x32_bf16 a[68:71], a[236:239], v[192:195], a[68:71]\n"
"s_waitcnt lgkmcnt(6)\n"
"v_mfma_f32_16x16x32_bf16 a[72:75], a[216:219], v[184:187], a[72:75]\n"
"ds_read_b64_tr_b16 a[232:233], v238 offset:256\n"
"v_mfma_f32_16x16x32_bf16 a[76:79], a[216:219], v[188:191], a[76:79]\n"
"ds_read_b64_tr_b16 a[234:235], v238 offset:6400\n"
"v_mfma_f32_16x16x32_bf16 a[80:83], a[216:219], v[192:195], a[80:83]\n"
"s_add_u32 s64, s64, 12288\n"
"buffer_load_dwordx4 %[dKV0], %[rK], s68 offen offset:0 nt lds\n"
"s_waitcnt lgkmcnt(6)\n"
"v_mfma_f32_16x16x32_bf16 a[84:87], a[220:223], v[184:187], a[84:87]\n"
"ds_read_b64_tr_b16 a[236:237], v239 offset:256\n"
"v_mfma_f32_16x16x32_bf16 a[88:91], a[220:223], v[188:191], a[88:91]\n"
"ds_read_b64_tr_b16 a[238:239], v239 offset:6400\n"
"v_mfma_f32_16x16x32_bf16 a[92:95], a[220:223], v[192:195], a[92:95]\n"
"buffer_load_dwordx4 %[dKV1], %[rK], s68 offen offset:1024 nt lds\n"
"s_waitcnt lgkmcnt(6)\n"
"v_mfma_f32_16x16x32_bf16 a[96:99], a[224:227], v[184:187], a[96:99]\n"
"s_add_u32 s61, %[m0b], s60\n"
"s_add_u32 s60, s60, 12288\n"
"s_cmp_eq_u32 s60, 61440\n"
"s_cselect_b32 s60, 0, s60\n"
"v_add_u32 v236, s60, %[rdK]\n"
"v_mfma_f32_16x16x32_bf16 a[100:103], a[224:227], v[188:191], a[100:103]\n"
"v_xor_b32 v237, 64, v236\n"
"v_mfma_f32_16x16x32_bf16 a[104:107], a[224:227], v[192:195], a[104:107]\n"
"s_waitcnt vmcnt(20) lgkmcnt(0)\n"
"s_barrier\n"
"ds_read_b128 a[216:219], v236 offset:0\n"
"v_mfma_f32_16x16x32_bf16 a[108:111], a[228:231], v[184:187], a[108:111]\n"
"ds_read_b128 a[220:223], v237 offset:0\n"
"v_mfma_f32_16x16x32_bf16 a[112:115], a[228:231], v[188:191], a[112:115]\n"
"v_mfma_f32_16x16x32_bf16 a[116:119], a[228:231], v[192:195], a[116:119]\n"
"v_mfma_f32_16x16x32_bf16 a[120:123], a[232:235], v[184:187], a[120:123]\n"
"ds_read_b128 a[224:227], v236 offset:128\n"
"v_mfma_f32_16x16x32_bf16 a[124:127], a[232:235], v[188:191], a[124:127]\n"
"v_mfma_f32_16x16x32_bf16 a[128:131], a[232:235], v[192:195], a[128:131]\n"
"buffer_load_dwordx4 %[dKV2], %[rK], s68 offen offset:2048 nt lds\n"
"v_mfma_f32_16x16x32_bf16 a[132:135], a[236:239], v[184:187], a[132:135]\n"
"ds_read_b128 a[228:231], v237 offset:128\n"
"v_mfma_f32_16x16x32_bf16 a[136:139], a[236:239], v[188:191], a[136:139]\n"
"v_mfma_f32_16x16x32_bf16 a[140:143], a[236:239], v[192:195], a[140:143]\n"
"v_mfma_f32_16x16x32_bf16 v[208:211], v[220:223], v[184:187], v[208:211]\n"
"v_mfma_f32_16x16x32_bf16 v[212:215], v[220:223], v[188:191], v[212:215]\n"
"v_mfma_f32_16x16x32_bf16 v[216:219], v[220:223], v[192:195], v[216:219]\n"
"s_waitcnt lgkmcnt(3)\n"
"v_mfma_f32_16x16x32_bf16 v[160:163], a[216:219], a[144:147], v[196:199]\n"
"ds_read_b128 a[232:235], v236 offset:256\n"
"v_mfma_f32_16x16x32_bf16 v[164:167], a[216:219], a[148:151], v[200:203]\n"
"v_mfma_f32_16x16x32_bf16 v[168:171], a[216:219], a[152:155], v[204:207]\n"
"s_mov_b32 m0, s61\n"
"s_add_u32 s68, s66, 196608\n"
"buffer_load_dwordx4 %[dW0], %[rWq], s66 offen offset:0 lds\n"
"s_waitcnt lgkmcnt(3)\n"
"v_mfma_f32_16x16x32_bf16 v[160:163], a[220:223], a[156:159], v[160:163]\n"
"ds_read_b128 a[236:239], v237 offset:256\n"
"v_mfma_f32_16x16x32_bf16 v[164:167], a[220:223], a[160:163], v[164:167]\n"
"v_mfma_f32_16x16x32_bf16 v[168:171], a[220:223], a[164:167], v[168:171]\n"
"s_waitcnt lgkmcnt(3)\n"
"v_mfma_f32_16x16x32_bf16 v[160:163], a[224:227], a[168:171], v[160:163]\n"
"ds_read_b128 a[216:219], v236 offset:6144\n"
"v_mfma_f32_16x16x32_bf16 v[164:167], a[224:227], a[172:175], v[164:167]\n"
"v_mfma_f32_16x16x32_bf16 v[168:171], a[224:227], a[176:179], v[168:171]\n"
"buffer_load_dwordx4 %[dW1], %[rWq], s66 offen offset:1024 lds\n"
"s_waitcnt lgkmcnt(3)\n"
"v_mfma_f32_16x16x32_bf16 v[160:163], a[228:231], a[180:183], v[160:163]\n"
"ds_read_b128 a[220:223], v237 offset:6144\n"
"v_mfma_f32_16x16x32_bf16 v[164:167], a[228:231], a[184:187], v[164:167]\n"
"v_mfma_f32_16x16x32_bf16 v[168:171], a[228:231], a[188:191], v[168:171]\n"
"s_waitcnt lgkmcnt(3)\n"
"v_mfma_f32_16x16x32_bf16 v[160:163], a[232:235], a[192:195], v[160:163]\n"
"ds_read_b128 a[224:227], v236 offset:6272\n"
"v_mfma_f32_16x16x32_bf16 v[164:167], a[232:235], a[196:199], v[164:167]\n"
"v_mfma_f32_16x16x32_bf16 v[168:171], a[232:235], a[200:203], v[168:171]\n"
"buffer_load_dwordx4 %[dW2], %[rWq], s66 offen offset:2048 lds\n"
"s_add_u32 m0, s61, 3072\n"
"s_waitcnt lgkmcnt(3)\n"
"v_mfma_f32_16x16x32_bf16 v[160:163], a[236:239], a[204:207], v[160:163]\n"
"ds_read_b128 a[228:231], v237 offset:6272\n"
"v_mfma_f32_16x16x32_bf16 v[164:167], a[236:239], a[208:211], v[164:167]\n"
"v_mfma_f32_16x16x32_bf16 v[168:171], a[236:239], a[212:215], v[168:171]\n"
"s_waitcnt lgkmcnt(3)\n"
"v_mfma_f32_16x16x32_bf16 v[172:175], a[216:219], a[144:147], v[196:199]\n"
"ds_read_b128 a[232:235], v236 offset:6400\n"
"v_mfma_f32_16x16x32_bf16 v[176:179], a[216:219], a[148:151], v[200:203]\n"
"v_mfma_f32_16x16x32_bf16 v[180:183], a[216:219], a[152:155], v[204:207]\n"
"s_add_u32 s66, s66, 393216\n"
"buffer_load_dwordx4 %[dW0], %[rWq], s68 offen offset:0 lds\n"
"s_waitcnt lgkmcnt(3)\n"
"v_mfma_f32_16x16x32_bf16 v[172:175], a[220:223], a[156:159], v[172:175]\n"
"ds_read_b128 a[236:239], v237 offset:6400\n"
"v_mfma_f32_16x16x32_bf16 v[176:179], a[220:223], a[160:163], v[176:179]\n"
"v_mfma_f32_16x16x32_bf16 v[180:183], a[220:223], a[164:167], v[180:183]\n"
"buffer_load_dwordx4 %[dW1], %[rWq], s68 offen offset:1024 lds\n"
"s_waitcnt lgkmcnt(3)\n"
"v_mfma_f32_16x16x32_bf16 v[172:175], a[224:227], a[168:171], v[172:175]\n"
"s_add_u32 s61, %[m0b], s60\n"
"s_add_u32 s60, s60, 12288\n"
"s_cmp_eq_u32 s60, 61440\n"
"s_cselect_b32 s60, 0, s60\n"
"v_add_u32 v236, s60, %[rdW]\n"
"v_mfma_f32_16x16x32_bf16 v[176:179], a[224:227], a[172:175], v[176:179]\n"
"v_xor_b32 v237, 8, v236\n"
"v_xor_b32 v238, 64, v236\n"
"v_xor_b32 v239, 72, v236\n"
"v_add_u32 v240, s62, %[rdX]\n"
"s_add_u32 s62, s62, 12288\n"
"s_cmp_eq_u32 s62, 36864\n"
"s_cselect_b32 s62, 0, s62\n"
"v_mfma_f32_16x16x32_bf16 v[180:183], a[224:227], a[176:179], v[180:183]\n"
"s_waitcnt vmcnt(17) lgkmcnt(0)\n"
"s_barrier\n"
"ds_read_b64_tr_b16 a[216:217], v236 offset:0\n"
"ds_read_b64_tr_b16 a[218:219], v236 offset:6144\n"
"v_mfma_f32_16x16x32_bf16 v[172:175], a[228:231], a[180:183], v[172:175]\n"
"ds_read_b128 a[240:243], v240 offset:0\n"
"ds_read_b64_tr_b16 a[220:221], v237 offset:0\n"
"v_mfma_f32_16x16x32_bf16 v[176:179], a[228:231], a[184:187], v[176:179]\n"
"ds_read_b64_tr_b16 a[222:223], v237 offset:6144\n"
"ds_read_b128 a[244:247], v240 offset:2048\n"
"v_mfma_f32_16x16x32_bf16 v[180:183], a[228:231], a[188:191], v[180:183]\n"
"ds_read_b128 a[248:251], v240 offset:4096\n"
"v_mfma_f32_16x16x32_bf16 v[172:175], a[232:235], a[192:195], v[172:175]\n"
"ds_read_b64_tr_b16 a[224:225], v238 offset:0\n"
"v_mfma_f32_16x16x32_bf16 v[176:179], a[232:235], a[196:199], v[176:179]\n"
"ds_read_b64_tr_b16 a[226:227], v238 offset:6144\n"
"v_mfma_f32_16x16x32_bf16 v[180:183], a[232:235], a[200:203], v[180:183]\n"
"buffer_load_dwordx4 %[dW2], %[rWq], s68 offen offset:2048 lds\n"
"v_mfma_f32_16x16x32_bf16 v[172:175], a[236:239], a[204:207], v[172:175]\n"
"ds_read_b64_tr_b16 a[228:229], v239 offset:0\n"
"v_mfma_f32_16x16x32_bf16 v[176:179], a[236:239], a[208:211], v[176:179]\n"
"ds_read_b64_tr_b16 a[230:231], v239 offset:6144\n"
"v_mfma_f32_16x16x32_bf16 v[180:183], a[236:239], a[212:215], v[180:183]\n"
"s_waitcnt lgkmcnt(8)\n"
"v_mfma_f32_16x16x32_bf16 v[16:19], a[216:219], a[240:243], v[16:19]\n"
"ds_read_b64_tr_b16 a[232:233], v236 offset:128\n"
"s_waitcnt lgkmcnt(6)\n"
"v_mfma_f32_16x16x32_bf16 v[20:23], a[216:219], a[244:247], v[20:23]\n"
"ds_read_b64_tr_b16 a[234:235], v236 offset:6272\n"
"s_waitcnt lgkmcnt(6)\n"
"v_mfma_f32_16x16x32_bf16 v[24:27], a[216:219], a[248:251], v[24:27]\n"
"s_mov_b32 m0, s61\n"
"s_mul_i32 s66, s56, 6144\n"
"s_add_u32 s66, s66, s57\n"
"s_add_u32 s68, s66, 196608\n"
"buffer_load_dwordx4 %[dW0], %[rWq], s66 offen offset:0 lds\n"
"v_mfma_f32_16x16x32_bf16 v[28:31], a[220:223], a[240:243], v[28:31]\n"
"ds_read_b64_tr_b16 a[236:237], v237 offset:128\n"
"v_mfma_f32_16x16x32_bf16 v[32:35], a[220:223], a[244:247], v[32:35]\n"
"ds_read_b64_tr_b16 a[238:239], v237 offset:6272\n"
"v_max3_f32 v242, v160, v161, v162\n"
"v_max3_f32 v242, v242, v163, v164\n"
"v_max3_f32 v242, v242, v165, v166\n"
"v_mfma_f32_16x16x32_bf16 v[36:39], a[220:223], a[248:251], v[36:39]\n"
"buffer_load_dwordx4 %[dW1], %[rWq], s66 offen offset:1024 lds\n"
"v_max3_f32 v242, v242, v167, v168\n"
"v_max3_f32 v242, v242, v169, v170\n"
"s_waitcnt lgkmcnt(6)\n"
"v_mfma_f32_16x16x32_bf16 v[40:43], a[224:227], a[240:243], v[40:43]\n"
"ds_read_b64_tr_b16 a[216:217], v238 offset:128\n"
"v_max_f32 v242, v242, v171\n"
"v_max3_f32 v243, v172, v173, v174\n"
"v_mfma_f32_16x16x32_bf16 v[44:47], a[224:227], a[244:247], v[44:47]\n"
"ds_read_b64_tr_b16 a[218:219], v238 offset:6272\n"
"v_max3_f32 v243, v243, v175, v176\n"
"v_max3_f32 v243, v243, v177, v178\n"
"v_max3_f32 v243, v243, v179, v180\n"
"v_mfma_f32_16x16x32_bf16 v[48:51], a[224:227], a[248:251], v[48:51]\n"
"v_max3_f32 v243, v243, v181, v182\n"
"v_max3_f32 v243, v243, v183, v242\n"
"v_cmp_lt_f32 vcc, s70, v243\n"
"s_waitcnt lgkmcnt(6)\n"
"v_mfma_f32_16x16x32_bf16 v[52:55], a[228:231], a[240:243], v[52:55]\n"
"ds_read_b64_tr_b16 a[220:221], v239 offset:128\n"
"s_nop 1\n"
"s_cbranch_vccz 1\n"
"s_call_b64 s[76:77], Lslow_%=\n"
"v_mfma_f32_16x16x32_bf16 v[56:59], a[228:231], a[244:247], v[56:59]\n"
"ds_read_b64_tr_b16 a[222:223], v239 offset:6272\n"
"v_exp_f32 v160, v160\n"
"v_exp_f32 v161, v161\n"
"v_mfma_f32_16x16x32_bf16 v[60:63], a[228:231], a[248:251], v[60:63]\n"
"buffer_load_dwordx4 %[dW2], %[rWq], s66 offen offset:2048 lds\n"
"s_add_u32 m0, s61, 3072\n"
"s_waitcnt lgkmcnt(6)\n"
"v_mfma_f32_16x16x32_bf16 v[64:67], a[232:235], a[240:243], v[64:67]\n"
"ds_read_b64_tr_b16 a[224:225], v236 offset:256\n"
"v_exp_f32 v162, v162\n"
"v_exp_f32 v163, v163\n"
"v_mfma_f32_16x16x32_bf16 v[68:71], a[232:235], a[244:247], v[68:71]\n"
"ds_read_b64_tr_b16 a[226:227], v236 offset:6400\n"
"v_exp_f32 v172, v172\n"
"v_mfma_f32_16x16x32_bf16 v[72:75], a[232:235], a[248:251], v[72:75]\n"
"s_add_u32 s66, s66, 393216\n"
"buffer_load_dwordx4 %[dW0], %[rWq], s68 offen offset:0 lds\n"
"v_exp_f32 v173, v173\n"
"s_waitcnt lgkmcnt(6)\n"
"v_mfma_f32_16x16x32_bf16 v[76:79], a[236:239], a[240:243], v[76:79]\n"
"ds_read_b64_tr_b16 a[228:229], v237 offset:256\n"
"v_exp_f32 v174, v174\n"
"v_mfma_f32_16x16x32_bf16 v[80:83], a[236:239], a[244:247], v[80:83]\n"
"ds_read_b64_tr_b16 a[230:231], v237 offset:6400\n"
"v_exp_f32 v175, v175\n"
"v_cvt_pk_bf16_f32 v184, v160, v161\n"
"v_mfma_f32_16x16x32_bf16 v[84:87], a[236:239], a[248:251], v[84:87]\n"
"v_cvt_pk_bf16_f32 v185, v162, v163\n"
"v_exp_f32 v164, v164\n"
"v_exp_f32 v165, v165\n"
"s_waitcnt lgkmcnt(6)\n"
"v_mfma_f32_16x16x32_bf16 v[88:91], a[216:219], a[240:243], v[88:91]\n"
"ds_read_b64_tr_b16 a[232:233], v238 offset:256\n"
"v_exp_f32 v166, v166\n"
"v_mfma_f32_16x16x32_bf16 v[92:95], a[216:219], a[244:247], v[92:95]\n"
"ds_read_b64_tr_b16 a[234:235], v238 offset:6400\n"
"v_exp_f32 v167, v167\n"
"v_cvt_pk_bf16_f32 v186, v172, v173\n"
"v_mfma_f32_16x16x32_bf16 v[96:99], a[216:219], a[248:251], v[96:99]\n"
"buffer_load_dwordx4 %[dW1], %[rWq], s68 offen offset:1024 lds\n"
"v_cvt_pk_bf16_f32 v187, v174, v175\n"
"v_exp_f32 v176, v176\n"
"s_waitcnt lgkmcnt(6)\n"
"v_mfma_f32_16x16x32_bf16 v[100:103], a[220:223], a[240:243], v[100:103]\n"
"ds_read_b64_tr_b16 a[236:237], v239 offset:256\n"
"v_exp_f32 v177, v177\n"
"v_mfma_f32_16x16x32_bf16 v[104:107], a[220:223], a[244:247], v[104:107]\n"
"ds_read_b64_tr_b16 a[238:239], v239 offset:6400\n"
"v_exp_f32 v178, v178\n"
"v_exp_f32 v179, v179\n"
"v_mfma_f32_16x16x32_bf16 v[108:111], a[220:223], a[248:251], v[108:111]\n"
"buffer_load_dwordx4 %[dW2], %[rWq], s68 offen offset:2048 lds\n"
"v_cvt_pk_bf16_f32 v188, v164, v165\n"
"s_waitcnt lgkmcnt(6)\n"
"v_mfma_f32_16x16x32_bf16 v[112:115], a[224:227], a[240:243], v[112:115]\n"
"s_add_u32 s61, %[m0b], s60\n"
"s_add_u32 s60, s60, 12288\n"
"s_cmp_eq_u32 s60, 61440\n"
"s_cselect_b32 s60, 0, s60\n"
"v_add_u32 v236, s60, %[rdV]\n"
"v_mfma_f32_16x16x32_bf16 v[116:119], a[224:227], a[244:247], v[116:119]\n"
"v_xor_b32 v237, 32, v236\n"
"v_xor_b32 v238, 64, v236\n"
"v_xor_b32 v239, 96, v236\n"
"v_mfma_f32_16x16x32_bf16 v[120:123], a[224:227], a[248:251], v[120:123]\n"
"v_cvt_pk_bf16_f32 v189, v166, v167\n"
"s_waitcnt vmcnt(18) lgkmcnt(0)\n"
"s_barrier\n"
"ds_read_b64_tr_b16 a[216:217], v236 offset:0\n"
"ds_read_b64_tr_b16 a[218:219], v236 offset:6144\n"
"v_mfma_f32_16x16x32_bf16 v[124:127], a[228:231], a[240:243], v[124:127]\n"
"ds_read_b64_tr_b16 a[220:221], v237 offset:0\n"
"v_exp_f32 v168, v168\n"
"v_exp_f32 v169, v169\n"
"v_mfma_f32_16x16x32_bf16 v[128:131], a[228:231], a[244:247], v[128:131]\n"
"ds_read_b64_tr_b16 a[222:223], v237 offset:6144\n"
"v_exp_f32 v170, v170\n"
"v_mfma_f32_16x16x32_bf16 v[132:135], a[228:231], a[248:251], v[132:135]\n"
"v_exp_f32 v171, v171\n"
"v_cvt_pk_bf16_f32 v190, v176, v177\n"
"v_cvt_pk_bf16_f32 v191, v178, v179\n"
"v_mfma_f32_16x16x32_bf16 v[136:139], a[232:235], a[240:243], v[136:139]\n"
"ds_read_b64_tr_b16 a[224:225], v238 offset:0\n"
"v_exp_f32 v180, v180\n"
"v_exp_f32 v181, v181\n"
"v_mfma_f32_16x16x32_bf16 v[140:143], a[232:235], a[244:247], v[140:143]\n"
"ds_read_b64_tr_b16 a[226:227], v238 offset:6144\n"
"v_exp_f32 v182, v182\n"
"v_mfma_f32_16x16x32_bf16 v[144:147], a[232:235], a[248:251], v[144:147]\n"
"s_add_u32 m0, %[m0x], s63\n"
"s_add_u32 s67, %[x0], s56\n"
"s_add_u32 s75, s67, 9216\n"
"buffer_load_dwordx4 %[dX], %[rX], s67 offen offset:0 lds\n"
"v_mfma_f32_16x16x32_bf16 v[148:151], a[236:239], a[240:243], v[148:151]\n"
"ds_read_b64_tr_b16 a[228:229], v239 offset:0\n"
"v_exp_f32 v183, v183\n"
"v_mfma_f32_16x16x32_bf16 v[152:155], a[236:239], a[244:247], v[152:155]\n"
"ds_read_b64_tr_b16 a[230:231], v239 offset:6144\n"
"v_cvt_pk_bf16_f32 v192, v168, v169\n"
"v_cvt_pk_bf16_f32 v193, v170, v171\n"
"v_cvt_pk_bf16_f32 v194, v180, v181\n"
"v_mfma_f32_16x16x32_bf16 v[156:159], a[236:239], a[248:251], v[156:159]\n"
"buffer_load_dwordx4 %[dX], %[rX], s75 offen offset:1024 lds\n"
"s_add_u32 s75, s67, 18432\n"
"v_cvt_pk_bf16_f32 v195, v182, v183\n"
"s_waitcnt lgkmcnt(6)\n"
"v_mfma_f32_16x16x32_bf16 a[0:3], a[216:219], v[184:187], a[0:3]\n"
"ds_read_b64_tr_b16 a[232:233], v236 offset:128\n"
"v_mfma_f32_16x16x32_bf16 a[4:7], a[216:219], v[188:191], a[4:7]\n"
"ds_read_b64_tr_b16 a[234:235], v236 offset:6272\n"
"v_mfma_f32_16x16x32_bf16 a[8:11], a[216:219], v[192:195], a[8:11]\n"
"buffer_load_dwordx4 %[dX], %[rX], s75 offen offset:2048 lds\n"
"s_add_u32 s56, s56, 128\n"
"s_cmp_eq_u32 s56, 1280\n"
"s_cselect_b32 s56, 0, s56\n"
"s_add_u32 s63, s63, 12288\n"
"s_cmp_eq_u32 s63, 36864\n"
"s_cselect_b32 s63, 0, s63\n"
"s_waitcnt lgkmcnt(6)\n"
"v_mfma_f32_16x16x32_bf16 a[12:15], a[220:223], v[184:187], a[12:15]\n"
"ds_read_b64_tr_b16 a[236:237], v237 offset:128\n"
"v_mfma_f32_16x16x32_bf16 a[16:19], a[220:223], v[188:191], a[16:19]\n"
"ds_read_b64_tr_b16 a[238:239], v237 offset:6272\n"
"v_mfma_f32_16x16x32_bf16 a[20:23], a[220:223], v[192:195], a[20:23]\n"
"s_mov_b32 m0, s61\n"
"s_add_u32 s68, s66, 196608\n"
"buffer_load_dwordx4 %[dW0], %[rWq], s66 offen offset:0 lds\n"
"s_waitcnt lgkmcnt(6)\n"
"v_mfma_f32_16x16x32_bf16 a[24:27], a[224:227], v[184:187], a[24:27]\n"
"ds_read_b64_tr_b16 a[216:217], v238 offset:128\n"
"v_mfma_f32_16x16x32_bf16 a[28:31], a[224:227], v[188:191], a[28:31]\n"
"ds_read_b64_tr_b16 a[218:219], v238 offset:6272\n"
"v_mfma_f32_16x16x32_bf16 a[32:35], a[224:227], v[192:195], a[32:35]\n"
"s_waitcnt lgkmcnt(6)\n"
"v_mfma_f32_16x16x32_bf16 a[36:39], a[228:231], v[184:187], a[36:39]\n"
"ds_read_b64_tr_b16 a[220:221], v239 offset:128\n"
"v_mfma_f32_16x16x32_bf16 a[40:43], a[228:231], v[188:191], a[40:43]\n"
"ds_read_b64_tr_b16 a[222:223], v239 offset:6272\n"
"v_mfma_f32_16x16x32_bf16 a[44:47], a[228:231], v[192:195], a[44:47]\n"
"buffer_load_dwordx4 %[dW1], %[rWq], s66 offen offset:1024 lds\n"
"s_waitcnt lgkmcnt(6)\n"
"v_mfma_f32_16x16x32_bf16 a[48:51], a[232:235], v[184:187], a[48:51]\n"
"ds_read_b64_tr_b16 a[224:225], v236 offset:256\n"
"v_mfma_f32_16x16x32_bf16 a[52:55], a[232:235], v[188:191], a[52:55]\n"
"ds_read_b64_tr_b16 a[226:227], v236 offset:6400\n"
"v_mfma_f32_16x16x32_bf16 a[56:59], a[232:235], v[192:195], a[56:59]\n"
"s_waitcnt lgkmcnt(6)\n"
"v_mfma_f32_16x16x32_bf16 a[60:63], a[236:239], v[184:187], a[60:63]\n"
"ds_read_b64_tr_b16 a[228:229], v237 offset:256\n"
"v_mfma_f32_16x16x32_bf16 a[64:67], a[236:239], v[188:191], a[64:67]\n"
"ds_read_b64_tr_b16 a[230:231], v237 offset:6400\n"
"v_mfma_f32_16x16x32_bf16 a[68:71], a[236:239], v[192:195], a[68:71]\n"
"buffer_load_dwordx4 %[dW2], %[rWq], s66 offen offset:2048 lds\n"
"s_add_u32 m0, s61, 3072\n"
"s_waitcnt lgkmcnt(6)\n"
"v_mfma_f32_16x16x32_bf16 a[72:75], a[216:219], v[184:187], a[72:75]\n"
"ds_read_b64_tr_b16 a[232:233], v238 offset:256\n"
"v_mfma_f32_16x16x32_bf16 a[76:79], a[216:219], v[188:191], a[76:79]\n"
"ds_read_b64_tr_b16 a[234:235], v238 offset:6400\n"
"v_mfma_f32_16x16x32_bf16 a[80:83], a[216:219], v[192:195], a[80:83]\n"
"s_add_u32 s66, s66, 393216\n"
"buffer_load_dwordx4 %[dW0], %[rWq], s68 offen offset:0 lds\n"
"s_waitcnt lgkmcnt(6)\n"
"v_mfma_f32_16x16x32_bf16 a[84:87], a[220:223], v[184:187], a[84:87]\n"
"ds_read_b64_tr_b16 a[236:237], v239 offset:256\n"
"v_mfma_f32_16x16x32_bf16 a[88:91], a[220:223], v[188:191], a[88:91]\n"
"ds_read_b64_tr_b16 a[238:239], v239 offset:6400\n"
"v_mfma_f32_16x16x32_bf16 a[92:95], a[220:223], v[192:195], a[92:95]\n"
"buffer_load_dwordx4 %[dW1], %[rWq], s68 offen offset:1024 lds\n"
"s_waitcnt lgkmcnt(6)\n"
"v_mfma_f32_16x16x32_bf16 a[96:99], a[224:227], v[184:187], a[96:99]\n"
"s_add_u32 s61, %[m0b], s60\n"
"s_add_u32 s60, s60, 12288\n"
"s_cmp_eq_u32 s60, 61440\n"
"s_cselect_b32 s60, 0, s60\n"
"v_add_u32 v236, s60, %[rdK]\n"
"v_mfma_f32_16x16x32_bf16 a[100:103], a[224:227], v[188:191], a[100:103]\n"
"v_xor_b32 v237, 64, v236\n"
"v_mfma_f32_16x16x32_bf16 a[104:107], a[224:227], v[192:195], a[104:107]\n"
"s_waitcnt vmcnt(20) lgkmcnt(0)\n"
"s_barrier\n"
"ds_read_b128 a[216:219], v236 offset:0\n"
"v_mfma_f32_16x16x32_bf16 a[108:111], a[228:231], v[184:187], a[108:111]\n"
"ds_read_b128 a[220:223], v237 offset:0\n"
"v_mfma_f32_16x16x32_bf16 a[112:115], a[228:231], v[188:191], a[112:115]\n"
"v_mfma_f32_16x16x32_bf16 a[116:119], a[228:231], v[192:195], a[116:119]\n"
"v_mfma_f32_16x16x32_bf16 a[120:123], a[232:235], v[184:187], a[120:123]\n"
"ds_read_b128 a[224:227], v236 offset:128\n"
"v_mfma_f32_16x16x32_bf16 a[124:127], a[232:235], v[188:191], a[124:127]\n"
"v_mfma_f32_16x16x32_bf16 a[128:131], a[232:235], v[192:195], a[128:131]\n"
"buffer_load_dwordx4 %[dW2], %[rWq], s68 offen offset:2048 lds\n"
"v_mfma_f32_16x16x32_bf16 a[132:135], a[236:239], v[184:187], a[132:135]\n"
"ds_read_b128 a[228:231], v237 offset:128\n"
"v_mfma_f32_16x16x32_bf16 a[136:139], a[236:239], v[188:191], a[136:139]\n"
"v_mfma_f32_16x16x32_bf16 a[140:143], a[236:239], v[192:195], a[140:143]\n"
"v_mfma_f32_16x16x32_bf16 v[208:211], v[220:223], v[184:187], v[208:211]\n"
"v_mfma_f32_16x16x32_bf16 v[212:215], v[220:223], v[188:191], v[212:215]\n"
"v_mfma_f32_16x16x32_bf16 v[216:219], v[220:223], v[192:195], v[216:219]\n"
"s_waitcnt lgkmcnt(3)\n"
"v_mfma_f32_16x16x32_bf16 v[160:163], a[216:219], a[144:147], v[196:199]\n"
"ds_read_b128 a[232:235], v236 offset:256\n"
"v_mfma_f32_16x16x32_bf16 v[164:167], a[216:219], a[148:151], v[200:203]\n"
"v_mfma_f32_16x16x32_bf16 v[168:171], a[216:219], a[152:155], v[204:207]\n"
"s_mov_b32 m0, s61\n"
"s_add_u32 s68, s65, 3072\n"
"buffer_load_dwordx4 %[dKV0], %[rV], s65 offen offset:0 nt lds\n"
"s_waitcnt lgkmcnt(3)\n"
"v_mfma_f32_16x16x32_bf16 v[160:163], a[220:223], a[156:159], v[160:163]\n"
"ds_read_b128 a[236:239], v237 offset:256\n"
"v_mfma_f32_16x16x32_bf16 v[164:167], a[220:223], a[160:163], v[164:167]\n"
"v_mfma_f32_16x16x32_bf16 v[168:171], a[220:223], a[164:167], v[168:171]\n"
"s_waitcnt lgkmcnt(3)\n"
"v_mfma_f32_16x16x32_bf16 v[160:163], a[224:227], a[168:171], v[160:163]\n"
"ds_read_b128 a[216:219], v236 offset:6144\n"
"v_mfma_f32_16x16x32_bf16 v[164:167], a[224:227], a[172:175], v[164:167]\n"
"v_mfma_f32_16x16x32_bf16 v[168:171], a[224:227], a[176:179], v[168:171]\n"
"buffer_load_dwordx4 %[dKV1], %[rV], s65 offen offset:1024 nt lds\n"
"s_waitcnt lgkmcnt(3)\n"
"v_mfma_f32_16x16x32_bf16 v[160:163], a[228:231], a[180:183], v[160:163]\n"
"ds_read_b128 a[220:223], v237 offset:6144\n"
"v_mfma_f32_16x16x32_bf16 v[164:167], a[228:231], a[184:187], v[164:167]\n"
"v_mfma_f32_16x16x32_bf16 v[168:171], a[228:231], a[188:191], v[168:171]\n"
"s_waitcnt lgkmcnt(3)\n"
"v_mfma_f32_16x16x32_bf16 v[160:163], a[232:235], a[192:195], v[160:163]\n"
"ds_read_b128 a[224:227], v236 offset:6272\n"
"v_mfma_f32_16x16x32_bf16 v[164:167], a[232:235], a[196:199], v[164:167]\n"
"v_mfma_f32_16x16x32_bf16 v[168:171], a[232:235], a[200:203], v[168:171]\n"
"buffer_load_dwordx4 %[dKV2], %[rV], s65 offen offset:2048 nt lds\n"
"s_add_u32 m0, s61, 3072\n"
"s_waitcnt lgkmcnt(3)\n"
"v_mfma_f32_16x16x32_bf16 v[160:163], a[236:239], a[204:207], v[160:163]\n"
"ds_read_b128 a[228:231], v237 offset:6272\n"
"v_mfma_f32_16x16x32_bf16 v[164:167], a[236:239], a[208:211], v[164:167]\n"
"v_mfma_f32_16x16x32_bf16 v[168:171], a[236:239], a[212:215], v[168:171]\n"
"s_waitcnt lgkmcnt(3)\n"
"v_mfma_f32_16x16x32_bf16 v[172:175], a[216:219], a[144:147], v[196:199]\n"
"ds_read_b128 a[232:235], v236 offset:6400\n"
"v_mfma_f32_16x16x32_bf16 v[176:179], a[216:219], a[148:151], v[200:203]\n"
"v_mfma_f32_16x16x32_bf16 v[180:183], a[216:219], a[152:155], v[204:207]\n"
"s_add_u32 s65, s65, 12288\n"
"buffer_load_dwordx4 %[dKV0], %[rV], s68 offen offset:0 nt lds\n"
"s_waitcnt lgkmcnt(3)\n"
"v_mfma_f32_16x16x32_bf16 v[172:175], a[220:223], a[156:159], v[172:175]\n"
"ds_read_b128 a[236:239], v237 offset:6400\n"
"v_mfma_f32_16x16x32_bf16 v[176:179], a[220:223], a[160:163], v[176:179]\n"
"v_mfma_f32_16x16x32_bf16 v[180:183], a[220:223], a[164:167], v[180:183]\n"
"buffer_load_dwordx4 %[dKV1], %[rV], s68 offen offset:1024 nt lds\n"
"s_waitcnt lgkmcnt(3)\n"
"v_mfma_f32_16x16x32_bf16 v[172:175], a[224:227], a[168:171], v[172:175]\n"
"s_add_u32 s61, %[m0b], s60\n"
"s_add_u32 s60, s60, 12288\n"
"s_cmp_eq_u32 s60, 61440\n"
"s_cselect_b32 s60, 0, s60\n"
"v_add_u32 v236, s60, %[rdW]\n"
"v_mfma_f32_16x16x32_bf16 v[176:179], a[224:227], a[172:175], v[176:179]\n"
"v_xor_b32 v237, 8, v236\n"
"v_xor_b32 v238, 64, v236\n"
"v_xor_b32 v239, 72, v236\n"
"v_xor_b32 v240, 64, v240\n"
"v_mfma_f32_16x16x32_bf16 v[180:183], a[224:227], a[176:179], v[180:183]\n"
"s_waitcnt vmcnt(20) lgkmcnt(0)\n"
"s_barrier\n"
"ds_read_b64_tr_b16 a[216:217], v236 offset:0\n"
"ds_read_b64_tr_b16 a[218:219], v236 offset:6144\n"
"v_mfma_f32_16x16x32_bf16 v[172:175], a[228:231], a[180:183], v[172:175]\n"
"ds_read_b128 v[224:227], v240 offset:0\n"
"ds_read_b64_tr_b16 a[220:221], v237 offset:0\n"
"v_mfma_f32_16x16x32_bf16 v[176:179], a[228:231], a[184:187], v[176:179]\n"
"ds_read_b64_tr_b16 a[222:223], v237 offset:6144\n"
"ds_read_b128 v[228:231], v240 offset:2048\n"
"v_mfma_f32_16x16x32_bf16 v[180:183], a[228:231], a[188:191], v[180:183]\n"
"ds_read_b128 v[232:235], v240 offset:4096\n"
"v_mfma_f32_16x16x32_bf16 v[172:175], a[232:235], a[192:195], v[172:175]\n"
"ds_read_b64_tr_b16 a[224:225], v238 offset:0\n"
"v_mfma_f32_16x16x32_bf16 v[176:179], a[232:235], a[196:199], v[176:179]\n"
"ds_read_b64_tr_b16 a[226:227], v238 offset:6144\n"
"v_mfma_f32_16x16x32_bf16 v[180:183], a[232:235], a[200:203], v[180:183]\n"
"buffer_load_dwordx4 %[dKV2], %[rV], s68 offen offset:2048 nt lds\n"
"v_mfma_f32_16x16x32_bf16 v[172:175], a[236:239], a[204:207], v[172:175]\n"
"ds_read_b64_tr_b16 a[228:229], v239 offset:0\n"
"v_mfma_f32_16x16x32_bf16 v[176:179], a[236:239], a[208:211], v[176:179]\n"
"ds_read_b64_tr_b16 a[230:231], v239 offset:6144\n"
"v_mfma_f32_16x16x32_bf16 v[180:183], a[236:239], a[212:215], v[180:183]\n"
"s_waitcnt lgkmcnt(8)\n"
"v_mfma_f32_16x16x32_bf16 v[16:19], a[216:219], v[224:227], v[16:19]\n"
"ds_read_b64_tr_b16 a[232:233], v236 offset:128\n"
"s_waitcnt lgkmcnt(6)\n"
"v_mfma_f32_16x16x32_bf16 v[20:23], a[216:219], v[228:231], v[20:23]\n"
"ds_read_b64_tr_b16 a[234:235], v236 offset:6272\n"
"s_waitcnt lgkmcnt(6)\n"
"v_mfma_f32_16x16x32_bf16 v[24:27], a[216:219], v[232:235], v[24:27]\n"
"s_mov_b32 m0, s61\n"
"s_add_u32 s64, s64, 589824\n"
"s_add_u32 s68, s64, 3072\n"
"buffer_load_dwordx4 %[dKV0], %[rK], s64 offen offset:0 nt lds\n"
"v_mfma_f32_16x16x32_bf16 v[28:31], a[220:223], v[224:227], v[28:31]\n"
"ds_read_b64_tr_b16 a[236:237], v237 offset:128\n"
"v_max3_f32 v242, v160, v161, v162\n"
"v_mfma_f32_16x16x32_bf16 v[32:35], a[220:223], v[228:231], v[32:35]\n"
"ds_read_b64_tr_b16 a[238:239], v237 offset:6272\n"
"v_max3_f32 v242, v242, v163, v164\n"
"v_max3_f32 v242, v242, v165, v166\n"
"v_max3_f32 v242, v242, v167, v168\n"
"v_mfma_f32_16x16x32_bf16 v[36:39], a[220:223], v[232:235], v[36:39]\n"
"v_max3_f32 v242, v242, v169, v170\n"
"v_max_f32 v242, v242, v171\n"
"v_max3_f32 v243, v172, v173, v174\n"
"s_waitcnt lgkmcnt(6)\n"
"v_mfma_f32_16x16x32_bf16 v[40:43], a[224:227], v[224:227], v[40:43]\n"
"ds_read_b64_tr_b16 a[216:217], v238 offset:128\n"
"v_max3_f32 v243, v243, v175, v176\n"
"v_max3_f32 v243, v243, v177, v178\n"
"v_max3_f32 v243, v243, v179, v180\n"
"v_mfma_f32_16x16x32_bf16 v[44:47], a[224:227], v[228:231], v[44:47]\n"
"ds_read_b64_tr_b16 a[218:219], v238 offset:6272\n"
"v_max3_f32 v243, v243, v181, v182\n"
"v_max3_f32 v243, v243, v183, v242\n"
"v_mfma_f32_16x16x32_bf16 v[48:51], a[224:227], v[232:235], v[48:51]\n"
"buffer_load_dwordx4 %[dKV1], %[rK], s64 offen offset:1024 nt lds\n"
"v_cmp_lt_f32 vcc, s70, v243\n"
"s_waitcnt lgkmcnt(6)\n"
"v_mfma_f32_16x16x32_bf16 v[52:55], a[228:231], v[224:227], v[52:55]\n"
"ds_read_b64_tr_b16 a[220:221], v239 offset:128\n"
"s_nop 1\n"
"s_cbranch_vccz 1\n"
"s_call_b64 s[76:77], Lslow_%=\n"
"v_mfma_f32_16x16x32_bf16 v[56:59], a[228:231], v[228:231], v[56:59]\n"
"ds_read_b64_tr_b16 a[222:223], v239 offset:6272\n"
"v_exp_f32 v160, v160\n"
"v_mfma_f32_16x16x32_bf16 v[60:63], a[228:231], v[232:235], v[60:63]\n"
"v_exp_f32 v161, v161\n"
"v_exp_f32 v162, v162\n"
"s_waitcnt lgkmcnt(6)\n"
"v_mfma_f32_16x16x32_bf16 v[64:67], a[232:235], v[224:227], v[64:67]\n"
"ds_read_b64_tr_b16 a[224:225], v236 offset:256\n"
"v_exp_f32 v163, v163\n"
"v_exp_f32 v172, v172\n"
"v_mfma_f32_16x16x32_bf16 v[68:71], a[232:235], v[228:231], v[68:71]\n"
"ds_read_b64_tr_b16 a[226:227], v236 offset:6400\n"
"v_exp_f32 v173, v173\n"
"v_mfma_f32_16x16x32_bf16 v[72:75], a[232:235], v[232:235], v[72:75]\n"
"buffer_load_dwordx4 %[dKV2], %[rK], s64 offen offset:2048 nt lds\n"
"s_add_u32 m0, s61, 3072\n"
"v_exp_f32 v174, v174\n"
"s_waitcnt lgkmcnt(6)\n"
"v_mfma_f32_16x16x32_bf16 v[76:79], a[236:239], v[224:227], v[76:79]\n"
"ds_read_b64_tr_b16 a[228:229], v237 offset:256\n"
"v_exp_f32 v175, v175\n"
"v_cvt_pk_bf16_f32 v184, v160, v161\n"
"v_mfma_f32_16x16x32_bf16 v[80:83], a[236:239], v[228:231], v[80:83]\n"
"ds_read_b64_tr_b16 a[230:231], v237 offset:6400\n"
"v_cvt_pk_bf16_f32 v185, v162, v163\n"
"v_exp_f32 v164, v164\n"
"v_mfma_f32_16x16x32_bf16 v[84:87], a[236:239], v[232:235], v[84:87]\n"
"v_exp_f32 v165, v165\n"
"v_exp_f32 v166, v166\n"
"s_waitcnt lgkmcnt(6)\n"
"v_mfma_f32_16x16x32_bf16 v[88:91], a[216:219], v[224:227], v[88:91]\n"
"ds_read_b64_tr_b16 a[232:233], v238 offset:256\n"
"v_exp_f32 v167, v167\n"
"v_cvt_pk_bf16_f32 v186, v172, v173\n"
"v_mfma_f32_16x16x32_bf16 v[92:95], a[216:219], v[228:231], v[92:95]\n"
"ds_read_b64_tr_b16 a[234:235], v238 offset:6400\n"
"v_cvt_pk_bf16_f32 v187, v174, v175\n"
"v_exp_f32 v176, v176\n"
"v_mfma_f32_16x16x32_bf16 v[96:99], a[216:219], v[232:235], v[96:99]\n"
"s_add_u32 s64, s64, 12288\n"
"buffer_load_dwordx4 %[dKV0], %[rK], s68 offen offset:0 nt lds\n"
"s_waitcnt lgkmcnt(6)\n"
"v_mfma_f32_16x16x32_bf16 v[100:103], a[220:223], v[224:227], v[100:103]\n"
"ds_read_b64_tr_b16 a[236:237], v239 offset:256\n"
"v_exp_f32 v177, v177\n"
"v_exp_f32 v178, v178\n"
"v_mfma_f32_16x16x32_bf16 v[104:107], a[220:223], v[228:231], v[104:107]\n"
"ds_read_b64_tr_b16 a[238:239], v239 offset:6400\n"
"v_exp_f32 v179, v179\n"
"v_cvt_pk_bf16_f32 v188, v164, v165\n"
"v_mfma_f32_16x16x32_bf16 v[108:111], a[220:223], v[232:235], v[108:111]\n"
"buffer_load_dwordx4 %[dKV1], %[rK], s68 offen offset:1024 nt lds\n"
"v_cvt_pk_bf16_f32 v189, v166, v167\n"
"s_waitcnt lgkmcnt(6)\n"
"v_mfma_f32_16x16x32_bf16 v[112:115], a[224:227], v[224:227], v[112:115]\n"
"s_add_u32 s61, %[m0b], s60\n"
"s_add_u32 s60, s60, 12288\n"
"s_cmp_eq_u32 s60, 61440\n"
"s_cselect_b32 s60, 0, s60\n"
"v_add_u32 v236, s60, %[rdW]\n"
"v_mfma_f32_16x16x32_bf16 v[116:119], a[224:227], v[228:231], v[116:119]\n"
"v_xor_b32 v237, 8, v236\n"
"v_xor_b32 v238, 64, v236\n"
"v_xor_b32 v239, 72, v236\n"
"v_add_u32 v240, s62, %[rdX]\n"
"s_add_u32 s62, s62, 12288\n"
"s_cmp_eq_u32 s62, 36864\n"
"s_cselect_b32 s62, 0, s62\n"
"v_mfma_f32_16x16x32_bf16 v[120:123], a[224:227], v[232:235], v[120:123]\n"
"s_waitcnt vmcnt(17) lgkmcnt(0)\n"
"s_barrier\n"
"ds_read_b64_tr_b16 a[216:217], v236 offset:0\n"
"ds_read_b64_tr_b16 a[218:219], v236 offset:6144\n"
"v_mfma_f32_16x16x32_bf16 v[124:127], a[228:231], v[224:227], v[124:127]\n"
"ds_read_b128 a[240:243], v240 offset:0\n"
"ds_read_b64_tr_b16 a[220:221], v237 offset:0\n"
"v_mfma_f32_16x16x32_bf16 v[128:131], a[228:231], v[228:231], v[128:131]\n"
"ds_read_b64_tr_b16 a[222:223], v237 offset:6144\n"
"ds_read_b128 a[244:247], v240 offset:2048\n"
"v_exp_f32 v168, v168\n"
"v_mfma_f32_16x16x32_bf16 v[132:135], a[228:231], v[232:235], v[132:135]\n"
"ds_read_b128 a[248:251], v240 offset:4096\n"
"v_exp_f32 v169, v169\n"
"v_mfma_f32_16x16x32_bf16 v[136:139], a[232:235], v[224:227], v[136:139]\n"
"ds_read_b64_tr_b16 a[224:225], v238 offset:0\n"
"v_exp_f32 v170, v170\n"
"v_exp_f32 v171, v171\n"
"v_mfma_f32_16x16x32_bf16 v[140:143], a[232:235], v[228:231], v[140:143]\n"
"ds_read_b64_tr_b16 a[226:227], v238 offset:6144\n"
"v_cvt_pk_bf16_f32 v190, v176, v177\n"
"v_cvt_pk_bf16_f32 v191, v178, v179\n"
"v_mfma_f32_16x16x32_bf16 v[144:147], a[232:235], v[232:235], v[144:147]\n"
"buffer_load_dwordx4 %[dKV2], %[rK], s68 offen offset:2048 nt lds\n"
"v_exp_f32 v180, v180\n"
"v_mfma_f32_16x16x32_bf16 v[148:151], a[236:239], v[224:227], v[148:151]\n"
"ds_read_b64_tr_b16 a[228:229], v239 offset:0\n"
"v_exp_f32 v181, v181\n"
"v_exp_f32 v182, v182\n"
"v_mfma_f32_16x16x32_bf16 v[152:155], a[236:239], v[228:231], v[152:155]\n"
"ds_read_b64_tr_b16 a[230:231], v239 offset:6144\n"
"v_exp_f32 v183, v183\n"
"v_mfma_f32_16x16x32_bf16 v[156:159], a[236:239], v[232:235], v[156:159]\n"
"v_cvt_pk_bf16_f32 v192, v168, v169\n"
"v_cvt_pk_bf16_f32 v193, v170, v171\n"
"v_cvt_pk_bf16_f32 v194, v180, v181\n"
"v_cvt_pk_bf16_f32 v195, v182, v183\n"
"s_waitcnt lgkmcnt(8)\n"
"v_mfma_f32_16x16x32_bf16 v[16:19], a[216:219], a[240:243], v[16:19]\n"
"ds_read_b64_tr_b16 a[232:233], v236 offset:128\n"
"s_waitcnt lgkmcnt(6)\n"
"v_mfma_f32_16x16x32_bf16 v[20:23], a[216:219], a[244:247], v[20:23]\n"
"ds_read_b64_tr_b16 a[234:235], v236 offset:6272\n"
"s_waitcnt lgkmcnt(6)\n"
"v_mfma_f32_16x16x32_bf16 v[24:27], a[216:219], a[248:251], v[24:27]\n"
"s_mov_b32 m0, s61\n"
"s_add_u32 s65, s65, 589824\n"
"s_add_u32 s68, s65, 3072\n"
"buffer_load_dwordx4 %[dKV0], %[rV], s65 offen offset:0 nt lds\n"
"v_mfma_f32_16x16x32_bf16 v[28:31], a[220:223], a[240:243], v[28:31]\n"
"ds_read_b64_tr_b16 a[236:237], v237 offset:128\n"
"v_mfma_f32_16x16x32_bf16 v[32:35], a[220:223], a[244:247], v[32:35]\n"
"ds_read_b64_tr_b16 a[238:239], v237 offset:6272\n"
"v_mfma_f32_16x16x32_bf16 v[36:39], a[220:223], a[248:251], v[36:39]\n"
"s_waitcnt lgkmcnt(6)\n"
"v_mfma_f32_16x16x32_bf16 v[40:43], a[224:227], a[240:243], v[40:43]\n"
"ds_read_b64_tr_b16 a[216:217], v238 offset:128\n"
"v_mfma_f32_16x16x32_bf16 v[44:47], a[224:227], a[244:247], v[44:47]\n"
"ds_read_b64_tr_b16 a[218:219], v238 offset:6272\n"
"v_mfma_f32_16x16x32_bf16 v[48:51], a[224:227], a[248:251], v[48:51]\n"
"buffer_load_dwordx4 %[dKV1], %[rV], s65 offen offset:1024 nt lds\n"
"s_waitcnt lgkmcnt(6)\n"
"v_mfma_f32_16x16x32_bf16 v[52:55], a[228:231], a[240:243], v[52:55]\n"
"ds_read_b64_tr_b16 a[220:221], v239 offset:128\n"
"v_mfma_f32_16x16x32_bf16 v[56:59], a[228:231], a[244:247], v[56:59]\n"
"ds_read_b64_tr_b16 a[222:223], v239 offset:6272\n"
"v_mfma_f32_16x16x32_bf16 v[60:63], a[228:231], a[248:251], v[60:63]\n"
"s_waitcnt lgkmcnt(6)\n"
"v_mfma_f32_16x16x32_bf16 v[64:67], a[232:235], a[240:243], v[64:67]\n"
"ds_read_b64_tr_b16 a[224:225], v236 offset:256\n"
"v_mfma_f32_16x16x32_bf16 v[68:71], a[232:235], a[244:247], v[68:71]\n"
"ds_read_b64_tr_b16 a[226:227], v236 offset:6400\n"
"v_mfma_f32_16x16x32_bf16 v[72:75], a[232:235], a[248:251], v[72:75]\n"
"buffer_load_dwordx4 %[dKV2], %[rV], s65 offen offset:2048 nt lds\n"
"s_add_u32 m0, s61, 3072\n"
"s_waitcnt lgkmcnt(6)\n"
"v_mfma_f32_16x16x32_bf16 v[76:79], a[236:239], a[240:243], v[76:79]\n"
"ds_read_b64_tr_b16 a[228:229], v237 offset:256\n"
"v_mfma_f32_16x16x32_bf16 v[80:83], a[236:239], a[244:247], v[80:83]\n"
"ds_read_b64_tr_b16 a[230:231], v237 offset:6400\n"
"v_mfma_f32_16x16x32_bf16 v[84:87], a[236:239], a[248:251], v[84:87]\n"
"s_waitcnt lgkmcnt(6)\n"
"v_mfma_f32_16x16x32_bf16 v[88:91], a[216:219], a[240:243], v[88:91]\n"
"ds_read_b64_tr_b16 a[232:233], v238 offset:256\n"
"v_mfma_f32_16x16x32_bf16 v[92:95], a[216:219], a[244:247], v[92:95]\n"
"ds_read_b64_tr_b16 a[234:235], v238 offset:6400\n"
"v_mfma_f32_16x16x32_bf16 v[96:99], a[216:219], a[248:251], v[96:99]\n"
"s_add_u32 s65, s65, 12288\n"
"buffer_load_dwordx4 %[dKV0], %[rV], s68 offen offset:0 nt lds\n"
"s_waitcnt lgkmcnt(6)\n"
"v_mfma_f32_16x16x32_bf16 v[100:103], a[220:223], a[240:243], v[100:103]\n"
"ds_read_b64_tr_b16 a[236:237], v239 offset:256\n"
"v_mfma_f32_16x16x32_bf16 v[104:107], a[220:223], a[244:247], v[104:107]\n"
"ds_read_b64_tr_b16 a[238:239], v239 offset:6400\n"
"v_mfma_f32_16x16x32_bf16 v[108:111], a[220:223], a[248:251], v[108:111]\n"
"buffer_load_dwordx4 %[dKV1], %[rV], s68 offen offset:1024 nt lds\n"
"s_waitcnt lgkmcnt(6)\n"
"v_mfma_f32_16x16x32_bf16 v[112:115], a[224:227], a[240:243], v[112:115]\n"
"s_add_u32 s61, %[m0b], s60\n"
"s_add_u32 s60, s60, 12288\n"
"s_cmp_eq_u32 s60, 61440\n"
"s_cselect_b32 s60, 0, s60\n"
"v_add_u32 v236, s60, %[rdW]\n"
"v_mfma_f32_16x16x32_bf16 v[116:119], a[224:227], a[244:247], v[116:119]\n"
"v_xor_b32 v237, 8, v236\n"
"v_xor_b32 v238, 64, v236\n"
"v_xor_b32 v239, 72, v236\n"
"v_xor_b32 v240, 64, v240\n"
"v_mfma_f32_16x16x32_bf16 v[120:123], a[224:227], a[248:251], v[120:123]\n"
"s_waitcnt vmcnt(17) lgkmcnt(0)\n"
"s_barrier\n"
"ds_read_b64_tr_b16 a[216:217], v236 offset:0\n"
"ds_read_b64_tr_b16 a[218:219], v236 offset:6144\n"
"v_mfma_f32_16x16x32_bf16 v[124:127], a[228:231], a[240:243], v[124:127]\n"
"ds_read_b128 v[224:227], v240 offset:0\n"
"ds_read_b64_tr_b16 a[220:221], v237 offset:0\n"
"v_mfma_f32_16x16x32_bf16 v[128:131], a[228:231], a[244:247], v[128:131]\n"
"ds_read_b64_tr_b16 a[222:223], v237 offset:6144\n"
"ds_read_b128 v[228:231], v240 offset:2048\n"
"v_mfma_f32_16x16x32_bf16 v[132:135], a[228:231], a[248:251], v[132:135]\n"
"ds_read_b128 v[232:235], v240 offset:4096\n"
"v_mfma_f32_16x16x32_bf16 v[136:139], a[232:235], a[240:243], v[136:139]\n"
"ds_read_b64_tr_b16 a[224:225], v238 offset:0\n"
"v_mfma_f32_16x16x32_bf16 v[140:143], a[232:235], a[244:247], v[140:143]\n"
"ds_read_b64_tr_b16 a[226:227], v238 offset:6144\n"
"v_mfma_f32_16x16x32_bf16 v[144:147], a[232:235], a[248:251], v[144:147]\n"
"buffer_load_dwordx4 %[dKV2], %[rV], s68 offen offset:2048 nt lds\n"
"v_mfma_f32_16x16x32_bf16 v[148:151], a[236:239], a[240:243], v[148:151]\n"
"ds_read_b64_tr_b16 a[228:229], v239 offset:0\n"
"v_mfma_f32_16x16x32_bf16 v[152:155], a[236:239], a[244:247], v[152:155]\n"
"ds_read_b64_tr_b16 a[230:231], v239 offset:6144\n"
"v_mfma_f32_16x16x32_bf16 v[156:159], a[236:239], a[248:251], v[156:159]\n"
"s_waitcnt lgkmcnt(8)\n"
"v_mfma_f32_16x16x32_bf16 v[16:19], a[216:219], v[224:227], v[16:19]\n"
"ds_read_b64_tr_b16 a[232:233], v236 offset:128\n"
"s_waitcnt lgkmcnt(6)\n"
"v_mfma_f32_16x16x32_bf16 v[20:23], a[216:219], v[228:231], v[20:23]\n"
"ds_read_b64_tr_b16 a[234:235], v236 offset:6272\n"
"s_waitcnt lgkmcnt(6)\n"
"v_mfma_f32_16x16x32_bf16 v[24:27], a[216:219], v[232:235], v[24:27]\n"
"s_mov_b32 m0, s61\n"
"s_add_u32 s68, s64, 3072\n"
"buffer_load_dwordx4 %[dKV0], %[rK], s64 offen offset:0 nt lds\n"
"v_mfma_f32_16x16x32_bf16 v[28:31], a[220:223], v[224:227], v[28:31]\n"
"ds_read_b64_tr_b16 a[236:237], v237 offset:128\n"
"v_mfma_f32_16x16x32_bf16 v[32:35], a[220:223], v[228:231], v[32:35]\n"
"ds_read_b64_tr_b16 a[238:239], v237 offset:6272\n"
"v_mfma_f32_16x16x32_bf16 v[36:39], a[220:223], v[232:235], v[36:39]\n"
"s_waitcnt lgkmcnt(6)\n"
"v_mfma_f32_16x16x32_bf16 v[40:43], a[224:227], v[224:227], v[40:43]\n"
"ds_read_b64_tr_b16 a[216:217], v238 offset:128\n"
"v_mfma_f32_16x16x32_bf16 v[44:47], a[224:227], v[228:231], v[44:47]\n"
"ds_read_b64_tr_b16 a[218:219], v238 offset:6272\n"
"v_mfma_f32_16x16x32_bf16 v[48:51], a[224:227], v[232:235], v[48:51]\n"
"buffer_load_dwordx4 %[dKV1], %[rK], s64 offen offset:1024 nt lds\n"
"s_waitcnt lgkmcnt(6)\n"
"v_mfma_f32_16x16x32_bf16 v[52:55], a[228:231], v[224:227], v[52:55]\n"
"ds_read_b64_tr_b16 a[220:221], v239 offset:128\n"
"v_mfma_f32_16x16x32_bf16 v[56:59], a[228:231], v[228:231], v[56:59]\n"
"ds_read_b64_tr_b16 a[222:223], v239 offset:6272\n"
"v_mfma_f32_16x16x32_bf16 v[60:63], a[228:231], v[232:235], v[60:63]\n"
"s_waitcnt lgkmcnt(6)\n"
"v_mfma_f32_16x16x32_bf16 v[64:67], a[232:235], v[224:227], v[64:67]\n"
"ds_read_b64_tr_b16 a[224:225], v236 offset:256\n"
"v_mfma_f32_16x16x32_bf16 v[68:71], a[232:235], v[228:231], v[68:71]\n"
"ds_read_b64_tr_b16 a[226:227], v236 offset:6400\n"
"v_mfma_f32_16x16x32_bf16 v[72:75], a[232:235], v[232:235], v[72:75]\n"
"buffer_load_dwordx4 %[dKV2], %[rK], s64 offen offset:2048 nt lds\n"
"s_add_u32 m0, s61, 3072\n"
"s_waitcnt lgkmcnt(6)\n"
"v_mfma_f32_16x16x32_bf16 v[76:79], a[236:239], v[224:227], v[76:79]\n"
"ds_read_b64_tr_b16 a[228:229], v237 offset:256\n"
"v_mfma_f32_16x16x32_bf16 v[80:83], a[236:239], v[228:231], v[80:83]\n"
"ds_read_b64_tr_b16 a[230:231], v237 offset:6400\n"
"v_mfma_f32_16x16x32_bf16 v[84:87], a[236:239], v[232:235], v[84:87]\n"
"s_waitcnt lgkmcnt(6)\n"
"v_mfma_f32_16x16x32_bf16 v[88:91], a[216:219], v[224:227], v[88:91]\n"
"ds_read_b64_tr_b16 a[232:233], v238 offset:256\n"
"v_mfma_f32_16x16x32_bf16 v[92:95], a[216:219], v[228:231], v[92:95]\n"
"ds_read_b64_tr_b16 a[234:235], v238 offset:6400\n"
"v_mfma_f32_16x16x32_bf16 v[96:99], a[216:219], v[232:235], v[96:99]\n"
"s_add_u32 s64, s64, 12288\n"
"buffer_load_dwordx4 %[dKV0], %[rK], s68 offen offset:0 nt lds\n"
"s_waitcnt lgkmcnt(6)\n"
"v_mfma_f32_16x16x32_bf16 v[100:103], a[220:223], v[224:227], v[100:103]\n"
"ds_read_b64_tr_b16 a[236:237], v239 offset:256\n"
"v_mfma_f32_16x16x32_bf16 v[104:107], a[220:223], v[228:231], v[104:107]\n"
"ds_read_b64_tr_b16 a[238:239], v239 offset:6400\n"
"v_mfma_f32_16x16x32_bf16 v[108:111], a[220:223], v[232:235], v[108:111]\n"
"buffer_load_dwordx4 %[dKV1], %[rK], s68 offen offset:1024 nt lds\n"
"s_waitcnt lgkmcnt(6)\n"
"v_mfma_f32_16x16x32_bf16 v[112:115], a[224:227], v[224:227], v[112:115]\n"
"s_add_u32 s61, %[m0b], s60\n"
"s_add_u32 s60, s60, 12288\n"
"s_cmp_eq_u32 s60, 61440\n"
"s_cselect_b32 s60, 0, s60\n"
"v_add_u32 v236, s60, %[rdV]\n"
"v_mfma_f32_16x16x32_bf16 v[116:119], a[224:227], v[228:231], v[116:119]\n"
"v_xor_b32 v237, 32, v236\n"
"v_xor_b32 v238, 64, v236\n"
"v_xor_b32 v239, 96, v236\n"
"v_mfma_f32_16x16x32_bf16 v[120:123], a[224:227], v[232:235], v[120:123]\n"
"s_waitcnt vmcnt(17) lgkmcnt(0)\n"
"s_barrier\n"
"ds_read_b64_tr_b16 a[216:217], v236 offset:0\n"
"ds_read_b64_tr_b16 a[218:219], v236 offset:6144\n"
"v_mfma_f32_16x16x32_bf16 v[124:127], a[228:231], v[224:227], v[124:127]\n"
"ds_read_b64_tr_b16 a[220:221], v237 offset:0\n"
"v_mfma_f32_16x16x32_bf16 v[128:131], a[228:231], v[228:231], v[128:131]\n"
"ds_read_b64_tr_b16 a[222:223], v237 offset:6144\n"
"v_mfma_f32_16x16x32_bf16 v[132:135], a[228:231], v[232:235], v[132:135]\n"
"v_mfma_f32_16x16x32_bf16 v[136:139], a[232:235], v[224:227], v[136:139]\n"
"ds_read_b64_tr_b16 a[224:225], v238 offset:0\n"
"v_mfma_f32_16x16x32_bf16 v[140:143], a[232:235], v[228:231], v[140:143]\n"
"ds_read_b64_tr_b16 a[226:227], v238 offset:6144\n"
"v_mfma_f32_16x16x32_bf16 v[144:147], a[232:235], v[232:235], v[144:147]\n"
"buffer_load_dwordx4 %[dKV2], %[rK], s68 offen offset:2048 nt lds\n"
"v_mfma_f32_16x16x32_bf16 v[148:151], a[236:239], v[224:227], v[148:151]\n"
"ds_read_b64_tr_b16 a[228:229], v239 offset:0\n"
"v_mfma_f32_16x16x32_bf16 v[152:155], a[236:239], v[228:231], v[152:155]\n"
"ds_read_b64_tr_b16 a[230:231], v239 offset:6144\n"
"v_mfma_f32_16x16x32_bf16 v[156:159], a[236:239], v[232:235], v[156:159]\n"
"s_waitcnt lgkmcnt(6)\n"
"v_mfma_f32_16x16x32_bf16 a[0:3], a[216:219], v[184:187], a[0:3]\n"
"ds_read_b64_tr_b16 a[232:233], v236 offset:128\n"
"v_mfma_f32_16x16x32_bf16 a[4:7], a[216:219], v[188:191], a[4:7]\n"
"ds_read_b64_tr_b16 a[234:235], v236 offset:6272\n"
"v_mfma_f32_16x16x32_bf16 a[8:11], a[216:219], v[192:195], a[8:11]\n"
"s_mov_b32 m0, s61\n"
"s_add_u32 s68, s65, 3072\n"
"buffer_load_dwordx4 %[dKV0], %[rV], s65 offen offset:0 nt lds\n"
"s_waitcnt lgkmcnt(6)\n"
"v_mfma_f32_16x16x32_bf16 a[12:15], a[220:223], v[184:187], a[12:15]\n"
"ds_read_b64_tr_b16 a[236:237], v237 offset:128\n"
"v_mfma_f32_16x16x32_bf16 a[16:19], a[220:223], v[188:191], a[16:19]\n"
"ds_read_b64_tr_b16 a[238:239], v237 offset:6272\n"
"v_mfma_f32_16x16x32_bf16 a[20:23], a[220:223], v[192:195], a[20:23]\n"
"s_waitcnt lgkmcnt(6)\n"
"v_mfma_f32_16x16x32_bf16 a[24:27], a[224:227], v[184:187], a[24:27]\n"
"ds_read_b64_tr_b16 a[216:217], v238 offset:128\n"
"v_mfma_f32_16x16x32_bf16 a[28:31], a[224:227], v[188:191], a[28:31]\n"
"ds_read_b64_tr_b16 a[218:219], v238 offset:6272\n"
"v_mfma_f32_16x16x32_bf16 a[32:35], a[224:227], v[192:195], a[32:35]\n"
"buffer_load_dwordx4 %[dKV1], %[rV], s65 offen offset:1024 nt lds\n"
"s_waitcnt lgkmcnt(6)\n"
"v_mfma_f32_16x16x32_bf16 a[36:39], a[228:231], v[184:187], a[36:39]\n"
"ds_read_b64_tr_b16 a[220:221], v239 offset:128\n"
"v_mfma_f32_16x16x32_bf16 a[40:43], a[228:231], v[188:191], a[40:43]\n"
"ds_read_b64_tr_b16 a[222:223], v239 offset:6272\n"
"v_mfma_f32_16x16x32_bf16 a[44:47], a[228:231], v[192:195], a[44:47]\n"
"s_waitcnt lgkmcnt(6)\n"
"v_mfma_f32_16x16x32_bf16 a[48:51], a[232:235], v[184:187], a[48:51]\n"
"ds_read_b64_tr_b16 a[224:225], v236 offset:256\n"
"v_mfma_f32_16x16x32_bf16 a[52:55], a[232:235], v[188:191], a[52:55]\n"
"ds_read_b64_tr_b16 a[226:227], v236 offset:6400\n"
"v_mfma_f32_16x16x32_bf16 a[56:59], a[232:235], v[192:195], a[56:59]\n"
"buffer_load_dwordx4 %[dKV2], %[rV], s65 offen offset:2048 nt lds\n"
"s_add_u32 m0, s61, 3072\n"
"s_waitcnt lgkmcnt(6)\n"
"v_mfma_f32_16x16x32_bf16 a[60:63], a[236:239], v[184:187], a[60:63]\n"
"ds_read_b64_tr_b16 a[228:229], v237 offset:256\n"
"v_mfma_f32_16x16x32_bf16 a[64:67], a[236:239], v[188:191], a[64:67]\n"
"ds_read_b64_tr_b16 a[230:231], v237 offset:6400\n"
"v_mfma_f32_16x16x32_bf16 a[68:71], a[236:239], v[192:195], a[68:71]\n"
"s_waitcnt lgkmcnt(6)\n"
"v_mfma_f32_16x16x32_bf16 a[72:75], a[216:219], v[184:187], a[72:75]\n"
"ds_read_b64_tr_b16 a[232:233], v238 offset:256\n"
"v_mfma_f32_16x16x32_bf16 a[76:79], a[216:219], v[188:191], a[76:79]\n"
"ds_read_b64_tr_b16 a[234:235], v238 offset:6400\n"
"v_mfma_f32_16x16x32_bf16 a[80:83], a[216:219], v[192:195], a[80:83]\n"
"s_add_u32 s65, s65, 12288\n"
"buffer_load_dwordx4 %[dKV0], %[rV], s68 offen offset:0 nt lds\n"
"s_waitcnt lgkmcnt(6)\n"
"v_mfma_f32_16x16x32_bf16 a[84:87], a[220:223], v[184:187], a[84:87]\n"
"ds_read_b64_tr_b16 a[236:237], v239 offset:256\n"
"v_mfma_f32_16x16x32_bf16 a[88:91], a[220:223], v[188:191], a[88:91]\n"
"ds_read_b64_tr_b16 a[238:239], v239 offset:6400\n"
"v_mfma_f32_16x16x32_bf16 a[92:95], a[220:223], v[192:195], a[92:95]\n"
"buffer_load_dwordx4 %[dKV1], %[rV], s68 offen offset:1024 nt lds\n"
"s_waitcnt lgkmcnt(6)\n"
"v_mfma_f32_16x16x32_bf16 a[96:99], a[224:227], v[184:187], a[96:99]\n"
"s_add_u32 s61, %[m0b], s60\n"
"s_add_u32 s60, s60, 12288\n"
"s_cmp_eq_u32 s60, 61440\n"
"s_cselect_b32 s60, 0, s60\n"
"v_add_u32 v236, s60, %[rdK]\n"
"v_mfma_f32_16x16x32_bf16 a[100:103], a[224:227], v[188:191], a[100:103]\n"
"v_xor_b32 v237, 64, v236\n"
"v_mfma_f32_16x16x32_bf16 a[104:107], a[224:227], v[192:195], a[104:107]\n"
"s_waitcnt vmcnt(17) lgkmcnt(0)\n"
"s_barrier\n"
"ds_read_b128 a[216:219], v236 offset:0\n"
"v_mfma_f32_16x16x32_bf16 a[108:111], a[228:231], v[184:187], a[108:111]\n"
"ds_read_b128 a[220:223], v237 offset:0\n"
"v_mfma_f32_16x16x32_bf16 a[112:115], a[228:231], v[188:191], a[112:115]\n"
"v_mfma_f32_16x16x32_bf16 a[116:119], a[228:231], v[192:195], a[116:119]\n"
"v_mfma_f32_16x16x32_bf16 a[120:123], a[232:235], v[184:187], a[120:123]\n"
"ds_read_b128 a[224:227], v236 offset:128\n"
"v_mfma_f32_16x16x32_bf16 a[124:127], a[232:235], v[188:191], a[124:127]\n"
"v_mfma_f32_16x16x32_bf16 a[128:131], a[232:235], v[192:195], a[128:131]\n"
"buffer_load_dwordx4 %[dKV2], %[rV], s68 offen offset:2048 nt lds\n"
"v_mfma_f32_16x16x32_bf16 a[132:135], a[236:239], v[184:187], a[132:135]\n"
"ds_read_b128 a[228:231], v237 offset:128\n"
"v_mfma_f32_16x16x32_bf16 a[136:139], a[236:239], v[188:191], a[136:139]\n"
"v_mfma_f32_16x16x32_bf16 a[140:143], a[236:239], v[192:195], a[140:143]\n"
"v_mfma_f32_16x16x32_bf16 v[208:211], v[220:223], v[184:187], v[208:211]\n"
"v_mfma_f32_16x16x32_bf16 v[212:215], v[220:223], v[188:191], v[212:215]\n"
"v_mfma_f32_16x16x32_bf16 v[216:219], v[220:223], v[192:195], v[216:219]\n"
"v_pk_mul_f32 v[16:17], v[16:17], s[78:79] op_sel_hi:[1,0]\n"
"v_pk_mul_f32 v[18:19], v[18:19], s[78:79] op_sel_hi:[1,0]\n"
"v_pk_mul_f32 v[20:21], v[20:21], s[78:79] op_sel_hi:[1,0]\n"
"v_pk_mul_f32 v[22:23], v[22:23], s[78:79] op_sel_hi:[1,0]\n"
"v_pk_mul_f32 v[24:25], v[24:25], s[78:79] op_sel_hi:[1,0]\n"
"v_pk_mul_f32 v[26:27], v[26:27], s[78:79] op_sel_hi:[1,0]\n"
"v_pk_mul_f32 v[28:29], v[28:29], s[78:79] op_sel_hi:[1,0]\n"
"v_pk_mul_f32 v[30:31], v[30:31], s[78:79] op_sel_hi:[1,0]\n"
"v_pk_mul_f32 v[32:33], v[32:33], s[78:79] op_sel_hi:[1,0]\n"
"v_pk_mul_f32 v[34:35], v[34:35], s[78:79] op_sel_hi:[1,0]\n"
"v_pk_mul_f32 v[36:37], v[36:37], s[78:79] op_sel_hi:[1,0]\n"
"v_pk_mul_f32 v[38:39], v[38:39], s[78:79] op_sel_hi:[1,0]\n"
"v_pk_mul_f32 v[40:41], v[40:41], s[78:79] op_sel_hi:[1,0]\n"
"v_pk_mul_f32 v[42:43], v[42:43], s[78:79] op_sel_hi:[1,0]\n"
"v_pk_mul_f32 v[44:45], v[44:45], s[78:79] op_sel_hi:[1,0]\n"
"v_pk_mul_f32 v[46:47], v[46:47], s[78:79] op_sel_hi:[1,0]\n"
"v_pk_mul_f32 v[48:49], v[48:49], s[78:79] op_sel_hi:[1,0]\n"
"v_pk_mul_f32 v[50:51], v[50:51], s[78:79] op_sel_hi:[1,0]\n"
"v_pk_mul_f32 v[52:53], v[52:53], s[78:79] op_sel_hi:[1,0]\n"
"v_pk_mul_f32 v[54:55], v[54:55], s[78:79] op_sel_hi:[1,0]\n"
"v_pk_mul_f32 v[56:57], v[56:57], s[78:79] op_sel_hi:[1,0]\n"
"v_pk_mul_f32 v[58:59], v[58:59], s[78:79] op_sel_hi:[1,0]\n"
"v_pk_mul_f32 v[60:61], v[60:61], s[78:79] op_sel_hi:[1,0]\n"
"v_pk_mul_f32 v[62:63], v[62:63], s[78:79] op_sel_hi:[1,0]\n"
"v_pk_mul_f32 v[64:65], v[64:65], s[78:79] op_sel_hi:[1,0]\n"
"v_pk_mul_f32 v[66:67], v[66:67], s[78:79] op_sel_hi:[1,0]\n"
"v_pk_mul_f32 v[68:69], v[68:69], s[78:79] op_sel_hi:[1,0]\n"
"v_pk_mul_f32 v[70:71], v[70:71], s[78:79] op_sel_hi:[1,0]\n"
"v_pk_mul_f32 v[72:73], v[72:73], s[78:79] op_sel_hi:[1,0]\n"
"v_pk_mul_f32 v[74:75], v[74:75], s[78:79] op_sel_hi:[1,0]\n"
"v_pk_mul_f32 v[76:77], v[76:77], s[78:79] op_sel_hi:[1,0]\n"
"v_pk_mul_f32 v[78:79], v[78:79], s[78:79] op_sel_hi:[1,0]\n"
"v_pk_mul_f32 v[80:81], v[80:81], s[78:79] op_sel_hi:[1,0]\n"
"v_pk_mul_f32 v[82:83], v[82:83], s[78:79] op_sel_hi:[1,0]\n"
"v_pk_mul_f32 v[84:85], v[84:85], s[78:79] op_sel_hi:[1,0]\n"
"v_pk_mul_f32 v[86:87], v[86:87], s[78:79] op_sel_hi:[1,0]\n"
"v_pk_mul_f32 v[88:89], v[88:89], s[78:79] op_sel_hi:[1,0]\n"
"v_pk_mul_f32 v[90:91], v[90:91], s[78:79] op_sel_hi:[1,0]\n"
"v_pk_mul_f32 v[92:93], v[92:93], s[78:79] op_sel_hi:[1,0]\n"
"v_pk_mul_f32 v[94:95], v[94:95], s[78:79] op_sel_hi:[1,0]\n"
"v_pk_mul_f32 v[96:97], v[96:97], s[78:79] op_sel_hi:[1,0]\n"
"v_pk_mul_f32 v[98:99], v[98:99], s[78:79] op_sel_hi:[1,0]\n"
"v_pk_mul_f32 v[100:101], v[100:101], s[78:79] op_sel_hi:[1,0]\n"
"v_pk_mul_f32 v[102:103], v[102:103], s[78:79] op_sel_hi:[1,0]\n"
"v_pk_mul_f32 v[104:105], v[104:105], s[78:79] op_sel_hi:[1,0]\n"
"v_pk_mul_f32 v[106:107], v[106:107], s[78:79] op_sel_hi:[1,0]\n"
"v_pk_mul_f32 v[108:109], v[108:109], s[78:79] op_sel_hi:[1,0]\n"
"v_pk_mul_f32 v[110:111], v[110:111], s[78:79] op_sel_hi:[1,0]\n"
"v_pk_mul_f32 v[112:113], v[112:113], s[78:79] op_sel_hi:[1,0]\n"
"v_pk_mul_f32 v[114:115], v[114:115], s[78:79] op_sel_hi:[1,0]\n"
"v_pk_mul_f32 v[116:117], v[116:117], s[78:79] op_sel_hi:[1,0]\n"
"v_pk_mul_f32 v[118:119], v[118:119], s[78:79] op_sel_hi:[1,0]\n"
"v_pk_mul_f32 v[120:121], v[120:121], s[78:79] op_sel_hi:[1,0]\n"
"v_pk_mul_f32 v[122:123], v[122:123], s[78:79] op_sel_hi:[1,0]\n"
"v_pk_mul_f32 v[124:125], v[124:125], s[78:79] op_sel_hi:[1,0]\n"
"v_pk_mul_f32 v[126:127], v[126:127], s[78:79] op_sel_hi:[1,0]\n"
"v_pk_mul_f32 v[128:129], v[128:129], s[78:79] op_sel_hi:[1,0]\n"
"v_pk_mul_f32 v[130:131], v[130:131], s[78:79] op_sel_hi:[1,0]\n"
"v_pk_mul_f32 v[132:133], v[132:133], s[78:79] op_sel_hi:[1,0]\n"
"v_pk_mul_f32 v[134:135], v[134:135], s[78:79] op_sel_hi:[1,0]\n"
"v_pk_mul_f32 v[136:137], v[136:137], s[78:79] op_sel_hi:[1,0]\n"
"v_pk_mul_f32 v[138:139], v[138:139], s[78:79] op_sel_hi:[1,0]\n"
"v_pk_mul_f32 v[140:141], v[140:141], s[78:79] op_sel_hi:[1,0]\n"
"v_pk_mul_f32 v[142:143], v[142:143], s[78:79] op_sel_hi:[1,0]\n"
"v_pk_mul_f32 v[144:145], v[144:145], s[78:79] op_sel_hi:[1,0]\n"
"v_pk_mul_f32 v[146:147], v[146:147], s[78:79] op_sel_hi:[1,0]\n"
"v_pk_mul_f32 v[148:149], v[148:149], s[78:79] op_sel_hi:[1,0]\n"
"v_pk_mul_f32 v[150:151], v[150:151], s[78:79] op_sel_hi:[1,0]\n"
"v_pk_mul_f32 v[152:153], v[152:153], s[78:79] op_sel_hi:[1,0]\n"
"v_pk_mul_f32 v[154:155], v[154:155], s[78:79] op_sel_hi:[1,0]\n"
"v_pk_mul_f32 v[156:157], v[156:157], s[78:79] op_sel_hi:[1,0]\n"
"v_pk_mul_f32 v[158:159], v[158:159], s[78:79] op_sel_hi:[1,0]\n"
"v_cvt_pk_bf16_f32 v16, v16, v17\n"
"v_cvt_pk_bf16_f32 v17, v18, v19\n"
"v_cvt_pk_bf16_f32 v20, v20, v21\n"
"v_cvt_pk_bf16_f32 v21, v22, v23\n"
"v_cvt_pk_bf16_f32 v24, v24, v25\n"
"v_cvt_pk_bf16_f32 v25, v26, v27\n"
"v_cvt_pk_bf16_f32 v28, v28, v29\n"
"v_cvt_pk_bf16_f32 v29, v30, v31\n"
"v_cvt_pk_bf16_f32 v32, v32, v33\n"
"v_cvt_pk_bf16_f32 v33, v34, v35\n"
"v_cvt_pk_bf16_f32 v36, v36, v37\n"
"v_cvt_pk_bf16_f32 v37, v38, v39\n"
"v_cvt_pk_bf16_f32 v40, v40, v41\n"
"v_cvt_pk_bf16_f32 v41, v42, v43\n"
"v_cvt_pk_bf16_f32 v44, v44, v45\n"
"v_cvt_pk_bf16_f32 v45, v46, v47\n"
"v_cvt_pk_bf16_f32 v48, v48, v49\n"
"v_cvt_pk_bf16_f32 v49, v50, v51\n"
"v_cvt_pk_bf16_f32 v52, v52, v53\n"
"v_cvt_pk_bf16_f32 v53, v54, v55\n"
"v_cvt_pk_bf16_f32 v56, v56, v57\n"
"v_cvt_pk_bf16_f32 v57, v58, v59\n"
"v_cvt_pk_bf16_f32 v60, v60, v61\n"
"v_cvt_pk_bf16_f32 v61, v62, v63\n"
"v_cvt_pk_bf16_f32 v64, v64, v65\n"
"v_cvt_pk_bf16_f32 v65, v66, v67\n"
"v_cvt_pk_bf16_f32 v68, v68, v69\n"
"v_cvt_pk_bf16_f32 v69, v70, v71\n"
"v_cvt_pk_bf16_f32 v72, v72, v73\n"
"v_cvt_pk_bf16_f32 v73, v74, v75\n"
"v_cvt_pk_bf16_f32 v76, v76, v77\n"
"v_cvt_pk_bf16_f32 v77, v78, v79\n"
"v_cvt_pk_bf16_f32 v80, v80, v81\n"
"v_cvt_pk_bf16_f32 v81, v82, v83\n"
"v_cvt_pk_bf16_f32 v84, v84, v85\n"
"v_cvt_pk_bf16_f32 v85, v86, v87\n"
"v_cvt_pk_bf16_f32 v88, v88, v89\n"
"v_cvt_pk_bf16_f32 v89, v90, v91\n"
"v_cvt_pk_bf16_f32 v92, v92, v93\n"
"v_cvt_pk_bf16_f32 v93, v94, v95\n"
"v_cvt_pk_bf16_f32 v96, v96, v97\n"
"v_cvt_pk_bf16_f32 v97, v98, v99\n"
"v_cvt_pk_bf16_f32 v100, v100, v101\n"
"v_cvt_pk_bf16_f32 v101, v102, v103\n"
"v_cvt_pk_bf16_f32 v104, v104, v105\n"
"v_cvt_pk_bf16_f32 v105, v106, v107\n"
"v_cvt_pk_bf16_f32 v108, v108, v109\n"
"v_cvt_pk_bf16_f32 v109, v110, v111\n"
"v_cvt_pk_bf16_f32 v112, v112, v113\n"
"v_cvt_pk_bf16_f32 v113, v114, v115\n"
"v_cvt_pk_bf16_f32 v116, v116, v117\n"
"v_cvt_pk_bf16_f32 v117, v118, v119\n"
"v_cvt_pk_bf16_f32 v120, v120, v121\n"
"v_cvt_pk_bf16_f32 v121, v122, v123\n"
"v_cvt_pk_bf16_f32 v124, v124, v125\n"
"v_cvt_pk_bf16_f32 v125, v126, v127\n"
"v_cvt_pk_bf16_f32 v128, v128, v129\n"
"v_cvt_pk_bf16_f32 v129, v130, v131\n"
"v_cvt_pk_bf16_f32 v132, v132, v133\n"
"v_cvt_pk_bf16_f32 v133, v134, v135\n"
"v_cvt_pk_bf16_f32 v136, v136, v137\n"
"v_cvt_pk_bf16_f32 v137, v138, v139\n"
"v_cvt_pk_bf16_f32 v140, v140, v141\n"
"v_cvt_pk_bf16_f32 v141, v142, v143\n"
"v_cvt_pk_bf16_f32 v144, v144, v145\n"
"v_cvt_pk_bf16_f32 v145, v146, v147\n"
"v_cvt_pk_bf16_f32 v148, v148, v149\n"
"v_cvt_pk_bf16_f32 v149, v150, v151\n"
"v_cvt_pk_bf16_f32 v152, v152, v153\n"
"v_cvt_pk_bf16_f32 v153, v154, v155\n"
"v_cvt_pk_bf16_f32 v156, v156, v157\n"
"v_cvt_pk_bf16_f32 v157, v158, v159\n"
"s_mov_b64 exec, s[80:81]\n"
"s_nop 0\n"
"v_swap_b32 v16, v28\n"
"v_swap_b32 v17, v29\n"
"v_swap_b32 v20, v32\n"
"v_swap_b32 v21, v33\n"
"v_swap_b32 v24, v36\n"
"v_swap_b32 v25, v37\n"
"v_swap_b32 v40, v52\n"
"v_swap_b32 v41, v53\n"
"v_swap_b32 v44, v56\n"
"v_swap_b32 v45, v57\n"
"v_swap_b32 v48, v60\n"
"v_swap_b32 v49, v61\n"
"v_swap_b32 v64, v76\n"
"v_swap_b32 v65, v77\n"
"v_swap_b32 v68, v80\n"
"v_swap_b32 v69, v81\n"
"v_swap_b32 v72, v84\n"
"v_swap_b32 v73, v85\n"
"v_swap_b32 v88, v100\n"
"v_swap_b32 v89, v101\n"
"v_swap_b32 v92, v104\n"
"v_swap_b32 v93, v105\n"
"v_swap_b32 v96, v108\n"
"v_swap_b32 v97, v109\n"
"v_swap_b32 v112, v124\n"
"v_swap_b32 v113, v125\n"
"v_swap_b32 v116, v128\n"
"v_swap_b32 v117, v129\n"
"v_swap_b32 v120, v132\n"
"v_swap_b32 v121, v133\n"
"v_swap_b32 v136, v148\n"
"v_swap_b32 v137, v149\n"
"v_swap_b32 v140, v152\n"
"v_swap_b32 v141, v153\n"
"v_swap_b32 v144, v156\n"
"v_swap_b32 v145, v157\n"
"s_mov_b64 exec, -1\n"
"s_nop 0\n"
"v_accvgpr_write_b32 a144, v16\n"
"v_accvgpr_write_b32 a145, v17\n"
"v_accvgpr_write_b32 a148, v20\n"
"v_accvgpr_write_b32 a149, v21\n"
"v_accvgpr_write_b32 a152, v24\n"
"v_accvgpr_write_b32 a153, v25\n"
"v_accvgpr_write_b32 a146, v28\n"
"v_accvgpr_write_b32 a147, v29\n"
"v_accvgpr_write_b32 a150, v32\n"
"v_accvgpr_write_b32 a151, v33\n"
"v_accvgpr_write_b32 a154, v36\n"
"v_accvgpr_write_b32 a155, v37\n"
"v_accvgpr_write_b32 a156, v40\n"
"v_accvgpr_write_b32 a157, v41\n"
"v_accvgpr_write_b32 a160, v44\n"
"v_accvgpr_write_b32 a161, v45\n"
"v_accvgpr_write_b32 a164, v48\n"
"v_accvgpr_write_b32 a165, v49\n"
"v_accvgpr_write_b32 a158, v52\n"
"v_accvgpr_write_b32 a159, v53\n"
"v_accvgpr_write_b32 a162, v56\n"
"v_accvgpr_write_b32 a163, v57\n"
"v_accvgpr_write_b32 a166, v60\n"
"v_accvgpr_write_b32 a167, v61\n"
"v_accvgpr_write_b32 a168, v64\n"
"v_accvgpr_write_b32 a169, v65\n"
"v_accvgpr_write_b32 a172, v68\n"
"v_accvgpr_write_b32 a173, v69\n"
"v_accvgpr_write_b32 a176, v72\n"
"v_accvgpr_write_b32 a177, v73\n"
"v_accvgpr_write_b32 a170, v76\n"
"v_accvgpr_write_b32 a171, v77\n"
"v_accvgpr_write_b32 a174, v80\n"
"v_accvgpr_write_b32 a175, v81\n"
"v_accvgpr_write_b32 a178, v84\n"
"v_accvgpr_write_b32 a179, v85\n"
"v_accvgpr_write_b32 a180, v88\n"
"v_accvgpr_write_b32 a181, v89\n"
"v_accvgpr_write_b32 a184, v92\n"
"v_accvgpr_write_b32 a185, v93\n"
"v_accvgpr_write_b32 a188, v96\n"
"v_accvgpr_write_b32 a189, v97\n"
"v_accvgpr_write_b32 a182, v100\n"
"v_accvgpr_write_b32 a183, v101\n"
"v_accvgpr_write_b32 a186, v104\n"
"v_accvgpr_write_b32 a187, v105\n"
"v_accvgpr_write_b32 a190, v108\n"
"v_accvgpr_write_b32 a191, v109\n"
"v_accvgpr_write_b32 a192, v112\n"
"v_accvgpr_write_b32 a193, v113\n"
"v_accvgpr_write_b32 a196, v116\n"
"v_accvgpr_write_b32 a197, v117\n"
"v_accvgpr_write_b32 a200, v120\n"
"v_accvgpr_write_b32 a201, v121\n"
"v_accvgpr_write_b32 a194, v124\n"
"v_accvgpr_write_b32 a195, v125\n"
"v_accvgpr_write_b32 a198, v128\n"
"v_accvgpr_write_b32 a199, v129\n"
"v_accvgpr_write_b32 a202, v132\n"
"v_accvgpr_write_b32 a203, v133\n"
"v_accvgpr_write_b32 a204, v136\n"
"v_accvgpr_write_b32 a205, v137\n"
"v_accvgpr_write_b32 a208, v140\n"
"v_accvgpr_write_b32 a209, v141\n"
"v_accvgpr_write_b32 a212, v144\n"
"v_accvgpr_write_b32 a213, v145\n"
"v_accvgpr_write_b32 a206, v148\n"
"v_accvgpr_write_b32 a207, v149\n"
"v_accvgpr_write_b32 a210, v152\n"
"v_accvgpr_write_b32 a211, v153\n"
"v_accvgpr_write_b32 a214, v156\n"
"v_accvgpr_write_b32 a215, v157\n"
"v_rcp_f32 v250, v208\n"
"v_rcp_f32 v252, v212\n"
"v_rcp_f32 v254, v216\n"
"v_accvgpr_read_b32 v242, a0\n"
"v_accvgpr_read_b32 v243, a1\n"
"v_accvgpr_read_b32 v244, a2\n"
"v_accvgpr_read_b32 v245, a3\n"
"v_pk_mul_f32 v[242:243], v[242:243], v[250:251] op_sel_hi:[1,0]\n"
"v_pk_mul_f32 v[244:245], v[244:245], v[250:251] op_sel_hi:[1,0]\n"
"v_accvgpr_read_b32 v246, a4\n"
"v_accvgpr_read_b32 v247, a5\n"
"v_accvgpr_read_b32 v248, a6\n"
"v_accvgpr_read_b32 v249, a7\n"
"v_cvt_pk_bf16_f32 v16, v242, v243\n"
"v_cvt_pk_bf16_f32 v17, v244, v245\n"
"v_pk_mul_f32 v[246:247], v[246:247], v[252:253] op_sel_hi:[1,0]\n"
"v_pk_mul_f32 v[248:249], v[248:249], v[252:253] op_sel_hi:[1,0]\n"
"v_accvgpr_read_b32 v242, a8\n"
"v_accvgpr_read_b32 v243, a9\n"
"v_accvgpr_read_b32 v244, a10\n"
"v_accvgpr_read_b32 v245, a11\n"
"v_cvt_pk_bf16_f32 v20, v246, v247\n"
"v_cvt_pk_bf16_f32 v21, v248, v249\n"
"v_pk_mul_f32 v[242:243], v[242:243], v[254:255] op_sel_hi:[1,0]\n"
"v_pk_mul_f32 v[244:245], v[244:245], v[254:255] op_sel_hi:[1,0]\n"
"v_accvgpr_read_b32 v246, a12\n"
"v_accvgpr_read_b32 v247, a13\n"
"v_accvgpr_read_b32 v248, a14\n"
"v_accvgpr_read_b32 v249, a15\n"
"v_cvt_pk_bf16_f32 v24, v242, v243\n"
"v_cvt_pk_bf16_f32 v25, v244, v245\n"
"v_pk_mul_f32 v[246:247], v[246:247], v[250:251] op_sel_hi:[1,0]\n"
"v_pk_mul_f32 v[248:249], v[248:249], v[250:251] op_sel_hi:[1,0]\n"
"v_accvgpr_read_b32 v242, a16\n"
"v_accvgpr_read_b32 v243, a17\n"
"v_accvgpr_read_b32 v244, a18\n"
"v_accvgpr_read_b32 v245, a19\n"
"v_cvt_pk_bf16_f32 v18, v246, v247\n"
"v_cvt_pk_bf16_f32 v19, v248, v249\n"
"v_pk_mul_f32 v[242:243], v[242:243], v[252:253] op_sel_hi:[1,0]\n"
"v_pk_mul_f32 v[244:245], v[244:245], v[252:253] op_sel_hi:[1,0]\n"
"v_accvgpr_read_b32 v246, a20\n"
"v_accvgpr_read_b32 v247, a21\n"
"v_accvgpr_read_b32 v248, a22\n"
"v_accvgpr_read_b32 v249, a23\n"
"v_cvt_pk_bf16_f32 v22, v242, v243\n"
"v_cvt_pk_bf16_f32 v23, v244, v245\n"
"v_pk_mul_f32 v[246:247], v[246:247], v[254:255] op_sel_hi:[1,0]\n"
"v_pk_mul_f32 v[248:249], v[248:249], v[254:255] op_sel_hi:[1,0]\n"
"v_accvgpr_read_b32 v242, a24\n"
"v_accvgpr_read_b32 v243, a25\n"
"v_accvgpr_read_b32 v244, a26\n"
"v_accvgpr_read_b32 v245, a27\n"
"v_cvt_pk_bf16_f32 v26, v246, v247\n"
"v_cvt_pk_bf16_f32 v27, v248, v249\n"
"v_pk_mul_f32 v[242:243], v[242:243], v[250:251] op_sel_hi:[1,0]\n"
"v_pk_mul_f32 v[244:245], v[244:245], v[250:251] op_sel_hi:[1,0]\n"
"v_accvgpr_read_b32 v246, a28\n"
"v_accvgpr_read_b32 v247, a29\n"
"v_accvgpr_read_b32 v248, a30\n"
"v_accvgpr_read_b32 v249, a31\n"
"v_cvt_pk_bf16_f32 v28, v242, v243\n"
"v_cvt_pk_bf16_f32 v29, v244, v245\n"
"v_pk_mul_f32 v[246:247], v[246:247], v[252:253] op_sel_hi:[1,0]\n"
"v_pk_mul_f32 v[248:249], v[248:249], v[252:253] op_sel_hi:[1,0]\n"
"v_accvgpr_read_b32 v242, a32\n"
"v_accvgpr_read_b32 v243, a33\n"
"v_accvgpr_read_b32 v244, a34\n"
"v_accvgpr_read_b32 v245, a35\n"
"v_cvt_pk_bf16_f32 v32, v246, v247\n"
"v_cvt_pk_bf16_f32 v33, v248, v249\n"
"v_pk_mul_f32 v[242:243], v[242:243], v[254:255] op_sel_hi:[1,0]\n"
"v_pk_mul_f32 v[244:245], v[244:245], v[254:255] op_sel_hi:[1,0]\n"
"v_accvgpr_read_b32 v246, a36\n"
"v_accvgpr_read_b32 v247, a37\n"
"v_accvgpr_read_b32 v248, a38\n"
"v_accvgpr_read_b32 v249, a39\n"
"v_cvt_pk_bf16_f32 v36, v242, v243\n"
"v_cvt_pk_bf16_f32 v37, v244, v245\n"
"v_pk_mul_f32 v[246:247], v[246:247], v[250:251] op_sel_hi:[1,0]\n"
"v_pk_mul_f32 v[248:249], v[248:249], v[250:251] op_sel_hi:[1,0]\n"
"v_accvgpr_read_b32 v242, a40\n"
"v_accvgpr_read_b32 v243, a41\n"
"v_accvgpr_read_b32 v244, a42\n"
"v_accvgpr_read_b32 v245, a43\n"
"v_cvt_pk_bf16_f32 v30, v246, v247\n"
"v_cvt_pk_bf16_f32 v31, v248, v249\n"
"v_pk_mul_f32 v[242:243], v[242:243], v[252:253] op_sel_hi:[1,0]\n"
"v_pk_mul_f32 v[244:245], v[244:245], v[252:253] op_sel_hi:[1,0]\n"
"v_accvgpr_read_b32 v246, a44\n"
"v_accvgpr_read_b32 v247, a45\n"
"v_accvgpr_read_b32 v248, a46\n"
"v_accvgpr_read_b32 v249, a47\n"
"v_cvt_pk_bf16_f32 v34, v242, v243\n"
"v_cvt_pk_bf16_f32 v35, v244, v245\n"
"v_pk_mul_f32 v[246:247], v[246:247], v[254:255] op_sel_hi:[1,0]\n"
"v_pk_mul_f32 v[248:249], v[248:249], v[254:255] op_sel_hi:[1,0]\n"
"v_accvgpr_read_b32 v242, a48\n"
"v_accvgpr_read_b32 v243, a49\n"
"v_accvgpr_read_b32 v244, a50\n"
"v_accvgpr_read_b32 v245, a51\n"
"v_cvt_pk_bf16_f32 v38, v246, v247\n"
"v_cvt_pk_bf16_f32 v39, v248, v249\n"
"v_pk_mul_f32 v[242:243], v[242:243], v[250:251] op_sel_hi:[1,0]\n"
"v_pk_mul_f32 v[244:245], v[244:245], v[250:251] op_sel_hi:[1,0]\n"
"v_accvgpr_read_b32 v246, a52\n"
"v_accvgpr_read_b32 v247, a53\n"
"v_accvgpr_read_b32 v248, a54\n"
"v_accvgpr_read_b32 v249, a55\n"
"v_cvt_pk_bf16_f32 v40, v242, v243\n"
"v_cvt_pk_bf16_f32 v41, v244, v245\n"
"v_pk_mul_f32 v[246:247], v[246:247], v[252:253] op_sel_hi:[1,0]\n"
"v_pk_mul_f32 v[248:249], v[248:249], v[252:253] op_sel_hi:[1,0]\n"
"v_accvgpr_read_b32 v242, a56\n"
"v_accvgpr_read_b32 v243, a57\n"
"v_accvgpr_read_b32 v244, a58\n"
"v_accvgpr_read_b32 v245, a59\n"
"v_cvt_pk_bf16_f32 v44, v246, v247\n"
"v_cvt_pk_bf16_f32 v45, v248, v249\n"
"v_pk_mul_f32 v[242:243], v[242:243], v[254:255] op_sel_hi:[1,0]\n"
"v_pk_mul_f32 v[244:245], v[244:245], v[254:255] op_sel_hi:[1,0]\n"
"v_accvgpr_read_b32 v246, a60\n"
"v_accvgpr_read_b32 v247, a61\n"
"v_accvgpr_read_b32 v248, a62\n"
"v_accvgpr_read_b32 v249, a63\n"
"v_cvt_pk_bf16_f32 v48, v242, v243\n"
"v_cvt_pk_bf16_f32 v49, v244, v245\n"
"v_pk_mul_f32 v[246:247], v[246:247], v[250:251] op_sel_hi:[1,0]\n"
"v_pk_mul_f32 v[248:249], v[248:249], v[250:251] op_sel_hi:[1,0]\n"
"v_accvgpr_read_b32 v242, a64\n"
"v_accvgpr_read_b32 v243, a65\n"
"v_accvgpr_read_b32 v244, a66\n"
"v_accvgpr_read_b32 v245, a67\n"
"v_cvt_pk_bf16_f32 v42, v246, v247\n"
"v_cvt_pk_bf16_f32 v43, v248, v249\n"
"v_pk_mul_f32 v[242:243], v[242:243], v[252:253] op_sel_hi:[1,0]\n"
"v_pk_mul_f32 v[244:245], v[244:245], v[252:253] op_sel_hi:[1,0]\n"
"v_accvgpr_read_b32 v246, a68\n"
"v_accvgpr_read_b32 v247, a69\n"
"v_accvgpr_read_b32 v248, a70\n"
"v_accvgpr_read_b32 v249, a71\n"
"v_cvt_pk_bf16_f32 v46, v242, v243\n"
"v_cvt_pk_bf16_f32 v47, v244, v245\n"
"v_pk_mul_f32 v[246:247], v[246:247], v[254:255] op_sel_hi:[1,0]\n"
"v_pk_mul_f32 v[248:249], v[248:249], v[254:255] op_sel_hi:[1,0]\n"
"v_accvgpr_read_b32 v242, a72\n"
"v_accvgpr_read_b32 v243, a73\n"
"v_accvgpr_read_b32 v244, a74\n"
"v_accvgpr_read_b32 v245, a75\n"
"v_cvt_pk_bf16_f32 v50, v246, v247\n"
"v_cvt_pk_bf16_f32 v51, v248, v249\n"
"v_pk_mul_f32 v[242:243], v[242:243], v[250:251] op_sel_hi:[1,0]\n"
"v_pk_mul_f32 v[244:245], v[244:245], v[250:251] op_sel_hi:[1,0]\n"
"v_accvgpr_read_b32 v246, a76\n"
"v_accvgpr_read_b32 v247, a77\n"
"v_accvgpr_read_b32 v248, a78\n"
"v_accvgpr_read_b32 v249, a79\n"
"v_cvt_pk_bf16_f32 v52, v242, v243\n"
"v_cvt_pk_bf16_f32 v53, v244, v245\n"
"v_pk_mul_f32 v[246:247], v[246:247], v[252:253] op_sel_hi:[1,0]\n"
"v_pk_mul_f32 v[248:249], v[248:249], v[252:253] op_sel_hi:[1,0]\n"
"v_accvgpr_read_b32 v242, a80\n"
"v_accvgpr_read_b32 v243, a81\n"
"v_accvgpr_read_b32 v244, a82\n"
"v_accvgpr_read_b32 v245, a83\n"
"v_cvt_pk_bf16_f32 v56, v246, v247\n"
"v_cvt_pk_bf16_f32 v57, v248, v249\n"
"v_pk_mul_f32 v[242:243], v[242:243], v[254:255] op_sel_hi:[1,0]\n"
"v_pk_mul_f32 v[244:245], v[244:245], v[254:255] op_sel_hi:[1,0]\n"
"v_accvgpr_read_b32 v246, a84\n"
"v_accvgpr_read_b32 v247, a85\n"
"v_accvgpr_read_b32 v248, a86\n"
"v_accvgpr_read_b32 v249, a87\n"
"v_cvt_pk_bf16_f32 v60, v242, v243\n"
"v_cvt_pk_bf16_f32 v61, v244, v245\n"
"v_pk_mul_f32 v[246:247], v[246:247], v[250:251] op_sel_hi:[1,0]\n"
"v_pk_mul_f32 v[248:249], v[248:249], v[250:251] op_sel_hi:[1,0]\n"
"v_accvgpr_read_b32 v242, a88\n"
"v_accvgpr_read_b32 v243, a89\n"
"v_accvgpr_read_b32 v244, a90\n"
"v_accvgpr_read_b32 v245, a91\n"
"v_cvt_pk_bf16_f32 v54, v246, v247\n"
"v_cvt_pk_bf16_f32 v55, v248, v249\n"
"v_pk_mul_f32 v[242:243], v[242:243], v[252:253] op_sel_hi:[1,0]\n"
"v_pk_mul_f32 v[244:245], v[244:245], v[252:253] op_sel_hi:[1,0]\n"
"v_accvgpr_read_b32 v246, a92\n"
"v_accvgpr_read_b32 v247, a93\n"
"v_accvgpr_read_b32 v248, a94\n"
"v_accvgpr_read_b32 v249, a95\n"
"v_cvt_pk_bf16_f32 v58, v242, v243\n"
"v_cvt_pk_bf16_f32 v59, v244, v245\n"
"v_pk_mul_f32 v[246:247], v[246:247], v[254:255] op_sel_hi:[1,0]\n"
"v_pk_mul_f32 v[248:249], v[248:249], v[254:255] op_sel_hi:[1,0]\n"
"v_accvgpr_read_b32 v242, a96\n"
"v_accvgpr_read_b32 v243, a97\n"
"v_accvgpr_read_b32 v244, a98\n"
"v_accvgpr_read_b32 v245, a99\n"
"v_cvt_pk_bf16_f32 v62, v246, v247\n"
"v_cvt_pk_bf16_f32 v63, v248, v249\n"
"v_pk_mul_f32 v[242:243], v[242:243], v[250:251] op_sel_hi:[1,0]\n"
"v_pk_mul_f32 v[244:245], v[244:245], v[250:251] op_sel_hi:[1,0]\n"
"v_accvgpr_read_b32 v246, a100\n"
"v_accvgpr_read_b32 v247, a101\n"
"v_accvgpr_read_b32 v248, a102\n"
"v_accvgpr_read_b32 v249, a103\n"
"v_cvt_pk_bf16_f32 v64, v242, v243\n"
"v_cvt_pk_bf16_f32 v65, v244, v245\n"
"v_pk_mul_f32 v[246:247], v[246:247], v[252:253] op_sel_hi:[1,0]\n"
"v_pk_mul_f32 v[248:249], v[248:249], v[252:253] op_sel_hi:[1,0]\n"
"v_accvgpr_read_b32 v242, a104\n"
"v_accvgpr_read_b32 v243, a105\n"
"v_accvgpr_read_b32 v244, a106\n"
"v_accvgpr_read_b32 v245, a107\n"
"v_cvt_pk_bf16_f32 v68, v246, v247\n"
"v_cvt_pk_bf16_f32 v69, v248, v249\n"
"v_pk_mul_f32 v[242:243], v[242:243], v[254:255] op_sel_hi:[1,0]\n"
"v_pk_mul_f32 v[244:245], v[244:245], v[254:255] op_sel_hi:[1,0]\n"
"v_accvgpr_read_b32 v246, a108\n"
"v_accvgpr_read_b32 v247, a109\n"
"v_accvgpr_read_b32 v248, a110\n"
"v_accvgpr_read_b32 v249, a111\n"
"v_cvt_pk_bf16_f32 v72, v242, v243\n"
"v_cvt_pk_bf16_f32 v73, v244, v245\n"
"v_pk_mul_f32 v[246:247], v[246:247], v[250:251] op_sel_hi:[1,0]\n"
"v_pk_mul_f32 v[248:249], v[248:249], v[250:251] op_sel_hi:[1,0]\n"
"v_accvgpr_read_b32 v242, a112\n"
"v_accvgpr_read_b32 v243, a113\n"
"v_accvgpr_read_b32 v244, a114\n"
"v_accvgpr_read_b32 v245, a115\n"
"v_cvt_pk_bf16_f32 v66, v246, v247\n"
"v_cvt_pk_bf16_f32 v67, v248, v249\n"
"v_pk_mul_f32 v[242:243], v[242:243], v[252:253] op_sel_hi:[1,0]\n"
"v_pk_mul_f32 v[244:245], v[244:245], v[252:253] op_sel_hi:[1,0]\n"
"v_accvgpr_read_b32 v246, a116\n"
"v_accvgpr_read_b32 v247, a117\n"
"v_accvgpr_read_b32 v248, a118\n"
"v_accvgpr_read_b32 v249, a119\n"
"v_cvt_pk_bf16_f32 v70, v242, v243\n"
"v_cvt_pk_bf16_f32 v71, v244, v245\n"
"v_pk_mul_f32 v[246:247], v[246:247], v[254:255] op_sel_hi:[1,0]\n"
"v_pk_mul_f32 v[248:249], v[248:249], v[254:255] op_sel_hi:[1,0]\n"
"v_accvgpr_read_b32 v242, a120\n"
"v_accvgpr_read_b32 v243, a121\n"
"v_accvgpr_read_b32 v244, a122\n"
"v_accvgpr_read_b32 v245, a123\n"
"v_cvt_pk_bf16_f32 v74, v246, v247\n"
"v_cvt_pk_bf16_f32 v75, v248, v249\n"
"v_pk_mul_f32 v[242:243], v[242:243], v[250:251] op_sel_hi:[1,0]\n"
"v_pk_mul_f32 v[244:245], v[244:245], v[250:251] op_sel_hi:[1,0]\n"
"v_accvgpr_read_b32 v246, a124\n"
"v_accvgpr_read_b32 v247, a125\n"
"v_accvgpr_read_b32 v248, a126\n"
"v_accvgpr_read_b32 v249, a127\n"
"v_cvt_pk_bf16_f32 v76, v242, v243\n"
"v_cvt_pk_bf16_f32 v77, v244, v245\n"
"v_pk_mul_f32 v[246:247], v[246:247], v[252:253] op_sel_hi:[1,0]\n"
"v_pk_mul_f32 v[248:249], v[248:249], v[252:253] op_sel_hi:[1,0]\n"
"v_accvgpr_read_b32 v242, a128\n"
"v_accvgpr_read_b32 v243, a129\n"
"v_accvgpr_read_b32 v244, a130\n"
"v_accvgpr_read_b32 v245, a131\n"
"v_cvt_pk_bf16_f32 v80, v246, v247\n"
"v_cvt_pk_bf16_f32 v81, v248, v249\n"
"v_pk_mul_f32 v[242:243], v[242:243], v[254:255] op_sel_hi:[1,0]\n"
"v_pk_mul_f32 v[244:245], v[244:245], v[254:255] op_sel_hi:[1,0]\n"
"v_accvgpr_read_b32 v246, a132\n"
"v_accvgpr_read_b32 v247, a133\n"
"v_accvgpr_read_b32 v248, a134\n"
"v_accvgpr_read_b32 v249, a135\n"
"v_cvt_pk_bf16_f32 v84, v242, v243\n"
"v_cvt_pk_bf16_f32 v85, v244, v245\n"
"v_pk_mul_f32 v[246:247], v[246:247], v[250:251] op_sel_hi:[1,0]\n"
"v_pk_mul_f32 v[248:249], v[248:249], v[250:251] op_sel_hi:[1,0]\n"
"v_accvgpr_read_b32 v242, a136\n"
"v_accvgpr_read_b32 v243, a137\n"
"v_accvgpr_read_b32 v244, a138\n"
"v_accvgpr_read_b32 v245, a139\n"
"v_cvt_pk_bf16_f32 v78, v246, v247\n"
"v_cvt_pk_bf16_f32 v79, v248, v249\n"
"v_pk_mul_f32 v[242:243], v[242:243], v[252:253] op_sel_hi:[1,0]\n"
"v_pk_mul_f32 v[244:245], v[244:245], v[252:253] op_sel_hi:[1,0]\n"
"v_accvgpr_read_b32 v246, a140\n"
"v_accvgpr_read_b32 v247, a141\n"
"v_accvgpr_read_b32 v248, a142\n"
"v_accvgpr_read_b32 v249, a143\n"
"v_cvt_pk_bf16_f32 v82, v242, v243\n"
"v_cvt_pk_bf16_f32 v83, v244, v245\n"
"v_pk_mul_f32 v[246:247], v[246:247], v[254:255] op_sel_hi:[1,0]\n"
"v_pk_mul_f32 v[248:249], v[248:249], v[254:255] op_sel_hi:[1,0]\n"
"v_cvt_pk_bf16_f32 v86, v246, v247\n"
"v_cvt_pk_bf16_f32 v87, v248, v249\n"
"v_mov_b32 v196, 0\n"
"v_mov_b32 v197, 0\n"
"v_mov_b32 v198, 0\n"
"v_mov_b32 v199, 0\n"
"v_mov_b32 v200, 0\n"
"v_mov_b32 v201, 0\n"
"v_mov_b32 v202, 0\n"
"v_mov_b32 v203, 0\n"
"v_mov_b32 v204, 0\n"
"v_mov_b32 v205, 0\n"
"v_mov_b32 v206, 0\n"
"v_mov_b32 v207, 0\n"
"s_mov_b32 s70, 0xff800000\n"
"s_mov_b32 s71, 0xff800000\n"
"s_mov_b32 s74, 1\n"
"s_waitcnt lgkmcnt(3)\n"
"v_mfma_f32_16x16x32_bf16 v[160:163], a[216:219], a[144:147], v[196:199]\n"
"ds_read_b128 a[232:235], v236 offset:256\n"
"v_mfma_f32_16x16x32_bf16 v[164:167], a[216:219], a[148:151], v[200:203]\n"
"ds_write_b128 %[exW], v[16:19] offset:0\n"
"ds_write_b128 %[exW], v[20:23] offset:1024\n"
"v_mfma_f32_16x16x32_bf16 v[168:171], a[216:219], a[152:155], v[204:207]\n"
"s_mov_b32 m0, s61\n"
"s_add_u32 s68, s64, 3072\n"
"buffer_load_dwordx4 %[dKV0], %[rK], s64 offen offset:0 nt lds\n"
"s_waitcnt lgkmcnt(5)\n"
"v_mfma_f32_16x16x32_bf16 v[160:163], a[220:223], a[156:159], v[160:163]\n"
"ds_read_b128 a[236:239], v237 offset:256\n"
"v_mfma_f32_16x16x32_bf16 v[164:167], a[220:223], a[160:163], v[164:167]\n"
"v_mfma_f32_16x16x32_bf16 v[168:171], a[220:223], a[164:167], v[168:171]\n"
"s_waitcnt lgkmcnt(5)\n"
"v_mfma_f32_16x16x32_bf16 v[160:163], a[224:227], a[168:171], v[160:163]\n"
"ds_read_b128 a[216:219], v236 offset:6144\n"
"v_mfma_f32_16x16x32_bf16 v[164:167], a[224:227], a[172:175], v[164:167]\n"
"ds_write_b128 %[exW], v[24:27] offset:2048\n"
"v_mfma_f32_16x16x32_bf16 v[168:171], a[224:227], a[176:179], v[168:171]\n"
"buffer_load_dwordx4 %[dKV1], %[rK], s64 offen offset:1024 nt lds\n"
"s_waitcnt lgkmcnt(6)\n"
"v_mfma_f32_16x16x32_bf16 v[160:163], a[228:231], a[180:183], v[160:163]\n"
"ds_read_b128 a[220:223], v237 offset:6144\n"
"v_mfma_f32_16x16x32_bf16 v[164:167], a[228:231], a[184:187], v[164:167]\n"
"ds_write_b128 %[exW], v[28:31] offset:3072\n"
"v_mfma_f32_16x16x32_bf16 v[168:171], a[228:231], a[188:191], v[168:171]\n"
"ds_write_b128 %[exW], v[32:35] offset:4096\n"
"ds_write_b128 %[exW], v[36:39] offset:5120\n"
"s_waitcnt lgkmcnt(9)\n"
"v_mfma_f32_16x16x32_bf16 v[160:163], a[232:235], a[192:195], v[160:163]\n"
"ds_read_b128 a[224:227], v236 offset:6272\n"
"v_mfma_f32_16x16x32_bf16 v[164:167], a[232:235], a[196:199], v[164:167]\n"
"ds_write_b128 %[exW], v[40:43] offset:6144\n"
"v_mfma_f32_16x16x32_bf16 v[168:171], a[232:235], a[200:203], v[168:171]\n"
"buffer_load_dwordx4 %[dKV2], %[rK], s64 offen offset:2048 nt lds\n"
"s_add_u32 m0, s61, 3072\n"
"s_waitcnt lgkmcnt(8)\n"
"v_mfma_f32_16x16x32_bf16 v[160:163], a[236:239], a[204:207], v[160:163]\n"
"ds_read_b128 a[228:231], v237 offset:6272\n"
"v_mfma_f32_16x16x32_bf16 v[164:167], a[236:239], a[208:211], v[164:167]\n"
"v_mfma_f32_16x16x32_bf16 v[168:171], a[236:239], a[212:215], v[168:171]\n"
"ds_write_b128 %[exW], v[44:47] offset:7168\n"
"s_waitcnt lgkmcnt(9)\n"
"v_mfma_f32_16x16x32_bf16 v[172:175], a[216:219], a[144:147], v[196:199]\n"
"ds_read_b128 a[232:235], v236 offset:6400\n"
"ds_write_b128 %[exW], v[48:51] offset:8192\n"
"v_mfma_f32_16x16x32_bf16 v[176:179], a[216:219], a[148:151], v[200:203]\n"
"v_mfma_f32_16x16x32_bf16 v[180:183], a[216:219], a[152:155], v[204:207]\n"
"s_add_u32 s64, s64, 12288\n"
"buffer_load_dwordx4 %[dKV0], %[rK], s68 offen offset:0 nt lds\n"
"s_waitcnt lgkmcnt(9)\n"
"v_mfma_f32_16x16x32_bf16 v[172:175], a[220:223], a[156:159], v[172:175]\n"
"ds_read_b128 a[236:239], v237 offset:6400\n"
"v_mfma_f32_16x16x32_bf16 v[176:179], a[220:223], a[160:163], v[176:179]\n"
"v_mfma_f32_16x16x32_bf16 v[180:183], a[220:223], a[164:167], v[180:183]\n"
"buffer_load_dwordx4 %[dKV1], %[rK], s68 offen offset:1024 nt lds\n"
"s_waitcnt lgkmcnt(6)\n"
"v_mfma_f32_16x16x32_bf16 v[172:175], a[224:227], a[168:171], v[172:175]\n"
"s_add_u32 s61, %[m0b], s60\n"
"s_add_u32 s60, s60, 12288\n"
"s_cmp_eq_u32 s60, 61440\n"
"s_cselect_b32 s60, 0, s60\n"
"v_add_u32 v236, s60, %[rdV]\n"
"v_mfma_f32_16x16x32_bf16 v[176:179], a[224:227], a[172:175], v[176:179]\n"
"v_xor_b32 v237, 32, v236\n"
"v_xor_b32 v238, 64, v236\n"
"v_xor_b32 v239, 96, v236\n"
"v_mfma_f32_16x16x32_bf16 v[180:183], a[224:227], a[176:179], v[180:183]\n"
"s_waitcnt vmcnt(17) lgkmcnt(0)\n"
"s_barrier\n"
"ds_read_b64_tr_b16 a[216:217], v236 offset:0\n"
"ds_read_b64_tr_b16 a[218:219], v236 offset:6144\n"
"v_mfma_f32_16x16x32_bf16 v[172:175], a[228:231], a[180:183], v[172:175]\n"
"ds_read_b64_tr_b16 a[220:221], v237 offset:0\n"
"v_mfma_f32_16x16x32_bf16 v[176:179], a[228:231], a[184:187], v[176:179]\n"
"ds_read_b64_tr_b16 a[222:223], v237 offset:6144\n"
"v_mfma_f32_16x16x32_bf16 v[180:183], a[228:231], a[188:191], v[180:183]\n"
"v_mfma_f32_16x16x32_bf16 v[172:175], a[232:235], a[192:195], v[172:175]\n"
"ds_read_b64_tr_b16 a[224:225], v238 offset:0\n"
"v_mfma_f32_16x16x32_bf16 v[176:179], a[232:235], a[196:199], v[176:179]\n"
"ds_read_b64_tr_b16 a[226:227], v238 offset:6144\n"
"v_mfma_f32_16x16x32_bf16 v[180:183], a[232:235], a[200:203], v[180:183]\n"
"buffer_load_dwordx4 %[dKV2], %[rK], s68 offen offset:2048 nt lds\n"
"v_mfma_f32_16x16x32_bf16 v[172:175], a[236:239], a[204:207], v[172:175]\n"
"ds_read_b64_tr_b16 a[228:229], v239 offset:0\n"
"v_mfma_f32_16x16x32_bf16 v[176:179], a[236:239], a[208:211], v[176:179]\n"
"ds_read_b64_tr_b16 a[230:231], v239 offset:6144\n"
"v_mfma_f32_16x16x32_bf16 v[180:183], a[236:239], a[212:215], v[180:183]\n"
"v_max3_f32 v242, v160, v161, v162\n"
"v_max3_f32 v242, v242, v163, v164\n"
"v_max3_f32 v242, v242, v165, v166\n"
"v_max3_f32 v242, v242, v167, v168\n"
"v_max3_f32 v242, v242, v169, v170\n"
"v_max_f32 v242, v242, v171\n"
"v_max3_f32 v243, v172, v173, v174\n"
"v_max3_f32 v243, v243, v175, v176\n"
"v_max3_f32 v243, v243, v177, v178\n"
"v_max3_f32 v243, v243, v179, v180\n"
"v_max3_f32 v243, v243, v181, v182\n"
"v_max3_f32 v243, v243, v183, v242\n"
"v_cmp_lt_f32 vcc, s70, v243\n"
"s_nop 1\n"
"s_cbranch_vccz 1\n"
"s_call_b64 s[76:77], Lslow_%=\n"
"v_exp_f32 v160, v160\n"
"v_exp_f32 v161, v161\n"
"v_exp_f32 v162, v162\n"
"v_exp_f32 v163, v163\n"
"v_exp_f32 v172, v172\n"
"v_exp_f32 v173, v173\n"
"v_exp_f32 v174, v174\n"
"v_exp_f32 v175, v175\n"
"v_cvt_pk_bf16_f32 v184, v160, v161\n"
"v_cvt_pk_bf16_f32 v185, v162, v163\n"
"v_exp_f32 v164, v164\n"
"v_exp_f32 v165, v165\n"
"v_exp_f32 v166, v166\n"
"v_exp_f32 v167, v167\n"
"v_cvt_pk_bf16_f32 v186, v172, v173\n"
"v_cvt_pk_bf16_f32 v187, v174, v175\n"
"v_exp_f32 v176, v176\n"
"v_exp_f32 v177, v177\n"
"v_exp_f32 v178, v178\n"
"v_exp_f32 v179, v179\n"
"v_cvt_pk_bf16_f32 v188, v164, v165\n"
"v_cvt_pk_bf16_f32 v189, v166, v167\n"
"v_exp_f32 v168, v168\n"
"v_exp_f32 v169, v169\n"
"v_exp_f32 v170, v170\n"
"v_exp_f32 v171, v171\n"
"v_cvt_pk_bf16_f32 v190, v176, v177\n"
"v_cvt_pk_bf16_f32 v191, v178, v179\n"
"v_exp_f32 v180, v180\n"
"v_exp_f32 v181, v181\n"
"v_exp_f32 v182, v182\n"
"v_exp_f32 v183, v183\n"
"v_cvt_pk_bf16_f32 v192, v168, v169\n"
"v_cvt_pk_bf16_f32 v193, v170, v171\n"
"v_cvt_pk_bf16_f32 v194, v180, v181\n"
"v_cvt_pk_bf16_f32 v195, v182, v183\n"
"s_waitcnt lgkmcnt(6)\n"
"v_mfma_f32_16x16x32_bf16 a[0:3], a[216:219], v[184:187], 0\n"
"ds_read_b64_tr_b16 a[232:233], v236 offset:128\n"
"ds_read_b128 v[88:91], %[exR] offset:0\n"
"v_mfma_f32_16x16x32_bf16 a[4:7], a[216:219], v[188:191], 0\n"
"ds_read_b64_tr_b16 a[234:235], v236 offset:6272\n"
"v_mfma_f32_16x16x32_bf16 a[8:11], a[216:219], v[192:195], 0\n"
"s_mov_b32 m0, s61\n"
"s_add_u32 s68, s65, 3072\n"
"buffer_load_dwordx4 %[dKV0], %[rV], s65 offen offset:0 nt lds\n"
"s_waitcnt lgkmcnt(7)\n"
"v_mfma_f32_16x16x32_bf16 a[12:15], a[220:223], v[184:187], 0\n"
"ds_read_b64_tr_b16 a[236:237], v237 offset:128\n"
"v_mfma_f32_16x16x32_bf16 a[16:19], a[220:223], v[188:191], 0\n"
"ds_read_b64_tr_b16 a[238:239], v237 offset:6272\n"
"v_mfma_f32_16x16x32_bf16 a[20:23], a[220:223], v[192:195], 0\n"
"ds_read_b128 v[92:95], %[exR] offset:1024\n"
"s_waitcnt lgkmcnt(8)\n"
"v_mfma_f32_16x16x32_bf16 a[24:27], a[224:227], v[184:187], 0\n"
"ds_read_b64_tr_b16 a[216:217], v238 offset:128\n"
"v_mfma_f32_16x16x32_bf16 a[28:31], a[224:227], v[188:191], 0\n"
"ds_read_b64_tr_b16 a[218:219], v238 offset:6272\n"
"ds_read_b128 v[96:99], %[exR] offset:2048\n"
"v_mfma_f32_16x16x32_bf16 a[32:35], a[224:227], v[192:195], 0\n"
"buffer_load_dwordx4 %[dKV1], %[rV], s65 offen offset:1024 nt lds\n"
"s_waitcnt lgkmcnt(9)\n"
"v_mfma_f32_16x16x32_bf16 a[36:39], a[228:231], v[184:187], 0\n"
"ds_read_b64_tr_b16 a[220:221], v239 offset:128\n"
"v_mfma_f32_16x16x32_bf16 a[40:43], a[228:231], v[188:191], 0\n"
"ds_read_b64_tr_b16 a[222:223], v239 offset:6272\n"
"ds_read_b128 v[100:103], %[exR] offset:3072\n"
"v_mfma_f32_16x16x32_bf16 a[44:47], a[228:231], v[192:195], 0\n"
"ds_read_b128 v[104:107], %[exR] offset:4096\n"
"ds_read_b128 v[108:111], %[exR] offset:5120\n"
"s_waitcnt lgkmcnt(11)\n"
"v_mfma_f32_16x16x32_bf16 a[48:51], a[232:235], v[184:187], 0\n"
"ds_read_b64_tr_b16 a[224:225], v236 offset:256\n"
"v_mfma_f32_16x16x32_bf16 a[52:55], a[232:235], v[188:191], 0\n"
"ds_read_b64_tr_b16 a[226:227], v236 offset:6400\n"
"ds_read_b128 v[112:115], %[exR] offset:6144\n"
"v_mfma_f32_16x16x32_bf16 a[56:59], a[232:235], v[192:195], 0\n"
"buffer_load_dwordx4 %[dKV2], %[rV], s65 offen offset:2048 nt lds\n"
"s_add_u32 m0, s61, 3072\n"
"s_waitcnt lgkmcnt(12)\n"
"v_mfma_f32_16x16x32_bf16 a[60:63], a[236:239], v[184:187], 0\n"
"ds_read_b64_tr_b16 a[228:229], v237 offset:256\n"
"v_mfma_f32_16x16x32_bf16 a[64:67], a[236:239], v[188:191], 0\n"
"ds_read_b64_tr_b16 a[230:231], v237 offset:6400\n"
"v_mfma_f32_16x16x32_bf16 a[68:71], a[236:239], v[192:195], 0\n"
"ds_read_b128 v[116:119], %[exR] offset:7168\n"
"s_waitcnt lgkmcnt(12)\n"
"v_mfma_f32_16x16x32_bf16 a[72:75], a[216:219], v[184:187], 0\n"
"ds_read_b64_tr_b16 a[232:233], v238 offset:256\n"
"ds_read_b128 v[120:123], %[exR] offset:8192\n"
"v_mfma_f32_16x16x32_bf16 a[76:79], a[216:219], v[188:191], 0\n"
"ds_read_b64_tr_b16 a[234:235], v238 offset:6400\n"
"v_mfma_f32_16x16x32_bf16 a[80:83], a[216:219], v[192:195], 0\n"
"s_add_u32 s65, s65, 12288\n"
"buffer_load_dwordx4 %[dKV0], %[rV], s68 offen offset:0 nt lds\n"
"s_waitcnt lgkmcnt(12)\n"
"v_mfma_f32_16x16x32_bf16 a[84:87], a[220:223], v[184:187], 0\n"
"ds_read_b64_tr_b16 a[236:237], v239 offset:256\n"
"v_mfma_f32_16x16x32_bf16 a[88:91], a[220:223], v[188:191], 0\n"
"ds_read_b64_tr_b16 a[238:239], v239 offset:6400\n"
"v_mfma_f32_16x16x32_bf16 a[92:95], a[220:223], v[192:195], 0\n"
"buffer_load_dwordx4 %[dKV1], %[rV], s68 offen offset:1024 nt lds\n"
"s_waitcnt lgkmcnt(9)\n"
"v_mfma_f32_16x16x32_bf16 a[96:99], a[224:227], v[184:187], 0\n"
"s_add_u32 s61, %[m0b], s60\n"
"s_add_u32 s60, s60, 12288\n"
"s_cmp_eq_u32 s60, 61440\n"
"s_cselect_b32 s60, 0, s60\n"
"v_add_u32 v236, s60, %[rdK]\n"
"v_mfma_f32_16x16x32_bf16 a[100:103], a[224:227], v[188:191], 0\n"
"v_xor_b32 v237, 64, v236\n"
"v_mfma_f32_16x16x32_bf16 a[104:107], a[224:227], v[192:195], 0\n"
"s_waitcnt vmcnt(17) lgkmcnt(0)\n"
"s_barrier\n"
"ds_read_b128 a[216:219], v236 offset:0\n"
"v_mfma_f32_16x16x32_bf16 a[108:111], a[228:231], v[184:187], 0\n"
"ds_read_b128 a[220:223], v237 offset:0\n"
"v_mfma_f32_16x16x32_bf16 a[112:115], a[228:231], v[188:191], 0\n"
"v_mfma_f32_16x16x32_bf16 a[116:119], a[228:231], v[192:195], 0\n"
"v_mfma_f32_16x16x32_bf16 a[120:123], a[232:235], v[184:187], 0\n"
"ds_read_b128 a[224:227], v236 offset:128\n"
"v_mfma_f32_16x16x32_bf16 a[124:127], a[232:235], v[188:191], 0\n"
"v_mfma_f32_16x16x32_bf16 a[128:131], a[232:235], v[192:195], 0\n"
"buffer_load_dwordx4 %[dKV2], %[rV], s68 offen offset:2048 nt lds\n"
"v_mfma_f32_16x16x32_bf16 a[132:135], a[236:239], v[184:187], 0\n"
"ds_read_b128 a[228:231], v237 offset:128\n"
"v_mfma_f32_16x16x32_bf16 a[136:139], a[236:239], v[188:191], 0\n"
"v_mfma_f32_16x16x32_bf16 a[140:143], a[236:239], v[192:195], 0\n"
"v_mfma_f32_16x16x32_bf16 v[208:211], v[220:223], v[184:187], 0\n"
"v_mfma_f32_16x16x32_bf16 v[212:215], v[220:223], v[188:191], 0\n"
"v_mfma_f32_16x16x32_bf16 v[216:219], v[220:223], v[192:195], 0\n"
"s_waitcnt lgkmcnt(3)\n"
"v_mfma_f32_16x16x32_bf16 v[160:163], a[216:219], a[144:147], v[196:199]\n"
"ds_read_b128 a[232:235], v236 offset:256\n"
"v_mfma_f32_16x16x32_bf16 v[164:167], a[216:219], a[148:151], v[200:203]\n"
"ds_write_b128 %[exW], v[52:55] offset:0\n"
"ds_write_b128 %[exW], v[56:59] offset:1024\n"
"v_mfma_f32_16x16x32_bf16 v[168:171], a[216:219], a[152:155], v[204:207]\n"
"s_mov_b32 m0, s61\n"
"s_add_u32 s68, s64, 3072\n"
"buffer_load_dwordx4 %[dKV0], %[rK], s64 offen offset:0 nt lds\n"
"s_waitcnt lgkmcnt(5)\n"
"v_mfma_f32_16x16x32_bf16 v[160:163], a[220:223], a[156:159], v[160:163]\n"
"ds_read_b128 a[236:239], v237 offset:256\n"
"v_mfma_f32_16x16x32_bf16 v[164:167], a[220:223], a[160:163], v[164:167]\n"
"v_mfma_f32_16x16x32_bf16 v[168:171], a[220:223], a[164:167], v[168:171]\n"
"s_waitcnt lgkmcnt(5)\n"
"v_mfma_f32_16x16x32_bf16 v[160:163], a[224:227], a[168:171], v[160:163]\n"
"ds_read_b128 a[216:219], v236 offset:6144\n"
"v_mfma_f32_16x16x32_bf16 v[164:167], a[224:227], a[172:175], v[164:167]\n"
"ds_write_b128 %[exW], v[60:63] offset:2048\n"
"v_mfma_f32_16x16x32_bf16 v[168:171], a[224:227], a[176:179], v[168:171]\n"
"buffer_load_dwordx4 %[dKV1], %[rK], s64 offen offset:1024 nt lds\n"
"s_waitcnt lgkmcnt(6)\n"
"v_mfma_f32_16x16x32_bf16 v[160:163], a[228:231], a[180:183], v[160:163]\n"
"ds_read_b128 a[220:223], v237 offset:6144\n"
"v_mfma_f32_16x16x32_bf16 v[164:167], a[228:231], a[184:187], v[164:167]\n"
"ds_write_b128 %[exW], v[64:67] offset:3072\n"
"v_mfma_f32_16x16x32_bf16 v[168:171], a[228:231], a[188:191], v[168:171]\n"
"ds_write_b128 %[exW], v[68:71] offset:4096\n"
"ds_write_b128 %[exW], v[72:75] offset:5120\n"
"s_waitcnt lgkmcnt(9)\n"
"v_mfma_f32_16x16x32_bf16 v[160:163], a[232:235], a[192:195], v[160:163]\n"
"ds_read_b128 a[224:227], v236 offset:6272\n"
"v_mfma_f32_16x16x32_bf16 v[164:167], a[232:235], a[196:199], v[164:167]\n"
"ds_write_b128 %[exW], v[76:79] offset:6144\n"
"v_mfma_f32_16x16x32_bf16 v[168:171], a[232:235], a[200:203], v[168:171]\n"
"buffer_load_dwordx4 %[dKV2], %[rK], s64 offen offset:2048 nt lds\n"
"s_add_u32 m0, s61, 3072\n"
"s_waitcnt lgkmcnt(8)\n"
"v_mfma_f32_16x16x32_bf16 v[160:163], a[236:239], a[204:207], v[160:163]\n"
"ds_read_b128 a[228:231], v237 offset:6272\n"
"v_mfma_f32_16x16x32_bf16 v[164:167], a[236:239], a[208:211], v[164:167]\n"
"v_mfma_f32_16x16x32_bf16 v[168:171], a[236:239], a[212:215], v[168:171]\n"
"ds_write_b128 %[exW], v[80:83] offset:7168\n"
"s_waitcnt lgkmcnt(9)\n"
"v_mfma_f32_16x16x32_bf16 v[172:175], a[216:219], a[144:147], v[196:199]\n"
"ds_read_b128 a[232:235], v236 offset:6400\n"
"ds_write_b128 %[exW], v[84:87] offset:8192\n"
"v_mfma_f32_16x16x32_bf16 v[176:179], a[216:219], a[148:151], v[200:203]\n"
"v_mfma_f32_16x16x32_bf16 v[180:183], a[216:219], a[152:155], v[204:207]\n"
"s_add_u32 s64, s64, 12288\n"
"buffer_load_dwordx4 %[dKV0], %[rK], s68 offen offset:0 nt lds\n"
"s_waitcnt lgkmcnt(9)\n"
"v_mfma_f32_16x16x32_bf16 v[172:175], a[220:223], a[156:159], v[172:175]\n"
"ds_read_b128 a[236:239], v237 offset:6400\n"
"v_mfma_f32_16x16x32_bf16 v[176:179], a[220:223], a[160:163], v[176:179]\n"
"v_mfma_f32_16x16x32_bf16 v[180:183], a[220:223], a[164:167], v[180:183]\n"
"buffer_load_dwordx4 %[dKV1], %[rK], s68 offen offset:1024 nt lds\n"
"s_waitcnt lgkmcnt(6)\n"
"v_mfma_f32_16x16x32_bf16 v[172:175], a[224:227], a[168:171], v[172:175]\n"
"s_add_u32 s61, %[m0b], s60\n"
"s_add_u32 s60, s60, 12288\n"
"s_cmp_eq_u32 s60, 61440\n"
"s_cselect_b32 s60, 0, s60\n"
"v_add_u32 v236, s60, %[rdV]\n"
"v_mfma_f32_16x16x32_bf16 v[176:179], a[224:227], a[172:175], v[176:179]\n"
"v_xor_b32 v237, 32, v236\n"
"v_xor_b32 v238, 64, v236\n"
"v_xor_b32 v239, 96, v236\n"
"v_mfma_f32_16x16x32_bf16 v[180:183], a[224:227], a[176:179], v[180:183]\n"
"s_waitcnt vmcnt(17) lgkmcnt(0)\n"
"s_barrier\n"
"ds_read_b64_tr_b16 a[216:217], v236 offset:0\n"
"ds_read_b64_tr_b16 a[218:219], v236 offset:6144\n"
"v_mfma_f32_16x16x32_bf16 v[172:175], a[228:231], a[180:183], v[172:175]\n"
"ds_read_b64_tr_b16 a[220:221], v237 offset:0\n"
"v_mfma_f32_16x16x32_bf16 v[176:179], a[228:231], a[184:187], v[176:179]\n"
"ds_read_b64_tr_b16 a[222:223], v237 offset:6144\n"
"v_mfma_f32_16x16x32_bf16 v[180:183], a[228:231], a[188:191], v[180:183]\n"
"v_mfma_f32_16x16x32_bf16 v[172:175], a[232:235], a[192:195], v[172:175]\n"
"ds_read_b64_tr_b16 a[224:225], v238 offset:0\n"
"v_mfma_f32_16x16x32_bf16 v[176:179], a[232:235], a[196:199], v[176:179]\n"
"ds_read_b64_tr_b16 a[226:227], v238 offset:6144\n"
"v_mfma_f32_16x16x32_bf16 v[180:183], a[232:235], a[200:203], v[180:183]\n"
"buffer_load_dwordx4 %[dKV2], %[rK], s68 offen offset:2048 nt lds\n"
"v_mfma_f32_16x16x32_bf16 v[172:175], a[236:239], a[204:207], v[172:175]\n"
"ds_read_b64_tr_b16 a[228:229], v239 offset:0\n"
"v_mfma_f32_16x16x32_bf16 v[176:179], a[236:239], a[208:211], v[176:179]\n"
"ds_read_b64_tr_b16 a[230:231], v239 offset:6144\n"
"v_mfma_f32_16x16x32_bf16 v[180:183], a[236:239], a[212:215], v[180:183]\n"
"v_max3_f32 v242, v160, v161, v162\n"
"v_max3_f32 v242, v242, v163, v164\n"
"v_max3_f32 v242, v242, v165, v166\n"
"v_max3_f32 v242, v242, v167, v168\n"
"v_max3_f32 v242, v242, v169, v170\n"
"v_max_f32 v242, v242, v171\n"
"v_max3_f32 v243, v172, v173, v174\n"
"v_max3_f32 v243, v243, v175, v176\n"
"v_max3_f32 v243, v243, v177, v178\n"
"v_max3_f32 v243, v243, v179, v180\n"
"v_max3_f32 v243, v243, v181, v182\n"
"v_max3_f32 v243, v243, v183, v242\n"
"v_cmp_lt_f32 vcc, s70, v243\n"
"s_nop 1\n"
"s_cbranch_vccz 1\n"
"s_call_b64 s[76:77], Lslow_%=\n"
"v_exp_f32 v160, v160\n"
"v_exp_f32 v161, v161\n"
"v_exp_f32 v162, v162\n"
"v_exp_f32 v163, v163\n"
"v_exp_f32 v172, v172\n"
"v_exp_f32 v173, v173\n"
"v_exp_f32 v174, v174\n"
"v_exp_f32 v175, v175\n"
"v_cvt_pk_bf16_f32 v184, v160, v161\n"
"v_cvt_pk_bf16_f32 v185, v162, v163\n"
"v_exp_f32 v164, v164\n"
"v_exp_f32 v165, v165\n"
"v_exp_f32 v166, v166\n"
"v_exp_f32 v167, v167\n"
"v_cvt_pk_bf16_f32 v186, v172, v173\n"
"v_cvt_pk_bf16_f32 v187, v174, v175\n"
"v_exp_f32 v176, v176\n"
"v_exp_f32 v177, v177\n"
"v_exp_f32 v178, v178\n"
"v_exp_f32 v179, v179\n"
"v_cvt_pk_bf16_f32 v188, v164, v165\n"
"v_cvt_pk_bf16_f32 v189, v166, v167\n"
"v_exp_f32 v168, v168\n"
"v_exp_f32 v169, v169\n"
"v_exp_f32 v170, v170\n"
"v_exp_f32 v171, v171\n"
"v_cvt_pk_bf16_f32 v190, v176, v177\n"
"v_cvt_pk_bf16_f32 v191, v178, v179\n"
"v_exp_f32 v180, v180\n"
"v_exp_f32 v181, v181\n"
"v_exp_f32 v182, v182\n"
"v_exp_f32 v183, v183\n"
"v_cvt_pk_bf16_f32 v192, v168, v169\n"
"v_cvt_pk_bf16_f32 v193, v170, v171\n"
"v_cvt_pk_bf16_f32 v194, v180, v181\n"
"v_cvt_pk_bf16_f32 v195, v182, v183\n"
"s_waitcnt lgkmcnt(6)\n"
"v_mfma_f32_16x16x32_bf16 a[0:3], a[216:219], v[184:187], a[0:3]\n"
"ds_read_b64_tr_b16 a[232:233], v236 offset:128\n"
"ds_read_b128 v[124:127], %[exR] offset:0\n"
"v_mfma_f32_16x16x32_bf16 a[4:7], a[216:219], v[188:191], a[4:7]\n"
"ds_read_b64_tr_b16 a[234:235], v236 offset:6272\n"
"v_mfma_f32_16x16x32_bf16 a[8:11], a[216:219], v[192:195], a[8:11]\n"
"s_mov_b32 m0, s61\n"
"s_add_u32 s68, s65, 3072\n"
"buffer_load_dwordx4 %[dKV0], %[rV], s65 offen offset:0 nt lds\n"
"s_waitcnt lgkmcnt(7)\n"
"v_mfma_f32_16x16x32_bf16 a[12:15], a[220:223], v[184:187], a[12:15]\n"
"ds_read_b64_tr_b16 a[236:237], v237 offset:128\n"
"v_mfma_f32_16x16x32_bf16 a[16:19], a[220:223], v[188:191], a[16:19]\n"
"ds_read_b64_tr_b16 a[238:239], v237 offset:6272\n"
"v_mfma_f32_16x16x32_bf16 a[20:23], a[220:223], v[192:195], a[20:23]\n"
"ds_read_b128 v[128:131], %[exR] offset:1024\n"
"s_waitcnt lgkmcnt(8)\n"
"v_mfma_f32_16x16x32_bf16 a[24:27], a[224:227], v[184:187], a[24:27]\n"
"ds_read_b64_tr_b16 a[216:217], v238 offset:128\n"
"v_mfma_f32_16x16x32_bf16 a[28:31], a[224:227], v[188:191], a[28:31]\n"
"ds_read_b64_tr_b16 a[218:219], v238 offset:6272\n"
"ds_read_b128 v[132:135], %[exR] offset:2048\n"
"v_mfma_f32_16x16x32_bf16 a[32:35], a[224:227], v[192:195], a[32:35]\n"
"buffer_load_dwordx4 %[dKV1], %[rV], s65 offen offset:1024 nt lds\n"
"s_waitcnt lgkmcnt(9)\n"
"v_mfma_f32_16x16x32_bf16 a[36:39], a[228:231], v[184:187], a[36:39]\n"
"ds_read_b64_tr_b16 a[220:221], v239 offset:128\n"
"v_mfma_f32_16x16x32_bf16 a[40:43], a[228:231], v[188:191], a[40:43]\n"
"ds_read_b64_tr_b16 a[222:223], v239 offset:6272\n"
"ds_read_b128 v[136:139], %[exR] offset:3072\n"
"v_mfma_f32_16x16x32_bf16 a[44:47], a[228:231], v[192:195], a[44:47]\n"
"ds_read_b128 v[140:143], %[exR] offset:4096\n"
"ds_read_b128 v[144:147], %[exR] offset:5120\n"
"s_waitcnt lgkmcnt(11)\n"
"v_mfma_f32_16x16x32_bf16 a[48:51], a[232:235], v[184:187], a[48:51]\n"
"ds_read_b64_tr_b16 a[224:225], v236 offset:256\n"
"v_mfma_f32_16x16x32_bf16 a[52:55], a[232:235], v[188:191], a[52:55]\n"
"ds_read_b64_tr_b16 a[226:227], v236 offset:6400\n"
"ds_read_b128 v[148:151], %[exR] offset:6144\n"
"v_mfma_f32_16x16x32_bf16 a[56:59], a[232:235], v[192:195], a[56:59]\n"
"buffer_load_dwordx4 %[dKV2], %[rV], s65 offen offset:2048 nt lds\n"
"s_add_u32 m0, s61, 3072\n"
"s_waitcnt lgkmcnt(12)\n"
"v_mfma_f32_16x16x32_bf16 a[60:63], a[236:239], v[184:187], a[60:63]\n"
"ds_read_b64_tr_b16 a[228:229], v237 offset:256\n"
"v_mfma_f32_16x16x32_bf16 a[64:67], a[236:239], v[188:191], a[64:67]\n"
"ds_read_b64_tr_b16 a[230:231], v237 offset:6400\n"
"v_mfma_f32_16x16x32_bf16 a[68:71], a[236:239], v[192:195], a[68:71]\n"
"ds_read_b128 v[152:155], %[exR] offset:7168\n"
"s_waitcnt lgkmcnt(12)\n"
"v_mfma_f32_16x16x32_bf16 a[72:75], a[216:219], v[184:187], a[72:75]\n"
"ds_read_b64_tr_b16 a[232:233], v238 offset:256\n"
"ds_read_b128 v[156:159], %[exR] offset:8192\n"
"v_mfma_f32_16x16x32_bf16 a[76:79], a[216:219], v[188:191], a[76:79]\n"
"ds_read_b64_tr_b16 a[234:235], v238 offset:6400\n"
"v_mfma_f32_16x16x32_bf16 a[80:83], a[216:219], v[192:195], a[80:83]\n"
"s_add_u32 s65, s65, 12288\n"
"buffer_load_dwordx4 %[dKV0], %[rV], s68 offen offset:0 nt lds\n"
"s_waitcnt lgkmcnt(12)\n"
"v_mfma_f32_16x16x32_bf16 a[84:87], a[220:223], v[184:187], a[84:87]\n"
"ds_read_b64_tr_b16 a[236:237], v239 offset:256\n"
"v_mfma_f32_16x16x32_bf16 a[88:91], a[220:223], v[188:191], a[88:91]\n"
"ds_read_b64_tr_b16 a[238:239], v239 offset:6400\n"
"v_mfma_f32_16x16x32_bf16 a[92:95], a[220:223], v[192:195], a[92:95]\n"
"buffer_load_dwordx4 %[dKV1], %[rV], s68 offen offset:1024 nt lds\n"
"s_waitcnt lgkmcnt(9)\n"
"v_mfma_f32_16x16x32_bf16 a[96:99], a[224:227], v[184:187], a[96:99]\n"
"s_add_u32 s61, %[m0b], s60\n"
"s_add_u32 s60, s60, 12288\n"
"s_cmp_eq_u32 s60, 61440\n"
"s_cselect_b32 s60, 0, s60\n"
"v_add_u32 v236, s60, %[rdK]\n"
"v_mfma_f32_16x16x32_bf16 a[100:103], a[224:227], v[188:191], a[100:103]\n"
"v_xor_b32 v237, 64, v236\n"
"v_mfma_f32_16x16x32_bf16 a[104:107], a[224:227], v[192:195], a[104:107]\n"
"s_waitcnt vmcnt(17) lgkmcnt(0)\n"
"s_barrier\n"
"ds_read_b128 a[216:219], v236 offset:0\n"
"v_mfma_f32_16x16x32_bf16 a[108:111], a[228:231], v[184:187], a[108:111]\n"
"ds_read_b128 a[220:223], v237 offset:0\n"
"v_mfma_f32_16x16x32_bf16 a[112:115], a[228:231], v[188:191], a[112:115]\n"
"v_mfma_f32_16x16x32_bf16 a[116:119], a[228:231], v[192:195], a[116:119]\n"
"v_mfma_f32_16x16x32_bf16 a[120:123], a[232:235], v[184:187], a[120:123]\n"
"ds_read_b128 a[224:227], v236 offset:128\n"
"v_mfma_f32_16x16x32_bf16 a[124:127], a[232:235], v[188:191], a[124:127]\n"
"v_mfma_f32_16x16x32_bf16 a[128:131], a[232:235], v[192:195], a[128:131]\n"
"buffer_load_dwordx4 %[dKV2], %[rV], s68 offen offset:2048 nt lds\n"
"v_mfma_f32_16x16x32_bf16 a[132:135], a[236:239], v[184:187], a[132:135]\n"
"ds_read_b128 a[228:231], v237 offset:128\n"
"v_mfma_f32_16x16x32_bf16 a[136:139], a[236:239], v[188:191], a[136:139]\n"
"v_mfma_f32_16x16x32_bf16 a[140:143], a[236:239], v[192:195], a[140:143]\n"
"v_mfma_f32_16x16x32_bf16 v[208:211], v[220:223], v[184:187], v[208:211]\n"
"v_mfma_f32_16x16x32_bf16 v[212:215], v[220:223], v[188:191], v[212:215]\n"
"v_mfma_f32_16x16x32_bf16 v[216:219], v[220:223], v[192:195], v[216:219]\n"
"s_mov_b32 s69, 12\n"
".p2align 3\n"
"Lc_%=:\n"
"s_waitcnt lgkmcnt(3)\n"
"v_mfma_f32_16x16x32_bf16 v[160:163], a[216:219], a[144:147], v[196:199]\n"
"ds_read_b128 a[232:235], v236 offset:256\n"
"v_mfma_f32_16x16x32_bf16 v[164:167], a[216:219], a[148:151], v[200:203]\n"
"v_mfma_f32_16x16x32_bf16 v[168:171], a[216:219], a[152:155], v[204:207]\n"
"s_mov_b32 m0, s61\n"
"s_add_u32 s68, s64, 3072\n"
"buffer_load_dwordx4 %[dKV0], %[rK], s64 offen offset:0 nt lds\n"
"s_waitcnt lgkmcnt(3)\n"
"v_mfma_f32_16x16x32_bf16 v[160:163], a[220:223], a[156:159], v[160:163]\n"
"ds_read_b128 a[236:239], v237 offset:256\n"
"v_mfma_f32_16x16x32_bf16 v[164:167], a[220:223], a[160:163], v[164:167]\n"
"v_mfma_f32_16x16x32_bf16 v[168:171], a[220:223], a[164:167], v[168:171]\n"
"s_waitcnt lgkmcnt(3)\n"
"v_mfma_f32_16x16x32_bf16 v[160:163], a[224:227], a[168:171], v[160:163]\n"
"ds_read_b128 a[216:219], v236 offset:6144\n"
"v_mfma_f32_16x16x32_bf16 v[164:167], a[224:227], a[172:175], v[164:167]\n"
"v_mfma_f32_16x16x32_bf16 v[168:171], a[224:227], a[176:179], v[168:171]\n"
"buffer_load_dwordx4 %[dKV1], %[rK], s64 offen offset:1024 nt lds\n"
"s_waitcnt lgkmcnt(3)\n"
"v_mfma_f32_16x16x32_bf16 v[160:163], a[228:231], a[180:183], v[160:163]\n"
"ds_read_b128 a[220:223], v237 offset:6144\n"
"v_mfma_f32_16x16x32_bf16 v[164:167], a[228:231], a[184:187], v[164:167]\n"
"v_mfma_f32_16x16x32_bf16 v[168:171], a[228:231], a[188:191], v[168:171]\n"
"s_waitcnt lgkmcnt(3)\n"
"v_mfma_f32_16x16x32_bf16 v[160:163], a[232:235], a[192:195], v[160:163]\n"
"ds_read_b128 a[224:227], v236 offset:6272\n"
"v_mfma_f32_16x16x32_bf16 v[164:167], a[232:235], a[196:199], v[164:167]\n"
"v_mfma_f32_16x16x32_bf16 v[168:171], a[232:235], a[200:203], v[168:171]\n"
"buffer_load_dwordx4 %[dKV2], %[rK], s64 offen offset:2048 nt lds\n"
"s_add_u32 m0, s61, 3072\n"
"s_waitcnt lgkmcnt(3)\n"
"v_mfma_f32_16x16x32_bf16 v[160:163], a[236:239], a[204:207], v[160:163]\n"
"ds_read_b128 a[228:231], v237 offset:6272\n"
"v_mfma_f32_16x16x32_bf16 v[164:167], a[236:239], a[208:211], v[164:167]\n"
"v_mfma_f32_16x16x32_bf16 v[168:171], a[236:239], a[212:215], v[168:171]\n"
"s_waitcnt lgkmcnt(3)\n"
"v_mfma_f32_16x16x32_bf16 v[172:175], a[216:219], a[144:147], v[196:199]\n"
"ds_read_b128 a[232:235], v236 offset:6400\n"
"v_mfma_f32_16x16x32_bf16 v[176:179], a[216:219], a[148:151], v[200:203]\n"
"v_mfma_f32_16x16x32_bf16 v[180:183], a[216:219], a[152:155], v[204:207]\n"
"s_add_u32 s64, s64, 12288\n"
"buffer_load_dwordx4 %[dKV0], %[rK], s68 offen offset:0 nt lds\n"
"s_waitcnt lgkmcnt(3)\n"
"v_mfma_f32_16x16x32_bf16 v[172:175], a[220:223], a[156:159], v[172:175]\n"
"ds_read_b128 a[236:239], v237 offset:6400\n"
"v_mfma_f32_16x16x32_bf16 v[176:179], a[220:223], a[160:163], v[176:179]\n"
"v_mfma_f32_16x16x32_bf16 v[180:183], a[220:223], a[164:167], v[180:183]\n"
"buffer_load_dwordx4 %[dKV1], %[rK], s68 offen offset:1024 nt lds\n"
"s_waitcnt lgkmcnt(3)\n"
"v_mfma_f32_16x16x32_bf16 v[172:175], a[224:227], a[168:171], v[172:175]\n"
"s_add_u32 s61, %[m0b], s60\n"
"s_add_u32 s60, s60, 12288\n"
"s_cmp_eq_u32 s60, 61440\n"
"s_cselect_b32 s60, 0, s60\n"
"v_add_u32 v236, s60, %[rdV]\n"
"v_mfma_f32_16x16x32_bf16 v[176:179], a[224:227], a[172:175], v[176:179]\n"
"v_xor_b32 v237, 32, v236\n"
"v_xor_b32 v238, 64, v236\n"
"v_xor_b32 v239, 96, v236\n"
"v_mfma_f32_16x16x32_bf16 v[180:183], a[224:227], a[176:179], v[180:183]\n"
"s_waitcnt vmcnt(17) lgkmcnt(0)\n"
"s_barrier\n"
"ds_read_b64_tr_b16 a[216:217], v236 offset:0\n"
"ds_read_b64_tr_b16 a[218:219], v236 offset:6144\n"
"v_mfma_f32_16x16x32_bf16 v[172:175], a[228:231], a[180:183], v[172:175]\n"
"ds_read_b64_tr_b16 a[220:221], v237 offset:0\n"
"v_mfma_f32_16x16x32_bf16 v[176:179], a[228:231], a[184:187], v[176:179]\n"
"ds_read_b64_tr_b16 a[222:223], v237 offset:6144\n"
"v_mfma_f32_16x16x32_bf16 v[180:183], a[228:231], a[188:191], v[180:183]\n"
"v_mfma_f32_16x16x32_bf16 v[172:175], a[232:235], a[192:195], v[172:175]\n"
"ds_read_b64_tr_b16 a[224:225], v238 offset:0\n"
"v_mfma_f32_16x16x32_bf16 v[176:179], a[232:235], a[196:199], v[176:179]\n"
"ds_read_b64_tr_b16 a[226:227], v238 offset:6144\n"
"v_mfma_f32_16x16x32_bf16 v[180:183], a[232:235], a[200:203], v[180:183]\n"
"buffer_load_dwordx4 %[dKV2], %[rK], s68 offen offset:2048 nt lds\n"
"v_mfma_f32_16x16x32_bf16 v[172:175], a[236:239], a[204:207], v[172:175]\n"
"ds_read_b64_tr_b16 a[228:229], v239 offset:0\n"
"v_mfma_f32_16x16x32_bf16 v[176:179], a[236:239], a[208:211], v[176:179]\n"
"ds_read_b64_tr_b16 a[230:231], v239 offset:6144\n"
"v_mfma_f32_16x16x32_bf16 v[180:183], a[236:239], a[212:215], v[180:183]\n"
"v_max3_f32 v242, v160, v161, v162\n"
"v_max3_f32 v242, v242, v163, v164\n"
"v_max3_f32 v242, v242, v165, v166\n"
"v_max3_f32 v242, v242, v167, v168\n"
"v_max3_f32 v242, v242, v169, v170\n"
"v_max_f32 v242, v242, v171\n"
"v_max3_f32 v243, v172, v173, v174\n"
"v_max3_f32 v243, v243, v175, v176\n"
"v_max3_f32 v243, v243, v177, v178\n"
"v_max3_f32 v243, v243, v179, v180\n"
"v_max3_f32 v243, v243, v181, v182\n"
"v_max3_f32 v243, v243, v183, v242\n"
"v_cmp_lt_f32 vcc, s70, v243\n"
"s_nop 1\n"
"s_cbranch_vccz 1\n"
"s_call_b64 s[76:77], Lslow_%=\n"
"v_exp_f32 v160, v160\n"
"v_exp_f32 v161, v161\n"
"v_exp_f32 v162, v162\n"
"v_exp_f32 v163, v163\n"
"v_exp_f32 v172, v172\n"
"v_exp_f32 v173, v173\n"
"v_exp_f32 v174, v174\n"
"v_exp_f32 v175, v175\n"
"v_cvt_pk_bf16_f32 v184, v160, v161\n"
"v_cvt_pk_bf16_f32 v185, v162, v163\n"
"v_exp_f32 v164, v164\n"
"v_exp_f32 v165, v165\n"
"v_exp_f32 v166, v166\n"
"v_exp_f32 v167, v167\n"
"v_cvt_pk_bf16_f32 v186, v172, v173\n"
"v_cvt_pk_bf16_f32 v187, v174, v175\n"
"v_exp_f32 v176, v176\n"
"v_exp_f32 v177, v177\n"
"v_exp_f32 v178, v178\n"
"v_exp_f32 v179, v179\n"
"v_cvt_pk_bf16_f32 v188, v164, v165\n"
"v_cvt_pk_bf16_f32 v189, v166, v167\n"
"v_exp_f32 v168, v168\n"
"v_exp_f32 v169, v169\n"
"v_exp_f32 v170, v170\n"
"v_exp_f32 v171, v171\n"
"v_cvt_pk_bf16_f32 v190, v176, v177\n"
"v_cvt_pk_bf16_f32 v191, v178, v179\n"
"v_exp_f32 v180, v180\n"
"v_exp_f32 v181, v181\n"
"v_exp_f32 v182, v182\n"
"v_exp_f32 v183, v183\n"
"v_cvt_pk_bf16_f32 v192, v168, v169\n"
"v_cvt_pk_bf16_f32 v193, v170, v171\n"
"v_cvt_pk_bf16_f32 v194, v180, v181\n"
"v_cvt_pk_bf16_f32 v195, v182, v183\n"
"s_waitcnt lgkmcnt(6)\n"
"v_mfma_f32_16x16x32_bf16 a[0:3], a[216:219], v[184:187], a[0:3]\n"
"ds_read_b64_tr_b16 a[232:233], v236 offset:128\n"
"v_mfma_f32_16x16x32_bf16 a[4:7], a[216:219], v[188:191], a[4:7]\n"
"ds_read_b64_tr_b16 a[234:235], v236 offset:6272\n"
"v_mfma_f32_16x16x32_bf16 a[8:11], a[216:219], v[192:195], a[8:11]\n"
"s_mov_b32 m0, s61\n"
"s_add_u32 s68, s65, 3072\n"
"buffer_load_dwordx4 %[dKV0], %[rV], s65 offen offset:0 nt lds\n"
"s_waitcnt lgkmcnt(6)\n"
"v_mfma_f32_16x16x32_bf16 a[12:15], a[220:223], v[184:187], a[12:15]\n"
"ds_read_b64_tr_b16 a[236:237], v237 offset:128\n"
"v_mfma_f32_16x16x32_bf16 a[16:19], a[220:223], v[188:191], a[16:19]\n"
"ds_read_b64_tr_b16 a[238:239], v237 offset:6272\n"
"v_mfma_f32_16x16x32_bf16 a[20:23], a[220:223], v[192:195], a[20:23]\n"
"s_waitcnt lgkmcnt(6)\n"
"v_mfma_f32_16x16x32_bf16 a[24:27], a[224:227], v[184:187], a[24:27]\n"
"ds_read_b64_tr_b16 a[216:217], v238 offset:128\n"
"v_mfma_f32_16x16x32_bf16 a[28:31], a[224:227], v[188:191], a[28:31]\n"
"ds_read_b64_tr_b16 a[218:219], v238 offset:6272\n"
"v_mfma_f32_16x16x32_bf16 a[32:35], a[224:227], v[192:195], a[32:35]\n"
"buffer_load_dwordx4 %[dKV1], %[rV], s65 offen offset:1024 nt lds\n"
"s_waitcnt lgkmcnt(6)\n"
"v_mfma_f32_16x16x32_bf16 a[36:39], a[228:231], v[184:187], a[36:39]\n"
"ds_read_b64_tr_b16 a[220:221], v239 offset:128\n"
"v_mfma_f32_16x16x32_bf16 a[40:43], a[228:231], v[188:191], a[40:43]\n"
"ds_read_b64_tr_b16 a[222:223], v239 offset:6272\n"
"v_mfma_f32_16x16x32_bf16 a[44:47], a[228:231], v[192:195], a[44:47]\n"
"s_waitcnt lgkmcnt(6)\n"
"v_mfma_f32_16x16x32_bf16 a[48:51], a[232:235], v[184:187], a[48:51]\n"
"ds_read_b64_tr_b16 a[224:225], v236 offset:256\n"
"v_mfma_f32_16x16x32_bf16 a[52:55], a[232:235], v[188:191], a[52:55]\n"
"ds_read_b64_tr_b16 a[226:227], v236 offset:6400\n"
"v_mfma_f32_16x16x32_bf16 a[56:59], a[232:235], v[192:195], a[56:59]\n"
"buffer_load_dwordx4 %[dKV2], %[rV], s65 offen offset:2048 nt lds\n"
"s_add_u32 m0, s61, 3072\n"
"s_waitcnt lgkmcnt(6)\n"
"v_mfma_f32_16x16x32_bf16 a[60:63], a[236:239], v[184:187], a[60:63]\n"
"ds_read_b64_tr_b16 a[228:229], v237 offset:256\n"
"v_mfma_f32_16x16x32_bf16 a[64:67], a[236:239], v[188:191], a[64:67]\n"
"ds_read_b64_tr_b16 a[230:231], v237 offset:6400\n"
"v_mfma_f32_16x16x32_bf16 a[68:71], a[236:239], v[192:195], a[68:71]\n"
"s_waitcnt lgkmcnt(6)\n"
"v_mfma_f32_16x16x32_bf16 a[72:75], a[216:219], v[184:187], a[72:75]\n"
"ds_read_b64_tr_b16 a[232:233], v238 offset:256\n"
"v_mfma_f32_16x16x32_bf16 a[76:79], a[216:219], v[188:191], a[76:79]\n"
"ds_read_b64_tr_b16 a[234:235], v238 offset:6400\n"
"v_mfma_f32_16x16x32_bf16 a[80:83], a[216:219], v[192:195], a[80:83]\n"
"s_add_u32 s65, s65, 12288\n"
"buffer_load_dwordx4 %[dKV0], %[rV], s68 offen offset:0 nt lds\n"
"s_waitcnt lgkmcnt(6)\n"
"v_mfma_f32_16x16x32_bf16 a[84:87], a[220:223], v[184:187], a[84:87]\n"
"ds_read_b64_tr_b16 a[236:237], v239 offset:256\n"
"v_mfma_f32_16x16x32_bf16 a[88:91], a[220:223], v[188:191], a[88:91]\n"
"ds_read_b64_tr_b16 a[238:239], v239 offset:6400\n"
"v_mfma_f32_16x16x32_bf16 a[92:95], a[220:223], v[192:195], a[92:95]\n"
"buffer_load_dwordx4 %[dKV1], %[rV], s68 offen offset:1024 nt lds\n"
"s_waitcnt lgkmcnt(6)\n"
"v_mfma_f32_16x16x32_bf16 a[96:99], a[224:227], v[184:187], a[96:99]\n"
"s_add_u32 s61, %[m0b], s60\n"
"s_add_u32 s60, s60, 12288\n"
"s_cmp_eq_u32 s60, 61440\n"
"s_cselect_b32 s60, 0, s60\n"
"v_add_u32 v236, s60, %[rdK]\n"
"v_mfma_f32_16x16x32_bf16 a[100:103], a[224:227], v[188:191], a[100:103]\n"
"v_xor_b32 v237, 64, v236\n"
"v_mfma_f32_16x16x32_bf16 a[104:107], a[224:227], v[192:195], a[104:107]\n"
"s_waitcnt vmcnt(17) lgkmcnt(0)\n"
"s_barrier\n"
"ds_read_b128 a[216:219], v236 offset:0\n"
"v_mfma_f32_16x16x32_bf16 a[108:111], a[228:231], v[184:187], a[108:111]\n"
"ds_read_b128 a[220:223], v237 offset:0\n"
"v_mfma_f32_16x16x32_bf16 a[112:115], a[228:231], v[188:191], a[112:115]\n"
"v_mfma_f32_16x16x32_bf16 a[116:119], a[228:231], v[192:195], a[116:119]\n"
"v_mfma_f32_16x16x32_bf16 a[120:123], a[232:235], v[184:187], a[120:123]\n"
"ds_read_b128 a[224:227], v236 offset:128\n"
"v_mfma_f32_16x16x32_bf16 a[124:127], a[232:235], v[188:191], a[124:127]\n"
"v_mfma_f32_16x16x32_bf16 a[128:131], a[232:235], v[192:195], a[128:131]\n"
"buffer_load_dwordx4 %[dKV2], %[rV], s68 offen offset:2048 nt lds\n"
"v_mfma_f32_16x16x32_bf16 a[132:135], a[236:239], v[184:187], a[132:135]\n"
"ds_read_b128 a[228:231], v237 offset:128\n"
"v_mfma_f32_16x16x32_bf16 a[136:139], a[236:239], v[188:191], a[136:139]\n"
"v_mfma_f32_16x16x32_bf16 a[140:143], a[236:239], v[192:195], a[140:143]\n"
"v_mfma_f32_16x16x32_bf16 v[208:211], v[220:223], v[184:187], v[208:211]\n"
"v_mfma_f32_16x16x32_bf16 v[212:215], v[220:223], v[188:191], v[212:215]\n"
"v_mfma_f32_16x16x32_bf16 v[216:219], v[220:223], v[192:195], v[216:219]\n"
"s_sub_u32 s69, s69, 1\n"
"s_cmp_lg_u32 s69, 0\n"
"s_cbranch_scc1 Lc_%=\n"
"s_waitcnt lgkmcnt(3)\n"
"v_mfma_f32_16x16x32_bf16 v[160:163], a[216:219], a[144:147], v[196:199]\n"
"ds_read_b128 a[232:235], v236 offset:256\n"
"v_mfma_f32_16x16x32_bf16 v[164:167], a[216:219], a[148:151], v[200:203]\n"
"s_getpc_b64 s[76:77]\n"
"Lpcd_%=:\n"
"v_min_u32 v255, Lend_%=-Lpcd_%=+1536, %[po]\n"
"s_nop 0\n"
"global_load_dword v254, v255, s[76:77] \n"
"v_mfma_f32_16x16x32_bf16 v[168:171], a[216:219], a[152:155], v[204:207]\n"
"s_mov_b32 m0, s61\n"
"s_add_u32 s68, %[wo0], s72\n"
"buffer_load_dwordx4 %[dX], %[rWo], s68 offen offset:0 lds\n"
"s_add_u32 s68, s68, 9216\n"
"s_waitcnt lgkmcnt(3)\n"
"v_mfma_f32_16x16x32_bf16 v[160:163], a[220:223], a[156:159], v[160:163]\n"
"ds_read_b128 a[236:239], v237 offset:256\n"
"v_mfma_f32_16x16x32_bf16 v[164:167], a[220:223], a[160:163], v[164:167]\n"
"v_mfma_f32_16x16x32_bf16 v[168:171], a[220:223], a[164:167], v[168:171]\n"
"s_waitcnt lgkmcnt(3)\n"
"v_mfma_f32_16x16x32_bf16 v[160:163], a[224:227], a[168:171], v[160:163]\n"
"ds_read_b128 a[216:219], v236 offset:6144\n"
"v_mfma_f32_16x16x32_bf16 v[164:167], a[224:227], a[172:175], v[164:167]\n"
"v_mfma_f32_16x16x32_bf16 v[168:171], a[224:227], a[176:179], v[168:171]\n"
"buffer_load_dwordx4 %[dX], %[rWo], s68 offen offset:1024 lds\n"
"s_add_u32 s68, s68, 9216\n"
"s_waitcnt lgkmcnt(3)\n"
"v_mfma_f32_16x16x32_bf16 v[160:163], a[228:231], a[180:183], v[160:163]\n"
"ds_read_b128 a[220:223], v237 offset:6144\n"
"v_mfma_f32_16x16x32_bf16 v[164:167], a[228:231], a[184:187], v[164:167]\n"
"v_mfma_f32_16x16x32_bf16 v[168:171], a[228:231], a[188:191], v[168:171]\n"
"s_waitcnt lgkmcnt(3)\n"
"v_mfma_f32_16x16x32_bf16 v[160:163], a[232:235], a[192:195], v[160:163]\n"
"ds_read_b128 a[224:227], v236 offset:6272\n"
"v_mfma_f32_16x16x32_bf16 v[164:167], a[232:235], a[196:199], v[164:167]\n"
"v_mfma_f32_16x16x32_bf16 v[168:171], a[232:235], a[200:203], v[168:171]\n"
"buffer_load_dwordx4 %[dX], %[rWo], s68 offen offset:2048 lds\n"
"s_add_u32 m0, s61, 3072\n"
"s_add_u32 s68, s68, 12288\n"
"s_waitcnt lgkmcnt(3)\n"
"v_mfma_f32_16x16x32_bf16 v[160:163], a[236:239], a[204:207], v[160:163]\n"
"ds_read_b128 a[228:231], v237 offset:6272\n"
"v_mfma_f32_16x16x32_bf16 v[164:167], a[236:239], a[208:211], v[164:167]\n"
"v_mfma_f32_16x16x32_bf16 v[168:171], a[236:239], a[212:215], v[168:171]\n"
"s_waitcnt lgkmcnt(3)\n"
"v_mfma_f32_16x16x32_bf16 v[172:175], a[216:219], a[144:147], v[196:199]\n"
"ds_read_b128 a[232:235], v236 offset:6400\n"
"v_mfma_f32_16x16x32_bf16 v[176:179], a[216:219], a[148:151], v[200:203]\n"
"v_mfma_f32_16x16x32_bf16 v[180:183], a[216:219], a[152:155], v[204:207]\n"
"buffer_load_dwordx4 %[dX], %[rWo], s68 offen offset:0 lds\n"
"s_add_u32 s68, s68, 9216\n"
"s_waitcnt lgkmcnt(3)\n"
"v_mfma_f32_16x16x32_bf16 v[172:175], a[220:223], a[156:159], v[172:175]\n"
"ds_read_b128 a[236:239], v237 offset:6400\n"
"v_mfma_f32_16x16x32_bf16 v[176:179], a[220:223], a[160:163], v[176:179]\n"
"v_mfma_f32_16x16x32_bf16 v[180:183], a[220:223], a[164:167], v[180:183]\n"
"buffer_load_dwordx4 %[dX], %[rWo], s68 offen offset:1024 lds\n"
"s_add_u32 s68, s68, 9216\n"
"s_waitcnt lgkmcnt(3)\n"
"v_mfma_f32_16x16x32_bf16 v[172:175], a[224:227], a[168:171], v[172:175]\n"
"s_add_u32 s61, %[m0b], s60\n"
"s_add_u32 s60, s60, 12288\n"
"s_cmp_eq_u32 s60, 61440\n"
"s_cselect_b32 s60, 0, s60\n"
"v_add_u32 v236, s60, %[rdV]\n"
"v_mfma_f32_16x16x32_bf16 v[176:179], a[224:227], a[172:175], v[176:179]\n"
"v_xor_b32 v237, 32, v236\n"
"v_xor_b32 v238, 64, v236\n"
"v_xor_b32 v239, 96, v236\n"
"v_mfma_f32_16x16x32_bf16 v[180:183], a[224:227], a[176:179], v[180:183]\n"
"s_waitcnt vmcnt(18) lgkmcnt(0)\n"
"s_barrier\n"
"ds_read_b64_tr_b16 a[216:217], v236 offset:0\n"
"ds_read_b64_tr_b16 a[218:219], v236 offset:6144\n"
"v_mfma_f32_16x16x32_bf16 v[172:175], a[228:231], a[180:183], v[172:175]\n"
"ds_read_b64_tr_b16 a[220:221], v237 offset:0\n"
"v_mfma_f32_16x16x32_bf16 v[176:179], a[228:231], a[184:187], v[176:179]\n"
"ds_read_b64_tr_b16 a[222:223], v237 offset:6144\n"
"v_mfma_f32_16x16x32_bf16 v[180:183], a[228:231], a[188:191], v[180:183]\n"
"v_mfma_f32_16x16x32_bf16 v[172:175], a[232:235], a[192:195], v[172:175]\n"
"ds_read_b64_tr_b16 a[224:225], v238 offset:0\n"
"v_mfma_f32_16x16x32_bf16 v[176:179], a[232:235], a[196:199], v[176:179]\n"
"ds_read_b64_tr_b16 a[226:227], v238 offset:6144\n"
"v_mfma_f32_16x16x32_bf16 v[180:183], a[232:235], a[200:203], v[180:183]\n"
"buffer_load_dwordx4 %[dX], %[rWo], s68 offen offset:2048 lds\n"
"v_mfma_f32_16x16x32_bf16 v[172:175], a[236:239], a[204:207], v[172:175]\n"
"ds_read_b64_tr_b16 a[228:229], v239 offset:0\n"
"v_mfma_f32_16x16x32_bf16 v[176:179], a[236:239], a[208:211], v[176:179]\n"
"ds_read_b64_tr_b16 a[230:231], v239 offset:6144\n"
"v_mfma_f32_16x16x32_bf16 v[180:183], a[236:239], a[212:215], v[180:183]\n"
"v_max3_f32 v242, v160, v161, v162\n"
"v_max3_f32 v242, v242, v163, v164\n"
"v_max3_f32 v242, v242, v165, v166\n"
"v_max3_f32 v242, v242, v167, v168\n"
"v_max3_f32 v242, v242, v169, v170\n"
"v_max_f32 v242, v242, v171\n"
"v_max3_f32 v243, v172, v173, v174\n"
"v_max3_f32 v243, v243, v175, v176\n"
"v_max3_f32 v243, v243, v177, v178\n"
"v_max3_f32 v243, v243, v179, v180\n"
"v_max3_f32 v243, v243, v181, v182\n"
"v_max3_f32 v243, v243, v183, v242\n"
"v_cmp_lt_f32 vcc, s70, v243\n"
"s_nop 1\n"
"s_cbranch_vccz 1\n"
"s_call_b64 s[76:77], Lslow_%=\n"
"v_exp_f32 v160, v160\n"
"v_exp_f32 v161, v161\n"
"v_exp_f32 v162, v162\n"
"v_exp_f32 v163, v163\n"
"v_exp_f32 v172, v172\n"
"v_exp_f32 v173, v173\n"
"v_exp_f32 v174, v174\n"
"v_exp_f32 v175, v175\n"
"v_cvt_pk_bf16_f32 v184, v160, v161\n"
"v_cvt_pk_bf16_f32 v185, v162, v163\n"
"v_exp_f32 v164, v164\n"
"v_exp_f32 v165, v165\n"
"v_exp_f32 v166, v166\n"
"v_exp_f32 v167, v167\n"
"v_cvt_pk_bf16_f32 v186, v172, v173\n"
"v_cvt_pk_bf16_f32 v187, v174, v175\n"
"v_exp_f32 v176, v176\n"
"v_exp_f32 v177, v177\n"
"v_exp_f32 v178, v178\n"
"v_exp_f32 v179, v179\n"
"v_cvt_pk_bf16_f32 v188, v164, v165\n"
"v_cvt_pk_bf16_f32 v189, v166, v167\n"
"v_exp_f32 v168, v168\n"
"v_exp_f32 v169, v169\n"
"v_exp_f32 v170, v170\n"
"v_exp_f32 v171, v171\n"
"v_cvt_pk_bf16_f32 v190, v176, v177\n"
"v_cvt_pk_bf16_f32 v191, v178, v179\n"
"v_exp_f32 v180, v180\n"
"v_exp_f32 v181, v181\n"
"v_exp_f32 v182, v182\n"
"v_exp_f32 v183, v183\n"
"v_cvt_pk_bf16_f32 v192, v168, v169\n"
"v_cvt_pk_bf16_f32 v193, v170, v171\n"
"v_cvt_pk_bf16_f32 v194, v180, v181\n"
"v_cvt_pk_bf16_f32 v195, v182, v183\n"
"s_waitcnt lgkmcnt(6)\n"
"v_mfma_f32_16x16x32_bf16 a[0:3], a[216:219], v[184:187], a[0:3]\n"
"ds_read_b64_tr_b16 a[232:233], v236 offset:128\n"
"v_mfma_f32_16x16x32_bf16 a[4:7], a[216:219], v[188:191], a[4:7]\n"
"ds_read_b64_tr_b16 a[234:235], v236 offset:6272\n"
"v_mfma_f32_16x16x32_bf16 a[8:11], a[216:219], v[192:195], a[8:11]\n"
"s_mov_b32 m0, s61\n"
"s_add_u32 s68, %[wo0], s72\n"
"s_add_u32 s68, s68, 122880\n"
"buffer_load_dwordx4 %[dX], %[rWo], s68 offen offset:0 lds\n"
"s_add_u32 s68, s68, 9216\n"
"s_waitcnt lgkmcnt(6)\n"
"v_mfma_f32_16x16x32_bf16 a[12:15], a[220:223], v[184:187], a[12:15]\n"
"ds_read_b64_tr_b16 a[236:237], v237 offset:128\n"
"v_mfma_f32_16x16x32_bf16 a[16:19], a[220:223], v[188:191], a[16:19]\n"
"ds_read_b64_tr_b16 a[238:239], v237 offset:6272\n"
"v_mfma_f32_16x16x32_bf16 a[20:23], a[220:223], v[192:195], a[20:23]\n"
"s_waitcnt lgkmcnt(6)\n"
"v_mfma_f32_16x16x32_bf16 a[24:27], a[224:227], v[184:187], a[24:27]\n"
"ds_read_b64_tr_b16 a[216:217], v238 offset:128\n"
"v_mfma_f32_16x16x32_bf16 a[28:31], a[224:227], v[188:191], a[28:31]\n"
"ds_read_b64_tr_b16 a[218:219], v238 offset:6272\n"
"v_mfma_f32_16x16x32_bf16 a[32:35], a[224:227], v[192:195], a[32:35]\n"
"buffer_load_dwordx4 %[dX], %[rWo], s68 offen offset:1024 lds\n"
"s_add_u32 s68, s68, 9216\n"
"s_waitcnt lgkmcnt(6)\n"
"v_mfma_f32_16x16x32_bf16 a[36:39], a[228:231], v[184:187], a[36:39]\n"
"ds_read_b64_tr_b16 a[220:221], v239 offset:128\n"
"v_mfma_f32_16x16x32_bf16 a[40:43], a[228:231], v[188:191], a[40:43]\n"
"ds_read_b64_tr_b16 a[222:223], v239 offset:6272\n"
"v_mfma_f32_16x16x32_bf16 a[44:47], a[228:231], v[192:195], a[44:47]\n"
"s_waitcnt lgkmcnt(6)\n"
"v_mfma_f32_16x16x32_bf16 a[48:51], a[232:235], v[184:187], a[48:51]\n"
"ds_read_b64_tr_b16 a[224:225], v236 offset:256\n"
"v_mfma_f32_16x16x32_bf16 a[52:55], a[232:235], v[188:191], a[52:55]\n"
"ds_read_b64_tr_b16 a[226:227], v236 offset:6400\n"
"v_mfma_f32_16x16x32_bf16 a[56:59], a[232:235], v[192:195], a[56:59]\n"
"buffer_load_dwordx4 %[dX], %[rWo], s68 offen offset:2048 lds\n"
"s_add_u32 m0, s61, 3072\n"
"s_add_u32 s68, s68, 12288\n"
"s_waitcnt lgkmcnt(6)\n"
"v_mfma_f32_16x16x32_bf16 a[60:63], a[236:239], v[184:187], a[60:63]\n"
"ds_read_b64_tr_b16 a[228:229], v237 offset:256\n"
"v_mfma_f32_16x16x32_bf16 a[64:67], a[236:239], v[188:191], a[64:67]\n"
"ds_read_b64_tr_b16 a[230:231], v237 offset:6400\n"
"v_mfma_f32_16x16x32_bf16 a[68:71], a[236:239], v[192:195], a[68:71]\n"
"s_waitcnt lgkmcnt(6)\n"
"v_mfma_f32_16x16x32_bf16 a[72:75], a[216:219], v[184:187], a[72:75]\n"
"ds_read_b64_tr_b16 a[232:233], v238 offset:256\n"
"v_mfma_f32_16x16x32_bf16 a[76:79], a[216:219], v[188:191], a[76:79]\n"
"ds_read_b64_tr_b16 a[234:235], v238 offset:6400\n"
"v_mfma_f32_16x16x32_bf16 a[80:83], a[216:219], v[192:195], a[80:83]\n"
"buffer_load_dwordx4 %[dX], %[rWo], s68 offen offset:0 lds\n"
"s_add_u32 s68, s68, 9216\n"
"s_waitcnt lgkmcnt(6)\n"
"v_mfma_f32_16x16x32_bf16 a[84:87], a[220:223], v[184:187], a[84:87]\n"
"ds_read_b64_tr_b16 a[236:237], v239 offset:256\n"
"v_mfma_f32_16x16x32_bf16 a[88:91], a[220:223], v[188:191], a[88:91]\n"
"ds_read_b64_tr_b16 a[238:239], v239 offset:6400\n"
"v_mfma_f32_16x16x32_bf16 a[92:95], a[220:223], v[192:195], a[92:95]\n"
"buffer_load_dwordx4 %[dX], %[rWo], s68 offen offset:1024 lds\n"
"s_add_u32 s68, s68, 9216\n"
"s_waitcnt lgkmcnt(6)\n"
"v_mfma_f32_16x16x32_bf16 a[96:99], a[224:227], v[184:187], a[96:99]\n"
"s_add_u32 s61, %[m0b], s60\n"
"s_add_u32 s60, s60, 12288\n"
"s_cmp_eq_u32 s60, 61440\n"
"s_cselect_b32 s60, 0, s60\n"
"v_add_u32 v236, s60, %[rdK]\n"
"v_mfma_f32_16x16x32_bf16 a[100:103], a[224:227], v[188:191], a[100:103]\n"
"v_xor_b32 v237, 64, v236\n"
"v_mfma_f32_16x16x32_bf16 a[104:107], a[224:227], v[192:195], a[104:107]\n"
"s_waitcnt vmcnt(18) lgkmcnt(0)\n"
"s_barrier\n"
"ds_read_b128 a[216:219], v236 offset:0\n"
"v_mfma_f32_16x16x32_bf16 a[108:111], a[228:231], v[184:187], a[108:111]\n"
"ds_read_b128 a[220:223], v237 offset:0\n"
"v_mfma_f32_16x16x32_bf16 a[112:115], a[228:231], v[188:191], a[112:115]\n"
"v_mfma_f32_16x16x32_bf16 a[116:119], a[228:231], v[192:195], a[116:119]\n"
"v_mfma_f32_16x16x32_bf16 a[120:123], a[232:235], v[184:187], a[120:123]\n"
"ds_read_b128 a[224:227], v236 offset:128\n"
"v_mfma_f32_16x16x32_bf16 a[124:127], a[232:235], v[188:191], a[124:127]\n"
"v_mfma_f32_16x16x32_bf16 a[128:131], a[232:235], v[192:195], a[128:131]\n"
"buffer_load_dwordx4 %[dX], %[rWo], s68 offen offset:2048 lds\n"
"v_mfma_f32_16x16x32_bf16 a[132:135], a[236:239], v[184:187], a[132:135]\n"
"ds_read_b128 a[228:231], v237 offset:128\n"
"v_mfma_f32_16x16x32_bf16 a[136:139], a[236:239], v[188:191], a[136:139]\n"
"v_mfma_f32_16x16x32_bf16 a[140:143], a[236:239], v[192:195], a[140:143]\n"
"v_mfma_f32_16x16x32_bf16 v[208:211], v[220:223], v[184:187], v[208:211]\n"
"v_mfma_f32_16x16x32_bf16 v[212:215], v[220:223], v[188:191], v[212:215]\n"
"v_mfma_f32_16x16x32_bf16 v[216:219], v[220:223], v[192:195], v[216:219]\n"
"s_waitcnt lgkmcnt(3)\n"
"v_mfma_f32_16x16x32_bf16 v[160:163], a[216:219], a[144:147], v[196:199]\n"
"ds_read_b128 a[232:235], v236 offset:256\n"
"v_mfma_f32_16x16x32_bf16 v[164:167], a[216:219], a[148:151], v[200:203]\n"
"v_mfma_f32_16x16x32_bf16 v[168:171], a[216:219], a[152:155], v[204:207]\n"
"s_mov_b32 m0, s61\n"
"s_add_u32 s68, %[wo1], s72\n"
"buffer_load_dwordx4 %[dX], %[rWo], s68 offen offset:0 lds\n"
"s_add_u32 s68, s68, 9216\n"
"s_waitcnt lgkmcnt(3)\n"
"v_mfma_f32_16x16x32_bf16 v[160:163], a[220:223], a[156:159], v[160:163]\n"
"ds_read_b128 a[236:239], v237 offset:256\n"
"v_mfma_f32_16x16x32_bf16 v[164:167], a[220:223], a[160:163], v[164:167]\n"
"v_mfma_f32_16x16x32_bf16 v[168:171], a[220:223], a[164:167], v[168:171]\n"
"s_waitcnt lgkmcnt(3)\n"
"v_mfma_f32_16x16x32_bf16 v[160:163], a[224:227], a[168:171], v[160:163]\n"
"ds_read_b128 a[216:219], v236 offset:6144\n"
"v_mfma_f32_16x16x32_bf16 v[164:167], a[224:227], a[172:175], v[164:167]\n"
"v_mfma_f32_16x16x32_bf16 v[168:171], a[224:227], a[176:179], v[168:171]\n"
"buffer_load_dwordx4 %[dX], %[rWo], s68 offen offset:1024 lds\n"
"s_add_u32 s68, s68, 9216\n"
"s_waitcnt lgkmcnt(3)\n"
"v_mfma_f32_16x16x32_bf16 v[160:163], a[228:231], a[180:183], v[160:163]\n"
"ds_read_b128 a[220:223], v237 offset:6144\n"
"v_mfma_f32_16x16x32_bf16 v[164:167], a[228:231], a[184:187], v[164:167]\n"
"v_mfma_f32_16x16x32_bf16 v[168:171], a[228:231], a[188:191], v[168:171]\n"
"s_waitcnt lgkmcnt(3)\n"
"v_mfma_f32_16x16x32_bf16 v[160:163], a[232:235], a[192:195], v[160:163]\n"
"ds_read_b128 a[224:227], v236 offset:6272\n"
"v_mfma_f32_16x16x32_bf16 v[164:167], a[232:235], a[196:199], v[164:167]\n"
"v_mfma_f32_16x16x32_bf16 v[168:171], a[232:235], a[200:203], v[168:171]\n"
"buffer_load_dwordx4 %[dX], %[rWo], s68 offen offset:2048 lds\n"
"s_add_u32 m0, s61, 3072\n"
"s_add_u32 s68, s68, 12288\n"
"s_waitcnt lgkmcnt(3)\n"
"v_mfma_f32_16x16x32_bf16 v[160:163], a[236:239], a[204:207], v[160:163]\n"
"ds_read_b128 a[228:231], v237 offset:6272\n"
"v_mfma_f32_16x16x32_bf16 v[164:167], a[236:239], a[208:211], v[164:167]\n"
"v_mfma_f32_16x16x32_bf16 v[168:171], a[236:239], a[212:215], v[168:171]\n"
"s_waitcnt lgkmcnt(3)\n"
"v_mfma_f32_16x16x32_bf16 v[172:175], a[216:219], a[144:147], v[196:199]\n"
"ds_read_b128 a[232:235], v236 offset:6400\n"
"v_mfma_f32_16x16x32_bf16 v[176:179], a[216:219], a[148:151], v[200:203]\n"
"v_mfma_f32_16x16x32_bf16 v[180:183], a[216:219], a[152:155], v[204:207]\n"
"buffer_load_dwordx4 %[dX], %[rWo], s68 offen offset:0 lds\n"
"s_add_u32 s68, s68, 9216\n"
"s_waitcnt lgkmcnt(3)\n"
"v_mfma_f32_16x16x32_bf16 v[172:175], a[220:223], a[156:159], v[172:175]\n"
"ds_read_b128 a[236:239], v237 offset:6400\n"
"v_mfma_f32_16x16x32_bf16 v[176:179], a[220:223], a[160:163], v[176:179]\n"
"v_mfma_f32_16x16x32_bf16 v[180:183], a[220:223], a[164:167], v[180:183]\n"
"buffer_load_dwordx4 %[dX], %[rWo], s68 offen offset:1024 lds\n"
"s_add_u32 s68, s68, 9216\n"
"s_waitcnt lgkmcnt(3)\n"
"v_mfma_f32_16x16x32_bf16 v[172:175], a[224:227], a[168:171], v[172:175]\n"
"s_add_u32 s61, %[m0b], s60\n"
"s_add_u32 s60, s60, 12288\n"
"s_cmp_eq_u32 s60, 61440\n"
"s_cselect_b32 s60, 0, s60\n"
"v_add_u32 v236, s60, %[rdV]\n"
"v_mfma_f32_16x16x32_bf16 v[176:179], a[224:227], a[172:175], v[176:179]\n"
"v_xor_b32 v237, 32, v236\n"
"v_xor_b32 v238, 64, v236\n"
"v_xor_b32 v239, 96, v236\n"
"v_mfma_f32_16x16x32_bf16 v[180:183], a[224:227], a[176:179], v[180:183]\n"
"s_waitcnt vmcnt(18) lgkmcnt(0)\n"
"s_barrier\n"
"ds_read_b64_tr_b16 a[216:217], v236 offset:0\n"
"ds_read_b64_tr_b16 a[218:219], v236 offset:6144\n"
"v_mfma_f32_16x16x32_bf16 v[172:175], a[228:231], a[180:183], v[172:175]\n"
"ds_read_b64_tr_b16 a[220:221], v237 offset:0\n"
"v_mfma_f32_16x16x32_bf16 v[176:179], a[228:231], a[184:187], v[176:179]\n"
"ds_read_b64_tr_b16 a[222:223], v237 offset:6144\n"
"v_mfma_f32_16x16x32_bf16 v[180:183], a[228:231], a[188:191], v[180:183]\n"
"v_mfma_f32_16x16x32_bf16 v[172:175], a[232:235], a[192:195], v[172:175]\n"
"ds_read_b64_tr_b16 a[224:225], v238 offset:0\n"
"v_mfma_f32_16x16x32_bf16 v[176:179], a[232:235], a[196:199], v[176:179]\n"
"ds_read_b64_tr_b16 a[226:227], v238 offset:6144\n"
"v_mfma_f32_16x16x32_bf16 v[180:183], a[232:235], a[200:203], v[180:183]\n"
"buffer_load_dwordx4 %[dX], %[rWo], s68 offen offset:2048 lds\n"
"v_mfma_f32_16x16x32_bf16 v[172:175], a[236:239], a[204:207], v[172:175]\n"
"ds_read_b64_tr_b16 a[228:229], v239 offset:0\n"
"v_mfma_f32_16x16x32_bf16 v[176:179], a[236:239], a[208:211], v[176:179]\n"
"ds_read_b64_tr_b16 a[230:231], v239 offset:6144\n"
"v_mfma_f32_16x16x32_bf16 v[180:183], a[236:239], a[212:215], v[180:183]\n"
"v_max3_f32 v242, v160, v161, v162\n"
"v_max3_f32 v242, v242, v163, v164\n"
"v_max3_f32 v242, v242, v165, v166\n"
"v_max3_f32 v242, v242, v167, v168\n"
"v_max3_f32 v242, v242, v169, v170\n"
"v_max_f32 v242, v242, v171\n"
"v_max3_f32 v243, v172, v173, v174\n"
"v_max3_f32 v243, v243, v175, v176\n"
"v_max3_f32 v243, v243, v177, v178\n"
"v_max3_f32 v243, v243, v179, v180\n"
"v_max3_f32 v243, v243, v181, v182\n"
"v_max3_f32 v243, v243, v183, v242\n"
"v_cmp_lt_f32 vcc, s70, v243\n"
"s_nop 1\n"
"s_cbranch_vccz 1\n"
"s_call_b64 s[76:77], Lslow_%=\n"
"v_exp_f32 v160, v160\n"
"v_exp_f32 v161, v161\n"
"v_exp_f32 v162, v162\n"
"v_exp_f32 v163, v163\n"
"v_exp_f32 v172, v172\n"
"v_exp_f32 v173, v173\n"
"v_exp_f32 v174, v174\n"
"v_exp_f32 v175, v175\n"
"v_cvt_pk_bf16_f32 v184, v160, v161\n"
"v_cvt_pk_bf16_f32 v185, v162, v163\n"
"v_exp_f32 v164, v164\n"
"v_exp_f32 v165, v165\n"
"v_exp_f32 v166, v166\n"
"v_exp_f32 v167, v167\n"
"v_cvt_pk_bf16_f32 v186, v172, v173\n"
"v_cvt_pk_bf16_f32 v187, v174, v175\n"
"v_exp_f32 v176, v176\n"
"v_exp_f32 v177, v177\n"
"v_exp_f32 v178, v178\n"
"v_exp_f32 v179, v179\n"
"v_cvt_pk_bf16_f32 v188, v164, v165\n"
"v_cvt_pk_bf16_f32 v189, v166, v167\n"
"v_exp_f32 v168, v168\n"
"v_exp_f32 v169, v169\n"
"v_exp_f32 v170, v170\n"
"v_exp_f32 v171, v171\n"
"v_cvt_pk_bf16_f32 v190, v176, v177\n"
"v_cvt_pk_bf16_f32 v191, v178, v179\n"
"v_exp_f32 v180, v180\n"
"v_exp_f32 v181, v181\n"
"v_exp_f32 v182, v182\n"
"v_exp_f32 v183, v183\n"
"v_cvt_pk_bf16_f32 v192, v168, v169\n"
"v_cvt_pk_bf16_f32 v193, v170, v171\n"
"v_cvt_pk_bf16_f32 v194, v180, v181\n"
"v_cvt_pk_bf16_f32 v195, v182, v183\n"
"s_waitcnt lgkmcnt(6)\n"
"v_mfma_f32_16x16x32_bf16 a[0:3], a[216:219], v[184:187], a[0:3]\n"
"ds_read_b64_tr_b16 a[232:233], v236 offset:128\n"
"v_mfma_f32_16x16x32_bf16 a[4:7], a[216:219], v[188:191], a[4:7]\n"
"ds_read_b64_tr_b16 a[234:235], v236 offset:6272\n"
"v_mfma_f32_16x16x32_bf16 a[8:11], a[216:219], v[192:195], a[8:11]\n"
"s_mov_b32 m0, s61\n"
"s_add_u32 s68, %[wo1], s72\n"
"s_add_u32 s68, s68, 122880\n"
"buffer_load_dwordx4 %[dX], %[rWo], s68 offen offset:0 lds\n"
"s_add_u32 s68, s68, 9216\n"
"s_waitcnt lgkmcnt(6)\n"
"v_mfma_f32_16x16x32_bf16 a[12:15], a[220:223], v[184:187], a[12:15]\n"
"ds_read_b64_tr_b16 a[236:237], v237 offset:128\n"
"v_mfma_f32_16x16x32_bf16 a[16:19], a[220:223], v[188:191], a[16:19]\n"
"ds_read_b64_tr_b16 a[238:239], v237 offset:6272\n"
"v_mfma_f32_16x16x32_bf16 a[20:23], a[220:223], v[192:195], a[20:23]\n"
"s_waitcnt lgkmcnt(6)\n"
"v_mfma_f32_16x16x32_bf16 a[24:27], a[224:227], v[184:187], a[24:27]\n"
"ds_read_b64_tr_b16 a[216:217], v238 offset:128\n"
"v_mfma_f32_16x16x32_bf16 a[28:31], a[224:227], v[188:191], a[28:31]\n"
"ds_read_b64_tr_b16 a[218:219], v238 offset:6272\n"
"v_mfma_f32_16x16x32_bf16 a[32:35], a[224:227], v[192:195], a[32:35]\n"
"buffer_load_dwordx4 %[dX], %[rWo], s68 offen offset:1024 lds\n"
"s_add_u32 s68, s68, 9216\n"
"s_waitcnt lgkmcnt(6)\n"
"v_mfma_f32_16x16x32_bf16 a[36:39], a[228:231], v[184:187], a[36:39]\n"
"ds_read_b64_tr_b16 a[220:221], v239 offset:128\n"
"v_mfma_f32_16x16x32_bf16 a[40:43], a[228:231], v[188:191], a[40:43]\n"
"ds_read_b64_tr_b16 a[222:223], v239 offset:6272\n"
"v_mfma_f32_16x16x32_bf16 a[44:47], a[228:231], v[192:195], a[44:47]\n"
"s_waitcnt lgkmcnt(6)\n"
"v_mfma_f32_16x16x32_bf16 a[48:51], a[232:235], v[184:187], a[48:51]\n"
"ds_read_b64_tr_b16 a[224:225], v236 offset:256\n"
"v_mfma_f32_16x16x32_bf16 a[52:55], a[232:235], v[188:191], a[52:55]\n"
"ds_read_b64_tr_b16 a[226:227], v236 offset:6400\n"
"v_mfma_f32_16x16x32_bf16 a[56:59], a[232:235], v[192:195], a[56:59]\n"
"buffer_load_dwordx4 %[dX], %[rWo], s68 offen offset:2048 lds\n"
"s_add_u32 m0, s61, 3072\n"
"s_add_u32 s68, s68, 12288\n"
"s_waitcnt lgkmcnt(6)\n"
"v_mfma_f32_16x16x32_bf16 a[60:63], a[236:239], v[184:187], a[60:63]\n"
"ds_read_b64_tr_b16 a[228:229], v237 offset:256\n"
"v_mfma_f32_16x16x32_bf16 a[64:67], a[236:239], v[188:191], a[64:67]\n"
"ds_read_b64_tr_b16 a[230:231], v237 offset:6400\n"
"v_mfma_f32_16x16x32_bf16 a[68:71], a[236:239], v[192:195], a[68:71]\n"
"s_waitcnt lgkmcnt(6)\n"
"v_mfma_f32_16x16x32_bf16 a[72:75], a[216:219], v[184:187], a[72:75]\n"
"ds_read_b64_tr_b16 a[232:233], v238 offset:256\n"
"v_mfma_f32_16x16x32_bf16 a[76:79], a[216:219], v[188:191], a[76:79]\n"
"ds_read_b64_tr_b16 a[234:235], v238 offset:6400\n"
"v_mfma_f32_16x16x32_bf16 a[80:83], a[216:219], v[192:195], a[80:83]\n"
"buffer_load_dwordx4 %[dX], %[rWo], s68 offen offset:0 lds\n"
"s_add_u32 s68, s68, 9216\n"
"s_waitcnt lgkmcnt(6)\n"
"v_mfma_f32_16x16x32_bf16 a[84:87], a[220:223], v[184:187], a[84:87]\n"
"ds_read_b64_tr_b16 a[236:237], v239 offset:256\n"
"v_mfma_f32_16x16x32_bf16 a[88:91], a[220:223], v[188:191], a[88:91]\n"
"ds_read_b64_tr_b16 a[238:239], v239 offset:6400\n"
"v_mfma_f32_16x16x32_bf16 a[92:95], a[220:223], v[192:195], a[92:95]\n"
"buffer_load_dwordx4 %[dX], %[rWo], s68 offen offset:1024 lds\n"
"s_add_u32 s68, s68, 9216\n"
"s_waitcnt lgkmcnt(6)\n"
"v_mfma_f32_16x16x32_bf16 a[96:99], a[224:227], v[184:187], a[96:99]\n"
"s_add_u32 s61, %[m0b], s60\n"
"s_add_u32 s60, s60, 12288\n"
"s_cmp_eq_u32 s60, 61440\n"
"s_cselect_b32 s60, 0, s60\n"
"v_add_u32 v236, s60, %[rdO]\n"
"v_mfma_f32_16x16x32_bf16 a[100:103], a[224:227], v[188:191], a[100:103]\n"
"v_xor_b32 v237, 32, v236\n"
"v_xor_b32 v238, 64, v236\n"
"v_xor_b32 v239, 96, v236\n"
"v_mfma_f32_16x16x32_bf16 a[104:107], a[224:227], v[192:195], a[104:107]\n"
"s_waitcnt vmcnt(17) lgkmcnt(0)\n"
"s_barrier\n"
"ds_read_b64_tr_b16 a[216:217], v236 offset:0\n"
"ds_read_b64_tr_b16 a[218:219], v236 offset:2048\n"
"v_mfma_f32_16x16x32_bf16 a[108:111], a[228:231], v[184:187], a[108:111]\n"
"ds_read_b64_tr_b16 a[220:221], v236 offset:4096\n"
"v_mfma_f32_16x16x32_bf16 a[112:115], a[228:231], v[188:191], a[112:115]\n"
"ds_read_b64_tr_b16 a[222:223], v236 offset:6144\n"
"v_mfma_f32_16x16x32_bf16 a[116:119], a[228:231], v[192:195], a[116:119]\n"
"v_mfma_f32_16x16x32_bf16 a[120:123], a[232:235], v[184:187], a[120:123]\n"
"ds_read_b64_tr_b16 a[224:225], v236 offset:8192\n"
"v_mfma_f32_16x16x32_bf16 a[124:127], a[232:235], v[188:191], a[124:127]\n"
"ds_read_b64_tr_b16 a[226:227], v236 offset:10240\n"
"v_mfma_f32_16x16x32_bf16 a[128:131], a[232:235], v[192:195], a[128:131]\n"
"buffer_load_dwordx4 %[dX], %[rWo], s68 offen offset:2048 lds\n"
"v_mfma_f32_16x16x32_bf16 a[132:135], a[236:239], v[184:187], a[132:135]\n"
"ds_read_b64_tr_b16 a[228:229], v237 offset:0\n"
"v_mfma_f32_16x16x32_bf16 a[136:139], a[236:239], v[188:191], a[136:139]\n"
"ds_read_b64_tr_b16 a[230:231], v237 offset:2048\n"
"v_mfma_f32_16x16x32_bf16 a[140:143], a[236:239], v[192:195], a[140:143]\n"
"v_mfma_f32_16x16x32_bf16 v[208:211], v[220:223], v[184:187], v[208:211]\n"
"v_mfma_f32_16x16x32_bf16 v[212:215], v[220:223], v[188:191], v[212:215]\n"
"v_mfma_f32_16x16x32_bf16 v[216:219], v[220:223], v[192:195], v[216:219]\n"
"s_nop 5\n"
"v_rcp_f32 v250, v208\n"
"v_rcp_f32 v252, v212\n"
"v_rcp_f32 v254, v216\n"
"v_accvgpr_read_b32 v242, a0\n"
"v_accvgpr_read_b32 v243, a1\n"
"v_accvgpr_read_b32 v244, a2\n"
"v_accvgpr_read_b32 v245, a3\n"
"v_pk_mul_f32 v[242:243], v[242:243], v[250:251] op_sel_hi:[1,0]\n"
"v_pk_mul_f32 v[244:245], v[244:245], v[250:251] op_sel_hi:[1,0]\n"
"v_accvgpr_read_b32 v246, a4\n"
"v_accvgpr_read_b32 v247, a5\n"
"v_accvgpr_read_b32 v248, a6\n"
"v_accvgpr_read_b32 v249, a7\n"
"v_cvt_pk_bf16_f32 v242, v242, v243\n"
"v_cvt_pk_bf16_f32 v244, v244, v245\n"
"v_accvgpr_write_b32 a144, v242\n"
"v_accvgpr_write_b32 a145, v244\n"
"v_pk_mul_f32 v[246:247], v[246:247], v[252:253] op_sel_hi:[1,0]\n"
"v_pk_mul_f32 v[248:249], v[248:249], v[252:253] op_sel_hi:[1,0]\n"
"v_accvgpr_read_b32 v242, a8\n"
"v_accvgpr_read_b32 v243, a9\n"
"v_accvgpr_read_b32 v244, a10\n"
"v_accvgpr_read_b32 v245, a11\n"
"v_cvt_pk_bf16_f32 v246, v246, v247\n"
"v_cvt_pk_bf16_f32 v248, v248, v249\n"
"v_accvgpr_write_b32 a148, v246\n"
"v_accvgpr_write_b32 a149, v248\n"
"v_pk_mul_f32 v[242:243], v[242:243], v[254:255] op_sel_hi:[1,0]\n"
"v_pk_mul_f32 v[244:245], v[244:245], v[254:255] op_sel_hi:[1,0]\n"
"v_accvgpr_read_b32 v246, a12\n"
"v_accvgpr_read_b32 v247, a13\n"
"v_accvgpr_read_b32 v248, a14\n"
"v_accvgpr_read_b32 v249, a15\n"
"v_cvt_pk_bf16_f32 v242, v242, v243\n"
"v_cvt_pk_bf16_f32 v244, v244, v245\n"
"v_accvgpr_write_b32 a152, v242\n"
"v_accvgpr_write_b32 a153, v244\n"
"v_pk_mul_f32 v[246:247], v[246:247], v[250:251] op_sel_hi:[1,0]\n"
"v_pk_mul_f32 v[248:249], v[248:249], v[250:251] op_sel_hi:[1,0]\n"
"v_accvgpr_read_b32 v242, a16\n"
"v_accvgpr_read_b32 v243, a17\n"
"v_accvgpr_read_b32 v244, a18\n"
"v_accvgpr_read_b32 v245, a19\n"
"v_cvt_pk_bf16_f32 v246, v246, v247\n"
"v_cvt_pk_bf16_f32 v248, v248, v249\n"
"v_accvgpr_write_b32 a146, v246\n"
"v_accvgpr_write_b32 a147, v248\n"
"v_pk_mul_f32 v[242:243], v[242:243], v[252:253] op_sel_hi:[1,0]\n"
"v_pk_mul_f32 v[244:245], v[244:245], v[252:253] op_sel_hi:[1,0]\n"
"v_accvgpr_read_b32 v246, a20\n"
"v_accvgpr_read_b32 v247, a21\n"
"v_accvgpr_read_b32 v248, a22\n"
"v_accvgpr_read_b32 v249, a23\n"
"v_cvt_pk_bf16_f32 v242, v242, v243\n"
"v_cvt_pk_bf16_f32 v244, v244, v245\n"
"v_accvgpr_write_b32 a150, v242\n"
"v_accvgpr_write_b32 a151, v244\n"
"v_pk_mul_f32 v[246:247], v[246:247], v[254:255] op_sel_hi:[1,0]\n"
"v_pk_mul_f32 v[248:249], v[248:249], v[254:255] op_sel_hi:[1,0]\n"
"v_accvgpr_read_b32 v242, a24\n"
"v_accvgpr_read_b32 v243, a25\n"
"v_accvgpr_read_b32 v244, a26\n"
"v_accvgpr_read_b32 v245, a27\n"
"v_cvt_pk_bf16_f32 v246, v246, v247\n"
"v_cvt_pk_bf16_f32 v248, v248, v249\n"
"v_accvgpr_write_b32 a154, v246\n"
"v_accvgpr_write_b32 a155, v248\n"
"v_pk_mul_f32 v[242:243], v[242:243], v[250:251] op_sel_hi:[1,0]\n"
"v_pk_mul_f32 v[244:245], v[244:245], v[250:251] op_sel_hi:[1,0]\n"
"v_accvgpr_read_b32 v246, a28\n"
"v_accvgpr_read_b32 v247, a29\n"
"v_accvgpr_read_b32 v248, a30\n"
"v_accvgpr_read_b32 v249, a31\n"
"v_cvt_pk_bf16_f32 v242, v242, v243\n"
"v_cvt_pk_bf16_f32 v244, v244, v245\n"
"v_accvgpr_write_b32 a156, v242\n"
"v_accvgpr_write_b32 a157, v244\n"
"v_pk_mul_f32 v[246:247], v[246:247], v[252:253] op_sel_hi:[1,0]\n"
"v_pk_mul_f32 v[248:249], v[248:249], v[252:253] op_sel_hi:[1,0]\n"
"v_accvgpr_read_b32 v242, a32\n"
"v_accvgpr_read_b32 v243, a33\n"
"v_accvgpr_read_b32 v244, a34\n"
"v_accvgpr_read_b32 v245, a35\n"
"v_cvt_pk_bf16_f32 v246, v246, v247\n"
"v_cvt_pk_bf16_f32 v248, v248, v249\n"
"v_accvgpr_write_b32 a160, v246\n"
"v_accvgpr_write_b32 a161, v248\n"
"v_pk_mul_f32 v[242:243], v[242:243], v[254:255] op_sel_hi:[1,0]\n"
"v_pk_mul_f32 v[244:245], v[244:245], v[254:255] op_sel_hi:[1,0]\n"
"v_accvgpr_read_b32 v246, a36\n"
"v_accvgpr_read_b32 v247, a37\n"
"v_accvgpr_read_b32 v248, a38\n"
"v_accvgpr_read_b32 v249, a39\n"
"v_cvt_pk_bf16_f32 v242, v242, v243\n"
"v_cvt_pk_bf16_f32 v244, v244, v245\n"
"v_accvgpr_write_b32 a164, v242\n"
"v_accvgpr_write_b32 a165, v244\n"
"v_pk_mul_f32 v[246:247], v[246:247], v[250:251] op_sel_hi:[1,0]\n"
"v_pk_mul_f32 v[248:249], v[248:249], v[250:251] op_sel_hi:[1,0]\n"
"v_accvgpr_read_b32 v242, a40\n"
"v_accvgpr_read_b32 v243, a41\n"
"v_accvgpr_read_b32 v244, a42\n"
"v_accvgpr_read_b32 v245, a43\n"
"v_cvt_pk_bf16_f32 v246, v246, v247\n"
"v_cvt_pk_bf16_f32 v248, v248, v249\n"
"v_accvgpr_write_b32 a158, v246\n"
"v_accvgpr_write_b32 a159, v248\n"
"v_pk_mul_f32 v[242:243], v[242:243], v[252:253] op_sel_hi:[1,0]\n"
"v_pk_mul_f32 v[244:245], v[244:245], v[252:253] op_sel_hi:[1,0]\n"
"v_accvgpr_read_b32 v246, a44\n"
"v_accvgpr_read_b32 v247, a45\n"
"v_accvgpr_read_b32 v248, a46\n"
"v_accvgpr_read_b32 v249, a47\n"
"v_cvt_pk_bf16_f32 v242, v242, v243\n"
"v_cvt_pk_bf16_f32 v244, v244, v245\n"
"v_accvgpr_write_b32 a162, v242\n"
"v_accvgpr_write_b32 a163, v244\n"
"v_pk_mul_f32 v[246:247], v[246:247], v[254:255] op_sel_hi:[1,0]\n"
"v_pk_mul_f32 v[248:249], v[248:249], v[254:255] op_sel_hi:[1,0]\n"
"v_accvgpr_read_b32 v242, a48\n"
"v_accvgpr_read_b32 v243, a49\n"
"v_accvgpr_read_b32 v244, a50\n"
"v_accvgpr_read_b32 v245, a51\n"
"v_cvt_pk_bf16_f32 v246, v246, v247\n"
"v_cvt_pk_bf16_f32 v248, v248, v249\n"
"v_accvgpr_write_b32 a166, v246\n"
"v_accvgpr_write_b32 a167, v248\n"
"v_pk_mul_f32 v[242:243], v[242:243], v[250:251] op_sel_hi:[1,0]\n"
"v_pk_mul_f32 v[244:245], v[244:245], v[250:251] op_sel_hi:[1,0]\n"
"v_accvgpr_read_b32 v246, a52\n"
"v_accvgpr_read_b32 v247, a53\n"
"v_accvgpr_read_b32 v248, a54\n"
"v_accvgpr_read_b32 v249, a55\n"
"v_cvt_pk_bf16_f32 v242, v242, v243\n"
"v_cvt_pk_bf16_f32 v244, v244, v245\n"
"v_accvgpr_write_b32 a168, v242\n"
"v_accvgpr_write_b32 a169, v244\n"
"v_pk_mul_f32 v[246:247], v[246:247], v[252:253] op_sel_hi:[1,0]\n"
"v_pk_mul_f32 v[248:249], v[248:249], v[252:253] op_sel_hi:[1,0]\n"
"v_accvgpr_read_b32 v242, a56\n"
"v_accvgpr_read_b32 v243, a57\n"
"v_accvgpr_read_b32 v244, a58\n"
"v_accvgpr_read_b32 v245, a59\n"
"v_cvt_pk_bf16_f32 v246, v246, v247\n"
"v_cvt_pk_bf16_f32 v248, v248, v249\n"
"v_accvgpr_write_b32 a172, v246\n"
"v_accvgpr_write_b32 a173, v248\n"
"v_pk_mul_f32 v[242:243], v[242:243], v[254:255] op_sel_hi:[1,0]\n"
"v_pk_mul_f32 v[244:245], v[244:245], v[254:255] op_sel_hi:[1,0]\n"
"v_accvgpr_read_b32 v246, a60\n"
"v_accvgpr_read_b32 v247, a61\n"
"v_accvgpr_read_b32 v248, a62\n"
"v_accvgpr_read_b32 v249, a63\n"
"v_cvt_pk_bf16_f32 v242, v242, v243\n"
"v_cvt_pk_bf16_f32 v244, v244, v245\n"
"v_accvgpr_write_b32 a176, v242\n"
"v_accvgpr_write_b32 a177, v244\n"
"v_pk_mul_f32 v[246:247], v[246:247], v[250:251] op_sel_hi:[1,0]\n"
"v_pk_mul_f32 v[248:249], v[248:249], v[250:251] op_sel_hi:[1,0]\n"
"v_accvgpr_read_b32 v242, a64\n"
"v_accvgpr_read_b32 v243, a65\n"
"v_accvgpr_read_b32 v244, a66\n"
"v_accvgpr_read_b32 v245, a67\n"
"v_cvt_pk_bf16_f32 v246, v246, v247\n"
"v_cvt_pk_bf16_f32 v248, v248, v249\n"
"v_accvgpr_write_b32 a170, v246\n"
"v_accvgpr_write_b32 a171, v248\n"
"v_pk_mul_f32 v[242:243], v[242:243], v[252:253] op_sel_hi:[1,0]\n"
"v_pk_mul_f32 v[244:245], v[244:245], v[252:253] op_sel_hi:[1,0]\n"
"v_accvgpr_read_b32 v246, a68\n"
"v_accvgpr_read_b32 v247, a69\n"
"v_accvgpr_read_b32 v248, a70\n"
"v_accvgpr_read_b32 v249, a71\n"
"v_cvt_pk_bf16_f32 v242, v242, v243\n"
"v_cvt_pk_bf16_f32 v244, v244, v245\n"
"v_accvgpr_write_b32 a174, v242\n"
"v_accvgpr_write_b32 a175, v244\n"
"v_pk_mul_f32 v[246:247], v[246:247], v[254:255] op_sel_hi:[1,0]\n"
"v_pk_mul_f32 v[248:249], v[248:249], v[254:255] op_sel_hi:[1,0]\n"
"v_accvgpr_read_b32 v242, a72\n"
"v_accvgpr_read_b32 v243, a73\n"
"v_accvgpr_read_b32 v244, a74\n"
"v_accvgpr_read_b32 v245, a75\n"
"v_cvt_pk_bf16_f32 v246, v246, v247\n"
"v_cvt_pk_bf16_f32 v248, v248, v249\n"
"v_accvgpr_write_b32 a178, v246\n"
"v_accvgpr_write_b32 a179, v248\n"
"v_pk_mul_f32 v[242:243], v[242:243], v[250:251] op_sel_hi:[1,0]\n"
"v_pk_mul_f32 v[244:245], v[244:245], v[250:251] op_sel_hi:[1,0]\n"
"v_accvgpr_read_b32 v246, a76\n"
"v_accvgpr_read_b32 v247, a77\n"
"v_accvgpr_read_b32 v248, a78\n"
"v_accvgpr_read_b32 v249, a79\n"
"v_cvt_pk_bf16_f32 v242, v242, v243\n"
"v_cvt_pk_bf16_f32 v244, v244, v245\n"
"v_accvgpr_write_b32 a180, v242\n"
"v_accvgpr_write_b32 a181, v244\n"
"v_pk_mul_f32 v[246:247], v[246:247], v[252:253] op_sel_hi:[1,0]\n"
"v_pk_mul_f32 v[248:249], v[248:249], v[252:253] op_sel_hi:[1,0]\n"
"v_accvgpr_read_b32 v242, a80\n"
"v_accvgpr_read_b32 v243, a81\n"
"v_accvgpr_read_b32 v244, a82\n"
"v_accvgpr_read_b32 v245, a83\n"
"v_cvt_pk_bf16_f32 v246, v246, v247\n"
"v_cvt_pk_bf16_f32 v248, v248, v249\n"
"v_accvgpr_write_b32 a184, v246\n"
"v_accvgpr_write_b32 a185, v248\n"
"v_pk_mul_f32 v[242:243], v[242:243], v[254:255] op_sel_hi:[1,0]\n"
"v_pk_mul_f32 v[244:245], v[244:245], v[254:255] op_sel_hi:[1,0]\n"
"v_accvgpr_read_b32 v246, a84\n"
"v_accvgpr_read_b32 v247, a85\n"
"v_accvgpr_read_b32 v248, a86\n"
"v_accvgpr_read_b32 v249, a87\n"
"v_cvt_pk_bf16_f32 v242, v242, v243\n"
"v_cvt_pk_bf16_f32 v244, v244, v245\n"
"v_accvgpr_write_b32 a188, v242\n"
"v_accvgpr_write_b32 a189, v244\n"
"v_pk_mul_f32 v[246:247], v[246:247], v[250:251] op_sel_hi:[1,0]\n"
"v_pk_mul_f32 v[248:249], v[248:249], v[250:251] op_sel_hi:[1,0]\n"
"v_accvgpr_read_b32 v242, a88\n"
"v_accvgpr_read_b32 v243, a89\n"
"v_accvgpr_read_b32 v244, a90\n"
"v_accvgpr_read_b32 v245, a91\n"
"v_cvt_pk_bf16_f32 v246, v246, v247\n"
"v_cvt_pk_bf16_f32 v248, v248, v249\n"
"v_accvgpr_write_b32 a182, v246\n"
"v_accvgpr_write_b32 a183, v248\n"
"v_pk_mul_f32 v[242:243], v[242:243], v[252:253] op_sel_hi:[1,0]\n"
"v_pk_mul_f32 v[244:245], v[244:245], v[252:253] op_sel_hi:[1,0]\n"
"v_accvgpr_read_b32 v246, a92\n"
"v_accvgpr_read_b32 v247, a93\n"
"v_accvgpr_read_b32 v248, a94\n"
"v_accvgpr_read_b32 v249, a95\n"
"v_cvt_pk_bf16_f32 v242, v242, v243\n"
"v_cvt_pk_bf16_f32 v244, v244, v245\n"
"v_accvgpr_write_b32 a186, v242\n"
"v_accvgpr_write_b32 a187, v244\n"
"v_pk_mul_f32 v[246:247], v[246:247], v[254:255] op_sel_hi:[1,0]\n"
"v_pk_mul_f32 v[248:249], v[248:249], v[254:255] op_sel_hi:[1,0]\n"
"v_accvgpr_read_b32 v242, a96\n"
"v_accvgpr_read_b32 v243, a97\n"
"v_accvgpr_read_b32 v244, a98\n"
"v_accvgpr_read_b32 v245, a99\n"
"v_cvt_pk_bf16_f32 v246, v246, v247\n"
"v_cvt_pk_bf16_f32 v248, v248, v249\n"
"v_accvgpr_write_b32 a190, v246\n"
"v_accvgpr_write_b32 a191, v248\n"
"v_pk_mul_f32 v[242:243], v[242:243], v[250:251] op_sel_hi:[1,0]\n"
"v_pk_mul_f32 v[244:245], v[244:245], v[250:251] op_sel_hi:[1,0]\n"
"v_accvgpr_read_b32 v246, a100\n"
"v_accvgpr_read_b32 v247, a101\n"
"v_accvgpr_read_b32 v248, a102\n"
"v_accvgpr_read_b32 v249, a103\n"
"v_cvt_pk_bf16_f32 v242, v242, v243\n"
"v_cvt_pk_bf16_f32 v244, v244, v245\n"
"v_accvgpr_write_b32 a192, v242\n"
"v_accvgpr_write_b32 a193, v244\n"
"v_pk_mul_f32 v[246:247], v[246:247], v[252:253] op_sel_hi:[1,0]\n"
"v_pk_mul_f32 v[248:249], v[248:249], v[252:253] op_sel_hi:[1,0]\n"
"v_accvgpr_read_b32 v242, a104\n"
"v_accvgpr_read_b32 v243, a105\n"
"v_accvgpr_read_b32 v244, a106\n"
"v_accvgpr_read_b32 v245, a107\n"
"v_cvt_pk_bf16_f32 v246, v246, v247\n"
"v_cvt_pk_bf16_f32 v248, v248, v249\n"
"v_accvgpr_write_b32 a196, v246\n"
"v_accvgpr_write_b32 a197, v248\n"
"v_pk_mul_f32 v[242:243], v[242:243], v[254:255] op_sel_hi:[1,0]\n"
"v_pk_mul_f32 v[244:245], v[244:245], v[254:255] op_sel_hi:[1,0]\n"
"v_accvgpr_read_b32 v246, a108\n"
"v_accvgpr_read_b32 v247, a109\n"
"v_accvgpr_read_b32 v248, a110\n"
"v_accvgpr_read_b32 v249, a111\n"
"v_cvt_pk_bf16_f32 v242, v242, v243\n"
"v_cvt_pk_bf16_f32 v244, v244, v245\n"
"v_accvgpr_write_b32 a200, v242\n"
"v_accvgpr_write_b32 a201, v244\n"
"v_pk_mul_f32 v[246:247], v[246:247], v[250:251] op_sel_hi:[1,0]\n"
"v_pk_mul_f32 v[248:249], v[248:249], v[250:251] op_sel_hi:[1,0]\n"
"v_accvgpr_read_b32 v242, a112\n"
"v_accvgpr_read_b32 v243, a113\n"
"v_accvgpr_read_b32 v244, a114\n"
"v_accvgpr_read_b32 v245, a115\n"
"v_cvt_pk_bf16_f32 v246, v246, v247\n"
"v_cvt_pk_bf16_f32 v248, v248, v249\n"
"v_accvgpr_write_b32 a194, v246\n"
"v_accvgpr_write_b32 a195, v248\n"
"v_pk_mul_f32 v[242:243], v[242:243], v[252:253] op_sel_hi:[1,0]\n"
"v_pk_mul_f32 v[244:245], v[244:245], v[252:253] op_sel_hi:[1,0]\n"
"v_accvgpr_read_b32 v246, a116\n"
"v_accvgpr_read_b32 v247, a117\n"
"v_accvgpr_read_b32 v248, a118\n"
"v_accvgpr_read_b32 v249, a119\n"
"v_cvt_pk_bf16_f32 v242, v242, v243\n"
"v_cvt_pk_bf16_f32 v244, v244, v245\n"
"v_accvgpr_write_b32 a198, v242\n"
"v_accvgpr_write_b32 a199, v244\n"
"v_pk_mul_f32 v[246:247], v[246:247], v[254:255] op_sel_hi:[1,0]\n"
"v_pk_mul_f32 v[248:249], v[248:249], v[254:255] op_sel_hi:[1,0]\n"
"v_accvgpr_read_b32 v242, a120\n"
"v_accvgpr_read_b32 v243, a121\n"
"v_accvgpr_read_b32 v244, a122\n"
"v_accvgpr_read_b32 v245, a123\n"
"v_cvt_pk_bf16_f32 v246, v246, v247\n"
"v_cvt_pk_bf16_f32 v248, v248, v249\n"
"v_accvgpr_write_b32 a202, v246\n"
"v_accvgpr_write_b32 a203, v248\n"
"v_pk_mul_f32 v[242:243], v[242:243], v[250:251] op_sel_hi:[1,0]\n"
"v_pk_mul_f32 v[244:245], v[244:245], v[250:251] op_sel_hi:[1,0]\n"
"v_accvgpr_read_b32 v246, a124\n"
"v_accvgpr_read_b32 v247, a125\n"
"v_accvgpr_read_b32 v248, a126\n"
"v_accvgpr_read_b32 v249, a127\n"
"v_cvt_pk_bf16_f32 v242, v242, v243\n"
"v_cvt_pk_bf16_f32 v244, v244, v245\n"
"v_accvgpr_write_b32 a204, v242\n"
"v_accvgpr_write_b32 a205, v244\n"
"v_pk_mul_f32 v[246:247], v[246:247], v[252:253] op_sel_hi:[1,0]\n"
"v_pk_mul_f32 v[248:249], v[248:249], v[252:253] op_sel_hi:[1,0]\n"
"v_accvgpr_read_b32 v242, a128\n"
"v_accvgpr_read_b32 v243, a129\n"
"v_accvgpr_read_b32 v244, a130\n"
"v_accvgpr_read_b32 v245, a131\n"
"v_cvt_pk_bf16_f32 v246, v246, v247\n"
"v_cvt_pk_bf16_f32 v248, v248, v249\n"
"v_accvgpr_write_b32 a208, v246\n"
"v_accvgpr_write_b32 a209, v248\n"
"v_pk_mul_f32 v[242:243], v[242:243], v[254:255] op_sel_hi:[1,0]\n"
"v_pk_mul_f32 v[244:245], v[244:245], v[254:255] op_sel_hi:[1,0]\n"
"v_accvgpr_read_b32 v246, a132\n"
"v_accvgpr_read_b32 v247, a133\n"
"v_accvgpr_read_b32 v248, a134\n"
"v_accvgpr_read_b32 v249, a135\n"
"v_cvt_pk_bf16_f32 v242, v242, v243\n"
"v_cvt_pk_bf16_f32 v244, v244, v245\n"
"v_accvgpr_write_b32 a212, v242\n"
"v_accvgpr_write_b32 a213, v244\n"
"v_pk_mul_f32 v[246:247], v[246:247], v[250:251] op_sel_hi:[1,0]\n"
"v_pk_mul_f32 v[248:249], v[248:249], v[250:251] op_sel_hi:[1,0]\n"
"v_accvgpr_read_b32 v242, a136\n"
"v_accvgpr_read_b32 v243, a137\n"
"v_accvgpr_read_b32 v244, a138\n"
"v_accvgpr_read_b32 v245, a139\n"
"v_cvt_pk_bf16_f32 v246, v246, v247\n"
"v_cvt_pk_bf16_f32 v248, v248, v249\n"
"v_accvgpr_write_b32 a206, v246\n"
"v_accvgpr_write_b32 a207, v248\n"
"v_pk_mul_f32 v[242:243], v[242:243], v[252:253] op_sel_hi:[1,0]\n"
"v_pk_mul_f32 v[244:245], v[244:245], v[252:253] op_sel_hi:[1,0]\n"
"v_accvgpr_read_b32 v246, a140\n"
"v_accvgpr_read_b32 v247, a141\n"
"v_accvgpr_read_b32 v248, a142\n"
"v_accvgpr_read_b32 v249, a143\n"
"v_cvt_pk_bf16_f32 v242, v242, v243\n"
"v_cvt_pk_bf16_f32 v244, v244, v245\n"
"v_accvgpr_write_b32 a210, v242\n"
"v_accvgpr_write_b32 a211, v244\n"
"v_pk_mul_f32 v[246:247], v[246:247], v[254:255] op_sel_hi:[1,0]\n"
"v_pk_mul_f32 v[248:249], v[248:249], v[254:255] op_sel_hi:[1,0]\n"
"v_cvt_pk_bf16_f32 v246, v246, v247\n"
"v_cvt_pk_bf16_f32 v248, v248, v249\n"
"v_accvgpr_write_b32 a214, v246\n"
"v_accvgpr_write_b32 a215, v248\n"
"s_waitcnt lgkmcnt(6)\n"
"v_mfma_f32_16x16x32_bf16 v[160:163], a[216:219], v[16:19], 0\n"
"ds_read_b64_tr_b16 a[232:233], v237 offset:4096\n"
"ds_write_b128 %[exW], a[144:147] offset:0\n"
"v_mfma_f32_16x16x32_bf16 v[164:167], a[216:219], v[20:23], 0\n"
"ds_read_b64_tr_b16 a[234:235], v237 offset:6144\n"
"ds_write_b128 %[exW], a[148:151] offset:1024\n"
"v_mfma_f32_16x16x32_bf16 v[168:171], a[216:219], v[24:27], 0\n"
"s_mov_b32 m0, s61\n"
"s_add_u32 s68, %[wo0], s72\n"
"s_add_u32 s68, s68, 983040\n"
"buffer_load_dwordx4 %[dX], %[rWo], s68 offen offset:0 lds\n"
"s_add_u32 s68, s68, 9216\n"
"s_waitcnt lgkmcnt(8)\n"
"v_mfma_f32_16x16x32_bf16 v[160:163], a[220:223], v[28:31], v[160:163]\n"
"ds_read_b64_tr_b16 a[236:237], v237 offset:8192\n"
"v_mfma_f32_16x16x32_bf16 v[164:167], a[220:223], v[32:35], v[164:167]\n"
"ds_read_b64_tr_b16 a[238:239], v237 offset:10240\n"
"v_mfma_f32_16x16x32_bf16 v[168:171], a[220:223], v[36:39], v[168:171]\n"
"s_waitcnt lgkmcnt(8)\n"
"v_mfma_f32_16x16x32_bf16 v[160:163], a[224:227], v[40:43], v[160:163]\n"
"ds_read_b64_tr_b16 a[216:217], v238 offset:0\n"
"v_mfma_f32_16x16x32_bf16 v[164:167], a[224:227], v[44:47], v[164:167]\n"
"ds_read_b64_tr_b16 a[218:219], v238 offset:2048\n"
"ds_write_b128 %[exW], a[152:155] offset:2048\n"
"v_mfma_f32_16x16x32_bf16 v[168:171], a[224:227], v[48:51], v[168:171]\n"
"buffer_load_dwordx4 %[dX], %[rWo], s68 offen offset:1024 lds\n"
"s_add_u32 s68, s68, 9216\n"
"s_waitcnt lgkmcnt(9)\n"
"v_mfma_f32_16x16x32_bf16 v[172:175], a[228:231], v[16:19], 0\n"
"ds_read_b64_tr_b16 a[220:221], v238 offset:4096\n"
"v_mfma_f32_16x16x32_bf16 v[176:179], a[228:231], v[20:23], 0\n"
"ds_read_b64_tr_b16 a[222:223], v238 offset:6144\n"
"v_mfma_f32_16x16x32_bf16 v[180:183], a[228:231], v[24:27], 0\n"
"ds_write_b128 %[exW], a[156:159] offset:3072\n"
"ds_write_b128 %[exW], a[160:163] offset:4096\n"
"s_waitcnt lgkmcnt(10)\n"
"v_mfma_f32_16x16x32_bf16 v[172:175], a[232:235], v[28:31], v[172:175]\n"
"ds_read_b64_tr_b16 a[224:225], v238 offset:8192\n"
"ds_write_b128 %[exW], a[164:167] offset:5120\n"
"v_mfma_f32_16x16x32_bf16 v[176:179], a[232:235], v[32:35], v[176:179]\n"
"ds_read_b64_tr_b16 a[226:227], v238 offset:10240\n"
"ds_write_b128 %[exW], a[168:171] offset:6144\n"
"v_mfma_f32_16x16x32_bf16 v[180:183], a[232:235], v[36:39], v[180:183]\n"
"buffer_load_dwordx4 %[dX], %[rWo], s68 offen offset:2048 lds\n"
"s_add_u32 m0, s61, 3072\n"
"s_add_u32 s68, s68, 12288\n"
"s_waitcnt lgkmcnt(11)\n"
"v_mfma_f32_16x16x32_bf16 v[172:175], a[236:239], v[40:43], v[172:175]\n"
"ds_read_b64_tr_b16 a[228:229], v239 offset:0\n"
"v_mfma_f32_16x16x32_bf16 v[176:179], a[236:239], v[44:47], v[176:179]\n"
"ds_read_b64_tr_b16 a[230:231], v239 offset:2048\n"
"v_mfma_f32_16x16x32_bf16 v[180:183], a[236:239], v[48:51], v[180:183]\n"
"ds_write_b128 %[exW], a[172:175] offset:7168\n"
"s_waitcnt lgkmcnt(12)\n"
"v_mfma_f32_16x16x32_bf16 v[184:187], a[216:219], v[16:19], 0\n"
"ds_read_b64_tr_b16 a[232:233], v239 offset:4096\n"
"ds_write_b128 %[exW], a[176:179] offset:8192\n"
"v_mfma_f32_16x16x32_bf16 v[188:191], a[216:219], v[20:23], 0\n"
"ds_read_b64_tr_b16 a[234:235], v239 offset:6144\n"
"v_mfma_f32_16x16x32_bf16 v[192:195], a[216:219], v[24:27], 0\n"
"buffer_load_dwordx4 %[dX], %[rWo], s68 offen offset:0 lds\n"
"s_add_u32 s68, s68, 9216\n"
"s_waitcnt lgkmcnt(12)\n"
"v_mfma_f32_16x16x32_bf16 v[184:187], a[220:223], v[28:31], v[184:187]\n"
"ds_read_b64_tr_b16 a[236:237], v239 offset:8192\n"
"v_mfma_f32_16x16x32_bf16 v[188:191], a[220:223], v[32:35], v[188:191]\n"
"ds_read_b64_tr_b16 a[238:239], v239 offset:10240\n"
"v_mfma_f32_16x16x32_bf16 v[192:195], a[220:223], v[36:39], v[192:195]\n"
"buffer_load_dwordx4 %[dX], %[rWo], s68 offen offset:1024 lds\n"
"s_add_u32 s68, s68, 9216\n"
"s_waitcnt lgkmcnt(9)\n"
"v_mfma_f32_16x16x32_bf16 v[184:187], a[224:227], v[40:43], v[184:187]\n"
"s_add_u32 s61, %[m0b], s60\n"
"s_add_u32 s60, s60, 12288\n"
"s_cmp_eq_u32 s60, 61440\n"
"s_cselect_b32 s60, 0, s60\n"
"v_add_u32 v236, s60, %[rdO]\n"
"v_mfma_f32_16x16x32_bf16 v[188:191], a[224:227], v[44:47], v[188:191]\n"
"v_xor_b32 v237, 32, v236\n"
"v_xor_b32 v238, 64, v236\n"
"v_xor_b32 v239, 96, v236\n"
"v_mfma_f32_16x16x32_bf16 v[192:195], a[224:227], v[48:51], v[192:195]\n"
"s_waitcnt vmcnt(17) lgkmcnt(0)\n"
"s_barrier\n"
"ds_read_b64_tr_b16 a[216:217], v236 offset:0\n"
"ds_read_b64_tr_b16 a[218:219], v236 offset:2048\n"
"v_mfma_f32_16x16x32_bf16 v[196:199], a[228:231], v[16:19], 0\n"
"ds_read_b64_tr_b16 a[220:221], v236 offset:4096\n"
"v_mfma_f32_16x16x32_bf16 v[200:203], a[228:231], v[20:23], 0\n"
"ds_read_b64_tr_b16 a[222:223], v236 offset:6144\n"
"v_mfma_f32_16x16x32_bf16 v[204:207], a[228:231], v[24:27], 0\n"
"v_mfma_f32_16x16x32_bf16 v[196:199], a[232:235], v[28:31], v[196:199]\n"
"ds_read_b64_tr_b16 a[224:225], v236 offset:8192\n"
"v_mfma_f32_16x16x32_bf16 v[200:203], a[232:235], v[32:35], v[200:203]\n"
"ds_read_b64_tr_b16 a[226:227], v236 offset:10240\n"
"v_mfma_f32_16x16x32_bf16 v[204:207], a[232:235], v[36:39], v[204:207]\n"
"buffer_load_dwordx4 %[dX], %[rWo], s68 offen offset:2048 lds\n"
"v_mfma_f32_16x16x32_bf16 v[196:199], a[236:239], v[40:43], v[196:199]\n"
"ds_read_b64_tr_b16 a[228:229], v237 offset:0\n"
"v_mfma_f32_16x16x32_bf16 v[200:203], a[236:239], v[44:47], v[200:203]\n"
"ds_read_b64_tr_b16 a[230:231], v237 offset:2048\n"
"v_mfma_f32_16x16x32_bf16 v[204:207], a[236:239], v[48:51], v[204:207]\n"
"s_waitcnt lgkmcnt(6)\n"
"v_mfma_f32_16x16x32_bf16 v[160:163], a[216:219], v[52:55], v[160:163]\n"
"ds_read_b64_tr_b16 a[232:233], v237 offset:4096\n"
"ds_read_b128 a[0:3], %[exR] offset:0\n"
"v_mfma_f32_16x16x32_bf16 v[164:167], a[216:219], v[56:59], v[164:167]\n"
"ds_read_b64_tr_b16 a[234:235], v237 offset:6144\n"
"ds_read_b128 a[4:7], %[exR] offset:1024\n"
"v_mfma_f32_16x16x32_bf16 v[168:171], a[216:219], v[60:63], v[168:171]\n"
"s_mov_b32 m0, s61\n"
"s_add_u32 s68, %[wo0], s72\n"
"s_add_u32 s68, s68, 1105920\n"
"buffer_load_dwordx4 %[dX], %[rWo], s68 offen offset:0 lds\n"
"s_add_u32 s68, s68, 9216\n"
"s_waitcnt lgkmcnt(8)\n"
"v_mfma_f32_16x16x32_bf16 v[160:163], a[220:223], v[64:67], v[160:163]\n"
"ds_read_b64_tr_b16 a[236:237], v237 offset:8192\n"
"v_mfma_f32_16x16x32_bf16 v[164:167], a[220:223], v[68:71], v[164:167]\n"
"ds_read_b64_tr_b16 a[238:239], v237 offset:10240\n"
"v_mfma_f32_16x16x32_bf16 v[168:171], a[220:223], v[72:75], v[168:171]\n"
"s_waitcnt lgkmcnt(8)\n"
"v_mfma_f32_16x16x32_bf16 v[160:163], a[224:227], v[76:79], v[160:163]\n"
"ds_read_b64_tr_b16 a[216:217], v238 offset:0\n"
"v_mfma_f32_16x16x32_bf16 v[164:167], a[224:227], v[80:83], v[164:167]\n"
"ds_read_b64_tr_b16 a[218:219], v238 offset:2048\n"
"ds_read_b128 a[8:11], %[exR] offset:2048\n"
"v_mfma_f32_16x16x32_bf16 v[168:171], a[224:227], v[84:87], v[168:171]\n"
"buffer_load_dwordx4 %[dX], %[rWo], s68 offen offset:1024 lds\n"
"s_add_u32 s68, s68, 9216\n"
"s_waitcnt lgkmcnt(9)\n"
"v_mfma_f32_16x16x32_bf16 v[172:175], a[228:231], v[52:55], v[172:175]\n"
"ds_read_b64_tr_b16 a[220:221], v238 offset:4096\n"
"v_mfma_f32_16x16x32_bf16 v[176:179], a[228:231], v[56:59], v[176:179]\n"
"ds_read_b64_tr_b16 a[222:223], v238 offset:6144\n"
"v_mfma_f32_16x16x32_bf16 v[180:183], a[228:231], v[60:63], v[180:183]\n"
"ds_read_b128 a[12:15], %[exR] offset:3072\n"
"ds_read_b128 a[16:19], %[exR] offset:4096\n"
"s_waitcnt lgkmcnt(10)\n"
"v_mfma_f32_16x16x32_bf16 v[172:175], a[232:235], v[64:67], v[172:175]\n"
"ds_read_b64_tr_b16 a[224:225], v238 offset:8192\n"
"ds_read_b128 a[20:23], %[exR] offset:5120\n"
"v_mfma_f32_16x16x32_bf16 v[176:179], a[232:235], v[68:71], v[176:179]\n"
"ds_read_b64_tr_b16 a[226:227], v238 offset:10240\n"
"ds_read_b128 a[24:27], %[exR] offset:6144\n"
"v_mfma_f32_16x16x32_bf16 v[180:183], a[232:235], v[72:75], v[180:183]\n"
"buffer_load_dwordx4 %[dX], %[rWo], s68 offen offset:2048 lds\n"
"s_add_u32 m0, s61, 3072\n"
"s_add_u32 s68, s68, 12288\n"
"s_waitcnt lgkmcnt(11)\n"
"v_mfma_f32_16x16x32_bf16 v[172:175], a[236:239], v[76:79], v[172:175]\n"
"ds_read_b64_tr_b16 a[228:229], v239 offset:0\n"
"v_mfma_f32_16x16x32_bf16 v[176:179], a[236:239], v[80:83], v[176:179]\n"
"ds_read_b64_tr_b16 a[230:231], v239 offset:2048\n"
"v_mfma_f32_16x16x32_bf16 v[180:183], a[236:239], v[84:87], v[180:183]\n"
"ds_read_b128 a[28:31], %[exR] offset:7168\n"
"s_waitcnt lgkmcnt(12)\n"
"v_mfma_f32_16x16x32_bf16 v[184:187], a[216:219], v[52:55], v[184:187]\n"
"ds_read_b64_tr_b16 a[232:233], v239 offset:4096\n"
"ds_read_b128 a[32:35], %[exR] offset:8192\n"
"v_mfma_f32_16x16x32_bf16 v[188:191], a[216:219], v[56:59], v[188:191]\n"
"ds_read_b64_tr_b16 a[234:235], v239 offset:6144\n"
"v_mfma_f32_16x16x32_bf16 v[192:195], a[216:219], v[60:63], v[192:195]\n"
"buffer_load_dwordx4 %[dX], %[rWo], s68 offen offset:0 lds\n"
"s_add_u32 s68, s68, 9216\n"
"s_waitcnt lgkmcnt(12)\n"
"v_mfma_f32_16x16x32_bf16 v[184:187], a[220:223], v[64:67], v[184:187]\n"
"ds_read_b64_tr_b16 a[236:237], v239 offset:8192\n"
"v_mfma_f32_16x16x32_bf16 v[188:191], a[220:223], v[68:71], v[188:191]\n"
"ds_read_b64_tr_b16 a[238:239], v239 offset:10240\n"
"v_mfma_f32_16x16x32_bf16 v[192:195], a[220:223], v[72:75], v[192:195]\n"
"buffer_load_dwordx4 %[dX], %[rWo], s68 offen offset:1024 lds\n"
"s_add_u32 s68, s68, 9216\n"
"s_waitcnt lgkmcnt(9)\n"
"v_mfma_f32_16x16x32_bf16 v[184:187], a[224:227], v[76:79], v[184:187]\n"
"s_add_u32 s61, %[m0b], s60\n"
"s_add_u32 s60, s60, 12288\n"
"s_cmp_eq_u32 s60, 61440\n"
"s_cselect_b32 s60, 0, s60\n"
"v_add_u32 v236, s60, %[rdO]\n"
"v_mfma_f32_16x16x32_bf16 v[188:191], a[224:227], v[80:83], v[188:191]\n"
"v_xor_b32 v237, 32, v236\n"
"v_xor_b32 v238, 64, v236\n"
"v_xor_b32 v239, 96, v236\n"
"v_mfma_f32_16x16x32_bf16 v[192:195], a[224:227], v[84:87], v[192:195]\n"
"s_waitcnt vmcnt(17) lgkmcnt(0)\n"
"s_barrier\n"
"ds_read_b64_tr_b16 a[216:217], v236 offset:0\n"
"ds_read_b64_tr_b16 a[218:219], v236 offset:2048\n"
"v_mfma_f32_16x16x32_bf16 v[196:199], a[228:231], v[52:55], v[196:199]\n"
"ds_read_b64_tr_b16 a[220:221], v236 offset:4096\n"
"v_mfma_f32_16x16x32_bf16 v[200:203], a[228:231], v[56:59], v[200:203]\n"
"ds_read_b64_tr_b16 a[222:223], v236 offset:6144\n"
"v_mfma_f32_16x16x32_bf16 v[204:207], a[228:231], v[60:63], v[204:207]\n"
"v_mfma_f32_16x16x32_bf16 v[196:199], a[232:235], v[64:67], v[196:199]\n"
"ds_read_b64_tr_b16 a[224:225], v236 offset:8192\n"
"v_mfma_f32_16x16x32_bf16 v[200:203], a[232:235], v[68:71], v[200:203]\n"
"ds_read_b64_tr_b16 a[226:227], v236 offset:10240\n"
"v_mfma_f32_16x16x32_bf16 v[204:207], a[232:235], v[72:75], v[204:207]\n"
"buffer_load_dwordx4 %[dX], %[rWo], s68 offen offset:2048 lds\n"
"v_mfma_f32_16x16x32_bf16 v[196:199], a[236:239], v[76:79], v[196:199]\n"
"ds_read_b64_tr_b16 a[228:229], v237 offset:0\n"
"v_mfma_f32_16x16x32_bf16 v[200:203], a[236:239], v[80:83], v[200:203]\n"
"ds_read_b64_tr_b16 a[230:231], v237 offset:2048\n"
"v_mfma_f32_16x16x32_bf16 v[204:207], a[236:239], v[84:87], v[204:207]\n"
"s_waitcnt lgkmcnt(6)\n"
"v_mfma_f32_16x16x32_bf16 v[160:163], a[216:219], v[88:91], v[160:163]\n"
"ds_read_b64_tr_b16 a[232:233], v237 offset:4096\n"
"ds_write_b128 %[exW], a[180:183] offset:0\n"
"v_mfma_f32_16x16x32_bf16 v[164:167], a[216:219], v[92:95], v[164:167]\n"
"ds_read_b64_tr_b16 a[234:235], v237 offset:6144\n"
"ds_write_b128 %[exW], a[184:187] offset:1024\n"
"v_mfma_f32_16x16x32_bf16 v[168:171], a[216:219], v[96:99], v[168:171]\n"
"s_mov_b32 m0, s61\n"
"s_add_u32 s68, %[wo1], s72\n"
"s_add_u32 s68, s68, 983040\n"
"buffer_load_dwordx4 %[dX], %[rWo], s68 offen offset:0 lds\n"
"s_add_u32 s68, s68, 9216\n"
"s_waitcnt lgkmcnt(8)\n"
"v_mfma_f32_16x16x32_bf16 v[160:163], a[220:223], v[100:103], v[160:163]\n"
"ds_read_b64_tr_b16 a[236:237], v237 offset:8192\n"
"v_mfma_f32_16x16x32_bf16 v[164:167], a[220:223], v[104:107], v[164:167]\n"
"ds_read_b64_tr_b16 a[238:239], v237 offset:10240\n"
"v_mfma_f32_16x16x32_bf16 v[168:171], a[220:223], v[108:111], v[168:171]\n"
"s_waitcnt lgkmcnt(8)\n"
"v_mfma_f32_16x16x32_bf16 v[160:163], a[224:227], v[112:115], v[160:163]\n"
"ds_read_b64_tr_b16 a[216:217], v238 offset:0\n"
"v_mfma_f32_16x16x32_bf16 v[164:167], a[224:227], v[116:119], v[164:167]\n"
"ds_read_b64_tr_b16 a[218:219], v238 offset:2048\n"
"ds_write_b128 %[exW], a[188:191] offset:2048\n"
"v_mfma_f32_16x16x32_bf16 v[168:171], a[224:227], v[120:123], v[168:171]\n"
"buffer_load_dwordx4 %[dX], %[rWo], s68 offen offset:1024 lds\n"
"s_add_u32 s68, s68, 9216\n"
"s_waitcnt lgkmcnt(9)\n"
"v_mfma_f32_16x16x32_bf16 v[172:175], a[228:231], v[88:91], v[172:175]\n"
"ds_read_b64_tr_b16 a[220:221], v238 offset:4096\n"
"v_mfma_f32_16x16x32_bf16 v[176:179], a[228:231], v[92:95], v[176:179]\n"
"ds_read_b64_tr_b16 a[222:223], v238 offset:6144\n"
"v_mfma_f32_16x16x32_bf16 v[180:183], a[228:231], v[96:99], v[180:183]\n"
"ds_write_b128 %[exW], a[192:195] offset:3072\n"
"ds_write_b128 %[exW], a[196:199] offset:4096\n"
"s_waitcnt lgkmcnt(10)\n"
"v_mfma_f32_16x16x32_bf16 v[172:175], a[232:235], v[100:103], v[172:175]\n"
"ds_read_b64_tr_b16 a[224:225], v238 offset:8192\n"
"ds_write_b128 %[exW], a[200:203] offset:5120\n"
"v_mfma_f32_16x16x32_bf16 v[176:179], a[232:235], v[104:107], v[176:179]\n"
"ds_read_b64_tr_b16 a[226:227], v238 offset:10240\n"
"ds_write_b128 %[exW], a[204:207] offset:6144\n"
"v_mfma_f32_16x16x32_bf16 v[180:183], a[232:235], v[108:111], v[180:183]\n"
"buffer_load_dwordx4 %[dX], %[rWo], s68 offen offset:2048 lds\n"
"s_add_u32 m0, s61, 3072\n"
"s_add_u32 s68, s68, 12288\n"
"s_waitcnt lgkmcnt(11)\n"
"v_mfma_f32_16x16x32_bf16 v[172:175], a[236:239], v[112:115], v[172:175]\n"
"ds_read_b64_tr_b16 a[228:229], v239 offset:0\n"
"v_mfma_f32_16x16x32_bf16 v[176:179], a[236:239], v[116:119], v[176:179]\n"
"ds_read_b64_tr_b16 a[230:231], v239 offset:2048\n"
"v_mfma_f32_16x16x32_bf16 v[180:183], a[236:239], v[120:123], v[180:183]\n"
"ds_write_b128 %[exW], a[208:211] offset:7168\n"
"s_waitcnt lgkmcnt(12)\n"
"v_mfma_f32_16x16x32_bf16 v[184:187], a[216:219], v[88:91], v[184:187]\n"
"ds_read_b64_tr_b16 a[232:233], v239 offset:4096\n"
"ds_write_b128 %[exW], a[212:215] offset:8192\n"
"v_mfma_f32_16x16x32_bf16 v[188:191], a[216:219], v[92:95], v[188:191]\n"
"ds_read_b64_tr_b16 a[234:235], v239 offset:6144\n"
"v_mfma_f32_16x16x32_bf16 v[192:195], a[216:219], v[96:99], v[192:195]\n"
"buffer_load_dwordx4 %[dX], %[rWo], s68 offen offset:0 lds\n"
"s_add_u32 s68, s68, 9216\n"
"s_waitcnt lgkmcnt(12)\n"
"v_mfma_f32_16x16x32_bf16 v[184:187], a[220:223], v[100:103], v[184:187]\n"
"ds_read_b64_tr_b16 a[236:237], v239 offset:8192\n"
"v_mfma_f32_16x16x32_bf16 v[188:191], a[220:223], v[104:107], v[188:191]\n"
"ds_read_b64_tr_b16 a[238:239], v239 offset:10240\n"
"v_mfma_f32_16x16x32_bf16 v[192:195], a[220:223], v[108:111], v[192:195]\n"
"buffer_load_dwordx4 %[dX], %[rWo], s68 offen offset:1024 lds\n"
"s_add_u32 s68, s68, 9216\n"
"s_waitcnt lgkmcnt(9)\n"
"v_mfma_f32_16x16x32_bf16 v[184:187], a[224:227], v[112:115], v[184:187]\n"
"s_add_u32 s61, %[m0b], s60\n"
"s_add_u32 s60, s60, 12288\n"
"s_cmp_eq_u32 s60, 61440\n"
"s_cselect_b32 s60, 0, s60\n"
"v_add_u32 v236, s60, %[rdO]\n"
"v_mfma_f32_16x16x32_bf16 v[188:191], a[224:227], v[116:119], v[188:191]\n"
"v_xor_b32 v237, 32, v236\n"
"v_xor_b32 v238, 64, v236\n"
"v_xor_b32 v239, 96, v236\n"
"v_mfma_f32_16x16x32_bf16 v[192:195], a[224:227], v[120:123], v[192:195]\n"
"s_waitcnt vmcnt(17) lgkmcnt(0)\n"
"s_barrier\n"
"ds_read_b64_tr_b16 a[216:217], v236 offset:0\n"
"ds_read_b64_tr_b16 a[218:219], v236 offset:2048\n"
"v_mfma_f32_16x16x32_bf16 v[196:199], a[228:231], v[88:91], v[196:199]\n"
"ds_read_b64_tr_b16 a[220:221], v236 offset:4096\n"
"v_mfma_f32_16x16x32_bf16 v[200:203], a[228:231], v[92:95], v[200:203]\n"
"ds_read_b64_tr_b16 a[222:223], v236 offset:6144\n"
"v_mfma_f32_16x16x32_bf16 v[204:207], a[228:231], v[96:99], v[204:207]\n"
"v_mfma_f32_16x16x32_bf16 v[196:199], a[232:235], v[100:103], v[196:199]\n"
"ds_read_b64_tr_b16 a[224:225], v236 offset:8192\n"
"v_mfma_f32_16x16x32_bf16 v[200:203], a[232:235], v[104:107], v[200:203]\n"
"ds_read_b64_tr_b16 a[226:227], v236 offset:10240\n"
"v_mfma_f32_16x16x32_bf16 v[204:207], a[232:235], v[108:111], v[204:207]\n"
"buffer_load_dwordx4 %[dX], %[rWo], s68 offen offset:2048 lds\n"
"v_mfma_f32_16x16x32_bf16 v[196:199], a[236:239], v[112:115], v[196:199]\n"
"ds_read_b64_tr_b16 a[228:229], v237 offset:0\n"
"v_mfma_f32_16x16x32_bf16 v[200:203], a[236:239], v[116:119], v[200:203]\n"
"ds_read_b64_tr_b16 a[230:231], v237 offset:2048\n"
"v_mfma_f32_16x16x32_bf16 v[204:207], a[236:239], v[120:123], v[204:207]\n"
"s_waitcnt lgkmcnt(6)\n"
"v_mfma_f32_16x16x32_bf16 v[160:163], a[216:219], v[124:127], v[160:163]\n"
"ds_read_b64_tr_b16 a[232:233], v237 offset:4096\n"
"ds_read_b128 a[36:39], %[exR] offset:0\n"
"v_mfma_f32_16x16x32_bf16 v[164:167], a[216:219], v[128:131], v[164:167]\n"
"ds_read_b64_tr_b16 a[234:235], v237 offset:6144\n"
"ds_read_b128 a[40:43], %[exR] offset:1024\n"
"v_mfma_f32_16x16x32_bf16 v[168:171], a[216:219], v[132:135], v[168:171]\n"
"s_mov_b32 m0, s61\n"
"s_add_u32 s68, %[wo1], s72\n"
"s_add_u32 s68, s68, 1105920\n"
"buffer_load_dwordx4 %[dX], %[rWo], s68 offen offset:0 lds\n"
"s_add_u32 s68, s68, 9216\n"
"s_waitcnt lgkmcnt(8)\n"
"v_mfma_f32_16x16x32_bf16 v[160:163], a[220:223], v[136:139], v[160:163]\n"
"ds_read_b64_tr_b16 a[236:237], v237 offset:8192\n"
"v_mfma_f32_16x16x32_bf16 v[164:167], a[220:223], v[140:143], v[164:167]\n"
"ds_read_b64_tr_b16 a[238:239], v237 offset:10240\n"
"v_mfma_f32_16x16x32_bf16 v[168:171], a[220:223], v[144:147], v[168:171]\n"
"s_waitcnt lgkmcnt(8)\n"
"v_mfma_f32_16x16x32_bf16 v[160:163], a[224:227], v[148:151], v[160:163]\n"
"ds_read_b64_tr_b16 a[216:217], v238 offset:0\n"
"v_mfma_f32_16x16x32_bf16 v[164:167], a[224:227], v[152:155], v[164:167]\n"
"ds_read_b64_tr_b16 a[218:219], v238 offset:2048\n"
"ds_read_b128 a[44:47], %[exR] offset:2048\n"
"v_mfma_f32_16x16x32_bf16 v[168:171], a[224:227], v[156:159], v[168:171]\n"
"buffer_load_dwordx4 %[dX], %[rWo], s68 offen offset:1024 lds\n"
"s_add_u32 s68, s68, 9216\n"
"s_waitcnt lgkmcnt(9)\n"
"v_mfma_f32_16x16x32_bf16 v[172:175], a[228:231], v[124:127], v[172:175]\n"
"ds_read_b64_tr_b16 a[220:221], v238 offset:4096\n"
"v_mfma_f32_16x16x32_bf16 v[176:179], a[228:231], v[128:131], v[176:179]\n"
"ds_read_b64_tr_b16 a[222:223], v238 offset:6144\n"
"v_mfma_f32_16x16x32_bf16 v[180:183], a[228:231], v[132:135], v[180:183]\n"
"ds_read_b128 a[48:51], %[exR] offset:3072\n"
"ds_read_b128 a[52:55], %[exR] offset:4096\n"
"s_waitcnt lgkmcnt(10)\n"
"v_mfma_f32_16x16x32_bf16 v[172:175], a[232:235], v[136:139], v[172:175]\n"
"ds_read_b64_tr_b16 a[224:225], v238 offset:8192\n"
"ds_read_b128 a[56:59], %[exR] offset:5120\n"
"v_mfma_f32_16x16x32_bf16 v[176:179], a[232:235], v[140:143], v[176:179]\n"
"ds_read_b64_tr_b16 a[226:227], v238 offset:10240\n"
"ds_read_b128 a[60:63], %[exR] offset:6144\n"
"v_mfma_f32_16x16x32_bf16 v[180:183], a[232:235], v[144:147], v[180:183]\n"
"buffer_load_dwordx4 %[dX], %[rWo], s68 offen offset:2048 lds\n"
"s_add_u32 m0, s61, 3072\n"
"s_add_u32 s68, s68, 12288\n"
"s_waitcnt lgkmcnt(11)\n"
"v_mfma_f32_16x16x32_bf16 v[172:175], a[236:239], v[148:151], v[172:175]\n"
"ds_read_b64_tr_b16 a[228:229], v239 offset:0\n"
"v_mfma_f32_16x16x32_bf16 v[176:179], a[236:239], v[152:155], v[176:179]\n"
"ds_read_b64_tr_b16 a[230:231], v239 offset:2048\n"
"v_mfma_f32_16x16x32_bf16 v[180:183], a[236:239], v[156:159], v[180:183]\n"
"ds_read_b128 a[64:67], %[exR] offset:7168\n"
"s_waitcnt lgkmcnt(12)\n"
"v_mfma_f32_16x16x32_bf16 v[184:187], a[216:219], v[124:127], v[184:187]\n"
"ds_read_b64_tr_b16 a[232:233], v239 offset:4096\n"
"ds_read_b128 a[68:71], %[exR] offset:8192\n"
"v_mfma_f32_16x16x32_bf16 v[188:191], a[216:219], v[128:131], v[188:191]\n"
"ds_read_b64_tr_b16 a[234:235], v239 offset:6144\n"
"v_mfma_f32_16x16x32_bf16 v[192:195], a[216:219], v[132:135], v[192:195]\n"
"buffer_load_dwordx4 %[dX], %[rWo], s68 offen offset:0 lds\n"
"s_add_u32 s68, s68, 9216\n"
"s_waitcnt lgkmcnt(12)\n"
"v_mfma_f32_16x16x32_bf16 v[184:187], a[220:223], v[136:139], v[184:187]\n"
"ds_read_b64_tr_b16 a[236:237], v239 offset:8192\n"
"v_mfma_f32_16x16x32_bf16 v[188:191], a[220:223], v[140:143], v[188:191]\n"
"ds_read_b64_tr_b16 a[238:239], v239 offset:10240\n"
"v_mfma_f32_16x16x32_bf16 v[192:195], a[220:223], v[144:147], v[192:195]\n"
"buffer_load_dwordx4 %[dX], %[rWo], s68 offen offset:1024 lds\n"
"s_add_u32 s68, s68, 9216\n"
"s_waitcnt lgkmcnt(9)\n"
"v_mfma_f32_16x16x32_bf16 v[184:187], a[224:227], v[148:151], v[184:187]\n"
"s_add_u32 s61, %[m0b], s60\n"
"s_add_u32 s60, s60, 12288\n"
"s_cmp_eq_u32 s60, 61440\n"
"s_cselect_b32 s60, 0, s60\n"
"v_add_u32 v236, s60, %[rdO]\n"
"v_mfma_f32_16x16x32_bf16 v[188:191], a[224:227], v[152:155], v[188:191]\n"
"v_xor_b32 v237, 32, v236\n"
"v_xor_b32 v238, 64, v236\n"
"v_xor_b32 v239, 96, v236\n"
"v_mfma_f32_16x16x32_bf16 v[192:195], a[224:227], v[156:159], v[192:195]\n"
"s_waitcnt vmcnt(17) lgkmcnt(0)\n"
"s_barrier\n"
"ds_read_b64_tr_b16 a[216:217], v236 offset:0\n"
"ds_read_b64_tr_b16 a[218:219], v236 offset:2048\n"
"v_mfma_f32_16x16x32_bf16 v[196:199], a[228:231], v[124:127], v[196:199]\n"
"ds_read_b64_tr_b16 a[220:221], v236 offset:4096\n"
"v_mfma_f32_16x16x32_bf16 v[200:203], a[228:231], v[128:131], v[200:203]\n"
"ds_read_b64_tr_b16 a[222:223], v236 offset:6144\n"
"v_mfma_f32_16x16x32_bf16 v[204:207], a[228:231], v[132:135], v[204:207]\n"
"v_mfma_f32_16x16x32_bf16 v[196:199], a[232:235], v[136:139], v[196:199]\n"
"ds_read_b64_tr_b16 a[224:225], v236 offset:8192\n"
"v_mfma_f32_16x16x32_bf16 v[200:203], a[232:235], v[140:143], v[200:203]\n"
"ds_read_b64_tr_b16 a[226:227], v236 offset:10240\n"
"v_mfma_f32_16x16x32_bf16 v[204:207], a[232:235], v[144:147], v[204:207]\n"
"buffer_load_dwordx4 %[dX], %[rWo], s68 offen offset:2048 lds\n"
"s_add_u32 s72, s72, 128\n"
"s_cmp_eq_u32 s72, 640\n"
"s_cselect_b32 s72, 0, s72\n"
"v_mfma_f32_16x16x32_bf16 v[196:199], a[236:239], v[148:151], v[196:199]\n"
"ds_read_b64_tr_b16 a[228:229], v237 offset:0\n"
"v_mfma_f32_16x16x32_bf16 v[200:203], a[236:239], v[152:155], v[200:203]\n"
"ds_read_b64_tr_b16 a[230:231], v237 offset:2048\n"
"v_mfma_f32_16x16x32_bf16 v[204:207], a[236:239], v[156:159], v[204:207]\n"
"s_waitcnt lgkmcnt(6)\n"
"v_mfma_f32_16x16x32_bf16 v[160:163], a[216:219], a[144:147], v[160:163]\n"
"ds_read_b64_tr_b16 a[232:233], v237 offset:4096\n"
"v_mfma_f32_16x16x32_bf16 v[164:167], a[216:219], a[148:151], v[164:167]\n"
"ds_read_b64_tr_b16 a[234:235], v237 offset:6144\n"
"v_mfma_f32_16x16x32_bf16 v[168:171], a[216:219], a[152:155], v[168:171]\n"
"s_mov_b32 m0, s61\n"
"s_add_u32 s68, %[wo0], s72\n"
"buffer_load_dwordx4 %[dX], %[rWo], s68 offen offset:0 lds\n"
"s_add_u32 s68, s68, 9216\n"
"s_waitcnt lgkmcnt(6)\n"
"v_mfma_f32_16x16x32_bf16 v[160:163], a[220:223], a[156:159], v[160:163]\n"
"ds_read_b64_tr_b16 a[236:237], v237 offset:8192\n"
"v_mfma_f32_16x16x32_bf16 v[164:167], a[220:223], a[160:163], v[164:167]\n"
"ds_read_b64_tr_b16 a[238:239], v237 offset:10240\n"
"v_mfma_f32_16x16x32_bf16 v[168:171], a[220:223], a[164:167], v[168:171]\n"
"s_waitcnt lgkmcnt(6)\n"
"v_mfma_f32_16x16x32_bf16 v[160:163], a[224:227], a[168:171], v[160:163]\n"
"ds_read_b64_tr_b16 a[216:217], v238 offset:0\n"
"v_mfma_f32_16x16x32_bf16 v[164:167], a[224:227], a[172:175], v[164:167]\n"
"ds_read_b64_tr_b16 a[218:219], v238 offset:2048\n"
"v_mfma_f32_16x16x32_bf16 v[168:171], a[224:227], a[176:179], v[168:171]\n"
"buffer_load_dwordx4 %[dX], %[rWo], s68 offen offset:1024 lds\n"
"s_add_u32 s68, s68, 9216\n"
"s_waitcnt lgkmcnt(6)\n"
"v_mfma_f32_16x16x32_bf16 v[172:175], a[228:231], a[144:147], v[172:175]\n"
"ds_read_b64_tr_b16 a[220:221], v238 offset:4096\n"
"v_mfma_f32_16x16x32_bf16 v[176:179], a[228:231], a[148:151], v[176:179]\n"
"ds_read_b64_tr_b16 a[222:223], v238 offset:6144\n"
"v_mfma_f32_16x16x32_bf16 v[180:183], a[228:231], a[152:155], v[180:183]\n"
"s_waitcnt lgkmcnt(6)\n"
"v_mfma_f32_16x16x32_bf16 v[172:175], a[232:235], a[156:159], v[172:175]\n"
"ds_read_b64_tr_b16 a[224:225], v238 offset:8192\n"
"v_mfma_f32_16x16x32_bf16 v[176:179], a[232:235], a[160:163], v[176:179]\n"
"ds_read_b64_tr_b16 a[226:227], v238 offset:10240\n"
"v_mfma_f32_16x16x32_bf16 v[180:183], a[232:235], a[164:167], v[180:183]\n"
"buffer_load_dwordx4 %[dX], %[rWo], s68 offen offset:2048 lds\n"
"s_add_u32 m0, s61, 3072\n"
"s_add_u32 s68, s68, 12288\n"
"s_waitcnt lgkmcnt(6)\n"
"v_mfma_f32_16x16x32_bf16 v[172:175], a[236:239], a[168:171], v[172:175]\n"
"ds_read_b64_tr_b16 a[228:229], v239 offset:0\n"
"v_mfma_f32_16x16x32_bf16 v[176:179], a[236:239], a[172:175], v[176:179]\n"
"ds_read_b64_tr_b16 a[230:231], v239 offset:2048\n"
"v_mfma_f32_16x16x32_bf16 v[180:183], a[236:239], a[176:179], v[180:183]\n"
"s_waitcnt lgkmcnt(6)\n"
"v_mfma_f32_16x16x32_bf16 v[184:187], a[216:219], a[144:147], v[184:187]\n"
"ds_read_b64_tr_b16 a[232:233], v239 offset:4096\n"
"v_mfma_f32_16x16x32_bf16 v[188:191], a[216:219], a[148:151], v[188:191]\n"
"ds_read_b64_tr_b16 a[234:235], v239 offset:6144\n"
"v_mfma_f32_16x16x32_bf16 v[192:195], a[216:219], a[152:155], v[192:195]\n"
"buffer_load_dwordx4 %[dX], %[rWo], s68 offen offset:0 lds\n"
"s_add_u32 s68, s68, 9216\n"
"s_waitcnt lgkmcnt(6)\n"
"v_mfma_f32_16x16x32_bf16 v[184:187], a[220:223], a[156:159], v[184:187]\n"
"ds_read_b64_tr_b16 a[236:237], v239 offset:8192\n"
"v_mfma_f32_16x16x32_bf16 v[188:191], a[220:223], a[160:163], v[188:191]\n"
"ds_read_b64_tr_b16 a[238:239], v239 offset:10240\n"
"v_mfma_f32_16x16x32_bf16 v[192:195], a[220:223], a[164:167], v[192:195]\n"
"buffer_load_dwordx4 %[dX], %[rWo], s68 offen offset:1024 lds\n"
"s_add_u32 s68, s68, 9216\n"
"s_waitcnt lgkmcnt(6)\n"
"v_mfma_f32_16x16x32_bf16 v[184:187], a[224:227], a[168:171], v[184:187]\n"
"s_add_u32 s61, %[m0b], s60\n"
"s_add_u32 s60, s60, 12288\n"
"s_cmp_eq_u32 s60, 61440\n"
"s_cselect_b32 s60, 0, s60\n"
"v_add_u32 v236, s60, %[rdO]\n"
"v_mfma_f32_16x16x32_bf16 v[188:191], a[224:227], a[172:175], v[188:191]\n"
"v_xor_b32 v237, 32, v236\n"
"v_xor_b32 v238, 64, v236\n"
"v_xor_b32 v239, 96, v236\n"
"v_mfma_f32_16x16x32_bf16 v[192:195], a[224:227], a[176:179], v[192:195]\n"
"s_waitcnt vmcnt(17) lgkmcnt(0)\n"
"s_barrier\n"
"ds_read_b64_tr_b16 a[216:217], v236 offset:0\n"
"ds_read_b64_tr_b16 a[218:219], v236 offset:2048\n"
"v_mfma_f32_16x16x32_bf16 v[196:199], a[228:231], a[144:147], v[196:199]\n"
"ds_read_b64_tr_b16 a[220:221], v236 offset:4096\n"
"v_mfma_f32_16x16x32_bf16 v[200:203], a[228:231], a[148:151], v[200:203]\n"
"ds_read_b64_tr_b16 a[222:223], v236 offset:6144\n"
"v_mfma_f32_16x16x32_bf16 v[204:207], a[228:231], a[152:155], v[204:207]\n"
"v_mfma_f32_16x16x32_bf16 v[196:199], a[232:235], a[156:159], v[196:199]\n"
"ds_read_b64_tr_b16 a[224:225], v236 offset:8192\n"
"v_mfma_f32_16x16x32_bf16 v[200:203], a[232:235], a[160:163], v[200:203]\n"
"ds_read_b64_tr_b16 a[226:227], v236 offset:10240\n"
"v_mfma_f32_16x16x32_bf16 v[204:207], a[232:235], a[164:167], v[204:207]\n"
"buffer_load_dwordx4 %[dX], %[rWo], s68 offen offset:2048 lds\n"
"v_mfma_f32_16x16x32_bf16 v[196:199], a[236:239], a[168:171], v[196:199]\n"
"ds_read_b64_tr_b16 a[228:229], v237 offset:0\n"
"v_mfma_f32_16x16x32_bf16 v[200:203], a[236:239], a[172:175], v[200:203]\n"
"ds_read_b64_tr_b16 a[230:231], v237 offset:2048\n"
"v_mfma_f32_16x16x32_bf16 v[204:207], a[236:239], a[176:179], v[204:207]\n"
"s_waitcnt lgkmcnt(6)\n"
"v_mfma_f32_16x16x32_bf16 v[160:163], a[216:219], a[180:183], v[160:163]\n"
"ds_read_b64_tr_b16 a[232:233], v237 offset:4096\n"
"v_mfma_f32_16x16x32_bf16 v[164:167], a[216:219], a[184:187], v[164:167]\n"
"ds_read_b64_tr_b16 a[234:235], v237 offset:6144\n"
"v_mfma_f32_16x16x32_bf16 v[168:171], a[216:219], a[188:191], v[168:171]\n"
"s_mov_b32 m0, s61\n"
"s_add_u32 s68, %[wo0], s72\n"
"s_add_u32 s68, s68, 122880\n"
"buffer_load_dwordx4 %[dX], %[rWo], s68 offen offset:0 lds\n"
"s_add_u32 s68, s68, 9216\n"
"s_waitcnt lgkmcnt(6)\n"
"v_mfma_f32_16x16x32_bf16 v[160:163], a[220:223], a[192:195], v[160:163]\n"
"ds_read_b64_tr_b16 a[236:237], v237 offset:8192\n"
"v_mfma_f32_16x16x32_bf16 v[164:167], a[220:223], a[196:199], v[164:167]\n"
"ds_read_b64_tr_b16 a[238:239], v237 offset:10240\n"
"v_mfma_f32_16x16x32_bf16 v[168:171], a[220:223], a[200:203], v[168:171]\n"
"s_waitcnt lgkmcnt(6)\n"
"v_mfma_f32_16x16x32_bf16 v[160:163], a[224:227], a[204:207], v[160:163]\n"
"ds_read_b64_tr_b16 a[216:217], v238 offset:0\n"
"v_mfma_f32_16x16x32_bf16 v[164:167], a[224:227], a[208:211], v[164:167]\n"
"ds_read_b64_tr_b16 a[218:219], v238 offset:2048\n"
"v_mfma_f32_16x16x32_bf16 v[168:171], a[224:227], a[212:215], v[168:171]\n"
"buffer_load_dwordx4 %[dX], %[rWo], s68 offen offset:1024 lds\n"
"s_add_u32 s68, s68, 9216\n"
"s_waitcnt lgkmcnt(6)\n"
"v_mfma_f32_16x16x32_bf16 v[172:175], a[228:231], a[180:183], v[172:175]\n"
"ds_read_b64_tr_b16 a[220:221], v238 offset:4096\n"
"v_mfma_f32_16x16x32_bf16 v[176:179], a[228:231], a[184:187], v[176:179]\n"
"ds_read_b64_tr_b16 a[222:223], v238 offset:6144\n"
"v_mfma_f32_16x16x32_bf16 v[180:183], a[228:231], a[188:191], v[180:183]\n"
"s_waitcnt lgkmcnt(6)\n"
"v_mfma_f32_16x16x32_bf16 v[172:175], a[232:235], a[192:195], v[172:175]\n"
"ds_read_b64_tr_b16 a[224:225], v238 offset:8192\n"
"v_mfma_f32_16x16x32_bf16 v[176:179], a[232:235], a[196:199], v[176:179]\n"
"ds_read_b64_tr_b16 a[226:227], v238 offset:10240\n"
"v_mfma_f32_16x16x32_bf16 v[180:183], a[232:235], a[200:203], v[180:183]\n"
"buffer_load_dwordx4 %[dX], %[rWo], s68 offen offset:2048 lds\n"
"s_add_u32 m0, s61, 3072\n"
"s_add_u32 s68, s68, 12288\n"
"s_waitcnt lgkmcnt(6)\n"
"v_mfma_f32_16x16x32_bf16 v[172:175], a[236:239], a[204:207], v[172:175]\n"
"ds_read_b64_tr_b16 a[228:229], v239 offset:0\n"
"v_mfma_f32_16x16x32_bf16 v[176:179], a[236:239], a[208:211], v[176:179]\n"
"ds_read_b64_tr_b16 a[230:231], v239 offset:2048\n"
"v_mfma_f32_16x16x32_bf16 v[180:183], a[236:239], a[212:215], v[180:183]\n"
"s_waitcnt lgkmcnt(6)\n"
"v_mfma_f32_16x16x32_bf16 v[184:187], a[216:219], a[180:183], v[184:187]\n"
"ds_read_b64_tr_b16 a[232:233], v239 offset:4096\n"
"v_mfma_f32_16x16x32_bf16 v[188:191], a[216:219], a[184:187], v[188:191]\n"
"ds_read_b64_tr_b16 a[234:235], v239 offset:6144\n"
"v_mfma_f32_16x16x32_bf16 v[192:195], a[216:219], a[188:191], v[192:195]\n"
"buffer_load_dwordx4 %[dX], %[rWo], s68 offen offset:0 lds\n"
"s_add_u32 s68, s68, 9216\n"
"s_waitcnt lgkmcnt(6)\n"
"v_mfma_f32_16x16x32_bf16 v[184:187], a[220:223], a[192:195], v[184:187]\n"
"ds_read_b64_tr_b16 a[236:237], v239 offset:8192\n"
"v_mfma_f32_16x16x32_bf16 v[188:191], a[220:223], a[196:199], v[188:191]\n"
"ds_read_b64_tr_b16 a[238:239], v239 offset:10240\n"
"v_mfma_f32_16x16x32_bf16 v[192:195], a[220:223], a[200:203], v[192:195]\n"
"buffer_load_dwordx4 %[dX], %[rWo], s68 offen offset:1024 lds\n"
"s_add_u32 s68, s68, 9216\n"
"s_waitcnt lgkmcnt(6)\n"
"v_mfma_f32_16x16x32_bf16 v[184:187], a[224:227], a[204:207], v[184:187]\n"
"s_add_u32 s61, %[m0b], s60\n"
"s_add_u32 s60, s60, 12288\n"
"s_cmp_eq_u32 s60, 61440\n"
"s_cselect_b32 s60, 0, s60\n"
"v_add_u32 v236, s60, %[rdO]\n"
"v_mfma_f32_16x16x32_bf16 v[188:191], a[224:227], a[208:211], v[188:191]\n"
"v_xor_b32 v237, 32, v236\n"
"v_xor_b32 v238, 64, v236\n"
"v_xor_b32 v239, 96, v236\n"
"v_mfma_f32_16x16x32_bf16 v[192:195], a[224:227], a[212:215], v[192:195]\n"
"s_waitcnt vmcnt(17) lgkmcnt(0)\n"
"s_barrier\n"
"ds_read_b64_tr_b16 a[216:217], v236 offset:0\n"
"ds_read_b64_tr_b16 a[218:219], v236 offset:2048\n"
"v_mfma_f32_16x16x32_bf16 v[196:199], a[228:231], a[180:183], v[196:199]\n"
"ds_read_b64_tr_b16 a[220:221], v236 offset:4096\n"
"v_mfma_f32_16x16x32_bf16 v[200:203], a[228:231], a[184:187], v[200:203]\n"
"ds_read_b64_tr_b16 a[222:223], v236 offset:6144\n"
"v_mfma_f32_16x16x32_bf16 v[204:207], a[228:231], a[188:191], v[204:207]\n"
"v_mfma_f32_16x16x32_bf16 v[196:199], a[232:235], a[192:195], v[196:199]\n"
"ds_read_b64_tr_b16 a[224:225], v236 offset:8192\n"
"v_mfma_f32_16x16x32_bf16 v[200:203], a[232:235], a[196:199], v[200:203]\n"
"ds_read_b64_tr_b16 a[226:227], v236 offset:10240\n"
"v_mfma_f32_16x16x32_bf16 v[204:207], a[232:235], a[200:203], v[204:207]\n"
"buffer_load_dwordx4 %[dX], %[rWo], s68 offen offset:2048 lds\n"
"v_mfma_f32_16x16x32_bf16 v[196:199], a[236:239], a[204:207], v[196:199]\n"
"ds_read_b64_tr_b16 a[228:229], v237 offset:0\n"
"v_mfma_f32_16x16x32_bf16 v[200:203], a[236:239], a[208:211], v[200:203]\n"
"ds_read_b64_tr_b16 a[230:231], v237 offset:2048\n"
"v_mfma_f32_16x16x32_bf16 v[204:207], a[236:239], a[212:215], v[204:207]\n"
"s_waitcnt lgkmcnt(6)\n"
"v_mfma_f32_16x16x32_bf16 v[160:163], a[216:219], a[0:3], v[160:163]\n"
"ds_read_b64_tr_b16 a[232:233], v237 offset:4096\n"
"v_mfma_f32_16x16x32_bf16 v[164:167], a[216:219], a[4:7], v[164:167]\n"
"ds_read_b64_tr_b16 a[234:235], v237 offset:6144\n"
"v_mfma_f32_16x16x32_bf16 v[168:171], a[216:219], a[8:11], v[168:171]\n"
"s_mov_b32 m0, s61\n"
"s_add_u32 s68, %[wo1], s72\n"
"buffer_load_dwordx4 %[dX], %[rWo], s68 offen offset:0 lds\n"
"s_add_u32 s68, s68, 9216\n"
"s_waitcnt lgkmcnt(6)\n"
"v_mfma_f32_16x16x32_bf16 v[160:163], a[220:223], a[12:15], v[160:163]\n"
"ds_read_b64_tr_b16 a[236:237], v237 offset:8192\n"
"v_mfma_f32_16x16x32_bf16 v[164:167], a[220:223], a[16:19], v[164:167]\n"
"ds_read_b64_tr_b16 a[238:239], v237 offset:10240\n"
"v_mfma_f32_16x16x32_bf16 v[168:171], a[220:223], a[20:23], v[168:171]\n"
"s_waitcnt lgkmcnt(6)\n"
"v_mfma_f32_16x16x32_bf16 v[160:163], a[224:227], a[24:27], v[160:163]\n"
"ds_read_b64_tr_b16 a[216:217], v238 offset:0\n"
"v_mfma_f32_16x16x32_bf16 v[164:167], a[224:227], a[28:31], v[164:167]\n"
"ds_read_b64_tr_b16 a[218:219], v238 offset:2048\n"
"v_mfma_f32_16x16x32_bf16 v[168:171], a[224:227], a[32:35], v[168:171]\n"
"buffer_load_dwordx4 %[dX], %[rWo], s68 offen offset:1024 lds\n"
"s_add_u32 s68, s68, 9216\n"
"s_waitcnt lgkmcnt(6)\n"
"v_mfma_f32_16x16x32_bf16 v[172:175], a[228:231], a[0:3], v[172:175]\n"
"ds_read_b64_tr_b16 a[220:221], v238 offset:4096\n"
"v_mfma_f32_16x16x32_bf16 v[176:179], a[228:231], a[4:7], v[176:179]\n"
"ds_read_b64_tr_b16 a[222:223], v238 offset:6144\n"
"v_mfma_f32_16x16x32_bf16 v[180:183], a[228:231], a[8:11], v[180:183]\n"
"s_waitcnt lgkmcnt(6)\n"
"v_mfma_f32_16x16x32_bf16 v[172:175], a[232:235], a[12:15], v[172:175]\n"
"ds_read_b64_tr_b16 a[224:225], v238 offset:8192\n"
"v_mfma_f32_16x16x32_bf16 v[176:179], a[232:235], a[16:19], v[176:179]\n"
"ds_read_b64_tr_b16 a[226:227], v238 offset:10240\n"
"v_mfma_f32_16x16x32_bf16 v[180:183], a[232:235], a[20:23], v[180:183]\n"
"buffer_load_dwordx4 %[dX], %[rWo], s68 offen offset:2048 lds\n"
"s_add_u32 m0, s61, 3072\n"
"s_add_u32 s68, s68, 12288\n"
"s_waitcnt lgkmcnt(6)\n"
"v_mfma_f32_16x16x32_bf16 v[172:175], a[236:239], a[24:27], v[172:175]\n"
"ds_read_b64_tr_b16 a[228:229], v239 offset:0\n"
"v_mfma_f32_16x16x32_bf16 v[176:179], a[236:239], a[28:31], v[176:179]\n"
"ds_read_b64_tr_b16 a[230:231], v239 offset:2048\n"
"v_mfma_f32_16x16x32_bf16 v[180:183], a[236:239], a[32:35], v[180:183]\n"
"s_waitcnt lgkmcnt(6)\n"
"v_mfma_f32_16x16x32_bf16 v[184:187], a[216:219], a[0:3], v[184:187]\n"
"ds_read_b64_tr_b16 a[232:233], v239 offset:4096\n"
"v_mfma_f32_16x16x32_bf16 v[188:191], a[216:219], a[4:7], v[188:191]\n"
"ds_read_b64_tr_b16 a[234:235], v239 offset:6144\n"
"v_mfma_f32_16x16x32_bf16 v[192:195], a[216:219], a[8:11], v[192:195]\n"
"buffer_load_dwordx4 %[dX], %[rWo], s68 offen offset:0 lds\n"
"s_add_u32 s68, s68, 9216\n"
"s_waitcnt lgkmcnt(6)\n"
"v_mfma_f32_16x16x32_bf16 v[184:187], a[220:223], a[12:15], v[184:187]\n"
"ds_read_b64_tr_b16 a[236:237], v239 offset:8192\n"
"v_mfma_f32_16x16x32_bf16 v[188:191], a[220:223], a[16:19], v[188:191]\n"
"ds_read_b64_tr_b16 a[238:239], v239 offset:10240\n"
"v_mfma_f32_16x16x32_bf16 v[192:195], a[220:223], a[20:23], v[192:195]\n"
"buffer_load_dwordx4 %[dX], %[rWo], s68 offen offset:1024 lds\n"
"s_add_u32 s68, s68, 9216\n"
"s_waitcnt lgkmcnt(6)\n"
"v_mfma_f32_16x16x32_bf16 v[184:187], a[224:227], a[24:27], v[184:187]\n"
"s_add_u32 s61, %[m0b], s60\n"
"s_add_u32 s60, s60, 12288\n"
"s_cmp_eq_u32 s60, 61440\n"
"s_cselect_b32 s60, 0, s60\n"
"v_add_u32 v236, s60, %[rdO]\n"
"v_mfma_f32_16x16x32_bf16 v[188:191], a[224:227], a[28:31], v[188:191]\n"
"v_xor_b32 v237, 32, v236\n"
"v_xor_b32 v238, 64, v236\n"
"v_xor_b32 v239, 96, v236\n"
"v_mfma_f32_16x16x32_bf16 v[192:195], a[224:227], a[32:35], v[192:195]\n"
"s_waitcnt vmcnt(17) lgkmcnt(0)\n"
"s_barrier\n"
"ds_read_b64_tr_b16 a[216:217], v236 offset:0\n"
"ds_read_b64_tr_b16 a[218:219], v236 offset:2048\n"
"v_mfma_f32_16x16x32_bf16 v[196:199], a[228:231], a[0:3], v[196:199]\n"
"ds_read_b64_tr_b16 a[220:221], v236 offset:4096\n"
"v_mfma_f32_16x16x32_bf16 v[200:203], a[228:231], a[4:7], v[200:203]\n"
"ds_read_b64_tr_b16 a[222:223], v236 offset:6144\n"
"v_mfma_f32_16x16x32_bf16 v[204:207], a[228:231], a[8:11], v[204:207]\n"
"v_mfma_f32_16x16x32_bf16 v[196:199], a[232:235], a[12:15], v[196:199]\n"
"ds_read_b64_tr_b16 a[224:225], v236 offset:8192\n"
"v_mfma_f32_16x16x32_bf16 v[200:203], a[232:235], a[16:19], v[200:203]\n"
"ds_read_b64_tr_b16 a[226:227], v236 offset:10240\n"
"v_mfma_f32_16x16x32_bf16 v[204:207], a[232:235], a[20:23], v[204:207]\n"
"buffer_load_dwordx4 %[dX], %[rWo], s68 offen offset:2048 lds\n"
"v_mfma_f32_16x16x32_bf16 v[196:199], a[236:239], a[24:27], v[196:199]\n"
"ds_read_b64_tr_b16 a[228:229], v237 offset:0\n"
"v_mfma_f32_16x16x32_bf16 v[200:203], a[236:239], a[28:31], v[200:203]\n"
"ds_read_b64_tr_b16 a[230:231], v237 offset:2048\n"
"v_mfma_f32_16x16x32_bf16 v[204:207], a[236:239], a[32:35], v[204:207]\n"
"s_waitcnt lgkmcnt(6)\n"
"v_mfma_f32_16x16x32_bf16 v[160:163], a[216:219], a[36:39], v[160:163]\n"
"ds_read_b64_tr_b16 a[232:233], v237 offset:4096\n"
"v_mfma_f32_16x16x32_bf16 v[164:167], a[216:219], a[40:43], v[164:167]\n"
"ds_read_b64_tr_b16 a[234:235], v237 offset:6144\n"
"v_mfma_f32_16x16x32_bf16 v[168:171], a[216:219], a[44:47], v[168:171]\n"
"s_mov_b32 m0, s61\n"
"s_add_u32 s68, %[wo1], s72\n"
"s_add_u32 s68, s68, 122880\n"
"buffer_load_dwordx4 %[dX], %[rWo], s68 offen offset:0 lds\n"
"s_add_u32 s68, s68, 9216\n"
"s_waitcnt lgkmcnt(6)\n"
"v_mfma_f32_16x16x32_bf16 v[160:163], a[220:223], a[48:51], v[160:163]\n"
"ds_read_b64_tr_b16 a[236:237], v237 offset:8192\n"
"v_mfma_f32_16x16x32_bf16 v[164:167], a[220:223], a[52:55], v[164:167]\n"
"ds_read_b64_tr_b16 a[238:239], v237 offset:10240\n"
"v_mfma_f32_16x16x32_bf16 v[168:171], a[220:223], a[56:59], v[168:171]\n"
"s_waitcnt lgkmcnt(6)\n"
"v_mfma_f32_16x16x32_bf16 v[160:163], a[224:227], a[60:63], v[160:163]\n"
"ds_read_b64_tr_b16 a[216:217], v238 offset:0\n"
"v_mfma_f32_16x16x32_bf16 v[164:167], a[224:227], a[64:67], v[164:167]\n"
"ds_read_b64_tr_b16 a[218:219], v238 offset:2048\n"
"v_mfma_f32_16x16x32_bf16 v[168:171], a[224:227], a[68:71], v[168:171]\n"
"buffer_load_dwordx4 %[dX], %[rWo], s68 offen offset:1024 lds\n"
"s_add_u32 s68, s68, 9216\n"
"s_waitcnt lgkmcnt(6)\n"
"v_mfma_f32_16x16x32_bf16 v[172:175], a[228:231], a[36:39], v[172:175]\n"
"ds_read_b64_tr_b16 a[220:221], v238 offset:4096\n"
"v_mfma_f32_16x16x32_bf16 v[176:179], a[228:231], a[40:43], v[176:179]\n"
"ds_read_b64_tr_b16 a[222:223], v238 offset:6144\n"
"v_mfma_f32_16x16x32_bf16 v[180:183], a[228:231], a[44:47], v[180:183]\n"
"s_waitcnt lgkmcnt(6)\n"
"v_mfma_f32_16x16x32_bf16 v[172:175], a[232:235], a[48:51], v[172:175]\n"
"ds_read_b64_tr_b16 a[224:225], v238 offset:8192\n"
"v_cvt_pk_bf16_f32 v208, v160, v161\n"
"v_mfma_f32_16x16x32_bf16 v[176:179], a[232:235], a[52:55], v[176:179]\n"
"ds_read_b64_tr_b16 a[226:227], v238 offset:10240\n"
"v_cvt_pk_bf16_f32 v209, v162, v163\n"
"v_cvt_pk_bf16_f32 v212, v164, v165\n"
"v_mfma_f32_16x16x32_bf16 v[180:183], a[232:235], a[56:59], v[180:183]\n"
"buffer_load_dwordx4 %[dX], %[rWo], s68 offen offset:2048 lds\n"
"s_add_u32 m0, s61, 3072\n"
"s_add_u32 s68, s68, 12288\n"
"s_waitcnt lgkmcnt(6)\n"
"v_mfma_f32_16x16x32_bf16 v[172:175], a[236:239], a[60:63], v[172:175]\n"
"ds_read_b64_tr_b16 a[228:229], v239 offset:0\n"
"v_mfma_f32_16x16x32_bf16 v[176:179], a[236:239], a[64:67], v[176:179]\n"
"ds_read_b64_tr_b16 a[230:231], v239 offset:2048\n"
"v_cvt_pk_bf16_f32 v213, v166, v167\n"
"v_mfma_f32_16x16x32_bf16 v[180:183], a[236:239], a[68:71], v[180:183]\n"
"v_cvt_pk_bf16_f32 v216, v168, v169\n"
"v_cvt_pk_bf16_f32 v217, v170, v171\n"
"s_waitcnt lgkmcnt(6)\n"
"v_mfma_f32_16x16x32_bf16 v[184:187], a[216:219], a[36:39], v[184:187]\n"
"ds_read_b64_tr_b16 a[232:233], v239 offset:4096\n"
"v_mfma_f32_16x16x32_bf16 v[188:191], a[216:219], a[40:43], v[188:191]\n"
"ds_read_b64_tr_b16 a[234:235], v239 offset:6144\n"
"v_mfma_f32_16x16x32_bf16 v[192:195], a[216:219], a[44:47], v[192:195]\n"
"buffer_load_dwordx4 %[dX], %[rWo], s68 offen offset:0 lds\n"
"s_add_u32 s68, s68, 9216\n"
"s_waitcnt lgkmcnt(6)\n"
"v_mfma_f32_16x16x32_bf16 v[184:187], a[220:223], a[48:51], v[184:187]\n"
"ds_read_b64_tr_b16 a[236:237], v239 offset:8192\n"
"v_cvt_pk_bf16_f32 v210, v172, v173\n"
"v_cvt_pk_bf16_f32 v211, v174, v175\n"
"v_cvt_pk_bf16_f32 v214, v176, v177\n"
"v_mfma_f32_16x16x32_bf16 v[188:191], a[220:223], a[52:55], v[188:191]\n"
"ds_read_b64_tr_b16 a[238:239], v239 offset:10240\n"
"v_cvt_pk_bf16_f32 v215, v178, v179\n"
"v_cvt_pk_bf16_f32 v218, v180, v181\n"
"v_cvt_pk_bf16_f32 v219, v182, v183\n"
"v_mfma_f32_16x16x32_bf16 v[192:195], a[220:223], a[56:59], v[192:195]\n"
"buffer_load_dwordx4 %[dX], %[rWo], s68 offen offset:1024 lds\n"
"s_add_u32 s68, s68, 9216\n"
"s_waitcnt lgkmcnt(6)\n"
"v_mfma_f32_16x16x32_bf16 v[184:187], a[224:227], a[60:63], v[184:187]\n"
"s_add_u32 s61, %[m0b], s60\n"
"s_add_u32 s60, s60, 12288\n"
"s_cmp_eq_u32 s60, 61440\n"
"s_cselect_b32 s60, 0, s60\n"
"v_add_u32 v236, s60, %[rdO]\n"
"v_mfma_f32_16x16x32_bf16 v[188:191], a[224:227], a[64:67], v[188:191]\n"
"v_xor_b32 v237, 32, v236\n"
"v_xor_b32 v238, 64, v236\n"
"v_xor_b32 v239, 96, v236\n"
"v_mfma_f32_16x16x32_bf16 v[192:195], a[224:227], a[68:71], v[192:195]\n"
"s_waitcnt vmcnt(17) lgkmcnt(0)\n"
"s_barrier\n"
"ds_read_b64_tr_b16 a[216:217], v236 offset:0\n"
"ds_read_b64_tr_b16 a[218:219], v236 offset:2048\n"
"v_mfma_f32_16x16x32_bf16 v[196:199], a[228:231], a[36:39], v[196:199]\n"
"ds_read_b64_tr_b16 a[220:221], v236 offset:4096\n"
"v_mfma_f32_16x16x32_bf16 v[200:203], a[228:231], a[40:43], v[200:203]\n"
"ds_read_b64_tr_b16 a[222:223], v236 offset:6144\n"
"v_mfma_f32_16x16x32_bf16 v[204:207], a[228:231], a[44:47], v[204:207]\n"
"v_mfma_f32_16x16x32_bf16 v[196:199], a[232:235], a[48:51], v[196:199]\n"
"ds_read_b64_tr_b16 a[224:225], v236 offset:8192\n"
"v_mfma_f32_16x16x32_bf16 v[200:203], a[232:235], a[52:55], v[200:203]\n"
"ds_read_b64_tr_b16 a[226:227], v236 offset:10240\n"
"v_mfma_f32_16x16x32_bf16 v[204:207], a[232:235], a[56:59], v[204:207]\n"
"buffer_load_dwordx4 %[dX], %[rWo], s68 offen offset:2048 lds\n"
"v_cvt_pk_bf16_f32 v220, v184, v185\n"
"v_cvt_pk_bf16_f32 v221, v186, v187\n"
"v_mfma_f32_16x16x32_bf16 v[196:199], a[236:239], a[60:63], v[196:199]\n"
"ds_read_b64_tr_b16 a[228:229], v237 offset:0\n"
"buffer_store_dwordx4 v[208:211], %[stv], %[rWs], s73 offen offset:0 sc1\n"
"v_cvt_pk_bf16_f32 v224, v188, v189\n"
"v_cvt_pk_bf16_f32 v225, v190, v191\n"
"v_mfma_f32_16x16x32_bf16 v[200:203], a[236:239], a[64:67], v[200:203]\n"
"ds_read_b64_tr_b16 a[230:231], v237 offset:2048\n"
"buffer_store_dwordx4 v[212:215], %[stv], %[rWs], s73 offen offset:1024 sc1\n"
"v_cvt_pk_bf16_f32 v228, v192, v193\n"
"v_mfma_f32_16x16x32_bf16 v[204:207], a[236:239], a[68:71], v[204:207]\n"
"buffer_store_dwordx4 v[216:219], %[stv], %[rWs], s73 offen offset:2048 sc1\n"
"s_add_u32 s73, s73, 3072\n"
"v_cvt_pk_bf16_f32 v229, v194, v195\n"
"s_mov_b32 s69, 2\n"
".p2align 3\n"
"Ld_%=:\n"
"s_waitcnt lgkmcnt(6)\n"
"v_mfma_f32_16x16x32_bf16 v[160:163], a[216:219], v[16:19], 0\n"
"ds_read_b64_tr_b16 a[232:233], v237 offset:4096\n"
"v_mfma_f32_16x16x32_bf16 v[164:167], a[216:219], v[20:23], 0\n"
"ds_read_b64_tr_b16 a[234:235], v237 offset:6144\n"
"v_mfma_f32_16x16x32_bf16 v[168:171], a[216:219], v[24:27], 0\n"
"s_mov_b32 m0, s61\n"
"s_add_u32 s68, %[wo0], s72\n"
"s_add_u32 s68, s68, 983040\n"
"buffer_load_dwordx4 %[dX], %[rWo], s68 offen offset:0 lds\n"
"s_add_u32 s68, s68, 9216\n"
"s_waitcnt lgkmcnt(6)\n"
"v_mfma_f32_16x16x32_bf16 v[160:163], a[220:223], v[28:31], v[160:163]\n"
"ds_read_b64_tr_b16 a[236:237], v237 offset:8192\n"
"v_cvt_pk_bf16_f32 v222, v196, v197\n"
"v_mfma_f32_16x16x32_bf16 v[164:167], a[220:223], v[32:35], v[164:167]\n"
"ds_read_b64_tr_b16 a[238:239], v237 offset:10240\n"
"v_cvt_pk_bf16_f32 v223, v198, v199\n"
"v_mfma_f32_16x16x32_bf16 v[168:171], a[220:223], v[36:39], v[168:171]\n"
"v_cvt_pk_bf16_f32 v226, v200, v201\n"
"v_cvt_pk_bf16_f32 v227, v202, v203\n"
"v_cvt_pk_bf16_f32 v230, v204, v205\n"
"s_waitcnt lgkmcnt(6)\n"
"v_mfma_f32_16x16x32_bf16 v[160:163], a[224:227], v[40:43], v[160:163]\n"
"ds_read_b64_tr_b16 a[216:217], v238 offset:0\n"
"v_cvt_pk_bf16_f32 v231, v206, v207\n"
"v_mfma_f32_16x16x32_bf16 v[164:167], a[224:227], v[44:47], v[164:167]\n"
"ds_read_b64_tr_b16 a[218:219], v238 offset:2048\n"
"v_mfma_f32_16x16x32_bf16 v[168:171], a[224:227], v[48:51], v[168:171]\n"
"buffer_load_dwordx4 %[dX], %[rWo], s68 offen offset:1024 lds\n"
"s_add_u32 s68, s68, 9216\n"
"s_waitcnt lgkmcnt(6)\n"
"v_mfma_f32_16x16x32_bf16 v[172:175], a[228:231], v[16:19], 0\n"
"ds_read_b64_tr_b16 a[220:221], v238 offset:4096\n"
"v_mfma_f32_16x16x32_bf16 v[176:179], a[228:231], v[20:23], 0\n"
"ds_read_b64_tr_b16 a[222:223], v238 offset:6144\n"
"v_mfma_f32_16x16x32_bf16 v[180:183], a[228:231], v[24:27], 0\n"
"s_waitcnt lgkmcnt(6)\n"
"v_mfma_f32_16x16x32_bf16 v[172:175], a[232:235], v[28:31], v[172:175]\n"
"ds_read_b64_tr_b16 a[224:225], v238 offset:8192\n"
"v_mfma_f32_16x16x32_bf16 v[176:179], a[232:235], v[32:35], v[176:179]\n"
"ds_read_b64_tr_b16 a[226:227], v238 offset:10240\n"
"v_mfma_f32_16x16x32_bf16 v[180:183], a[232:235], v[36:39], v[180:183]\n"
"buffer_load_dwordx4 %[dX], %[rWo], s68 offen offset:2048 lds\n"
"s_add_u32 m0, s61, 3072\n"
"s_add_u32 s68, s68, 12288\n"
"s_waitcnt lgkmcnt(6)\n"
"v_mfma_f32_16x16x32_bf16 v[172:175], a[236:239], v[40:43], v[172:175]\n"
"ds_read_b64_tr_b16 a[228:229], v239 offset:0\n"
"v_mfma_f32_16x16x32_bf16 v[176:179], a[236:239], v[44:47], v[176:179]\n"
"ds_read_b64_tr_b16 a[230:231], v239 offset:2048\n"
"v_mfma_f32_16x16x32_bf16 v[180:183], a[236:239], v[48:51], v[180:183]\n"
"s_waitcnt lgkmcnt(6)\n"
"v_mfma_f32_16x16x32_bf16 v[184:187], a[216:219], v[16:19], 0\n"
"ds_read_b64_tr_b16 a[232:233], v239 offset:4096\n"
"v_mfma_f32_16x16x32_bf16 v[188:191], a[216:219], v[20:23], 0\n"
"ds_read_b64_tr_b16 a[234:235], v239 offset:6144\n"
"v_mfma_f32_16x16x32_bf16 v[192:195], a[216:219], v[24:27], 0\n"
"buffer_load_dwordx4 %[dX], %[rWo], s68 offen offset:0 lds\n"
"s_add_u32 s68, s68, 9216\n"
"s_waitcnt lgkmcnt(6)\n"
"v_mfma_f32_16x16x32_bf16 v[184:187], a[220:223], v[28:31], v[184:187]\n"
"ds_read_b64_tr_b16 a[236:237], v239 offset:8192\n"
"v_mfma_f32_16x16x32_bf16 v[188:191], a[220:223], v[32:35], v[188:191]\n"
"ds_read_b64_tr_b16 a[238:239], v239 offset:10240\n"
"v_mfma_f32_16x16x32_bf16 v[192:195], a[220:223], v[36:39], v[192:195]\n"
"buffer_load_dwordx4 %[dX], %[rWo], s68 offen offset:1024 lds\n"
"s_add_u32 s68, s68, 9216\n"
"s_waitcnt lgkmcnt(6)\n"
"v_mfma_f32_16x16x32_bf16 v[184:187], a[224:227], v[40:43], v[184:187]\n"
"s_add_u32 s61, %[m0b], s60\n"
"s_add_u32 s60, s60, 12288\n"
"s_cmp_eq_u32 s60, 61440\n"
"s_cselect_b32 s60, 0, s60\n"
"v_add_u32 v236, s60, %[rdO]\n"
"v_mfma_f32_16x16x32_bf16 v[188:191], a[224:227], v[44:47], v[188:191]\n"
"v_xor_b32 v237, 32, v236\n"
"v_xor_b32 v238, 64, v236\n"
"v_xor_b32 v239, 96, v236\n"
"v_mfma_f32_16x16x32_bf16 v[192:195], a[224:227], v[48:51], v[192:195]\n"
"s_waitcnt vmcnt(20) lgkmcnt(0)\n"
"s_barrier\n"
"ds_read_b64_tr_b16 a[216:217], v236 offset:0\n"
"ds_read_b64_tr_b16 a[218:219], v236 offset:2048\n"
"v_mfma_f32_16x16x32_bf16 v[196:199], a[228:231], v[16:19], 0\n"
"ds_read_b64_tr_b16 a[220:221], v236 offset:4096\n"
"v_mfma_f32_16x16x32_bf16 v[200:203], a[228:231], v[20:23], 0\n"
"ds_read_b64_tr_b16 a[222:223], v236 offset:6144\n"
"v_mfma_f32_16x16x32_bf16 v[204:207], a[228:231], v[24:27], 0\n"
"v_mfma_f32_16x16x32_bf16 v[196:199], a[232:235], v[28:31], v[196:199]\n"
"ds_read_b64_tr_b16 a[224:225], v236 offset:8192\n"
"v_mfma_f32_16x16x32_bf16 v[200:203], a[232:235], v[32:35], v[200:203]\n"
"ds_read_b64_tr_b16 a[226:227], v236 offset:10240\n"
"v_mfma_f32_16x16x32_bf16 v[204:207], a[232:235], v[36:39], v[204:207]\n"
"buffer_load_dwordx4 %[dX], %[rWo], s68 offen offset:2048 lds\n"
"v_mfma_f32_16x16x32_bf16 v[196:199], a[236:239], v[40:43], v[196:199]\n"
"ds_read_b64_tr_b16 a[228:229], v237 offset:0\n"
"buffer_store_dwordx4 v[220:223], %[stv], %[rWs], s73 offen offset:0 sc1\n"
"v_mfma_f32_16x16x32_bf16 v[200:203], a[236:239], v[44:47], v[200:203]\n"
"ds_read_b64_tr_b16 a[230:231], v237 offset:2048\n"
"buffer_store_dwordx4 v[224:227], %[stv], %[rWs], s73 offen offset:1024 sc1\n"
"buffer_store_dwordx4 v[228:231], %[stv], %[rWs], s73 offen offset:2048 sc1\n"
"v_mfma_f32_16x16x32_bf16 v[204:207], a[236:239], v[48:51], v[204:207]\n"
"s_add_u32 s73, s73, 3072\n"
"s_cmp_eq_u32 s73, s58\n"
"s_cselect_b32 s73, %[st0], s73\n"
"s_waitcnt lgkmcnt(6)\n"
"v_mfma_f32_16x16x32_bf16 v[160:163], a[216:219], v[52:55], v[160:163]\n"
"ds_read_b64_tr_b16 a[232:233], v237 offset:4096\n"
"v_mfma_f32_16x16x32_bf16 v[164:167], a[216:219], v[56:59], v[164:167]\n"
"ds_read_b64_tr_b16 a[234:235], v237 offset:6144\n"
"v_mfma_f32_16x16x32_bf16 v[168:171], a[216:219], v[60:63], v[168:171]\n"
"s_mov_b32 m0, s61\n"
"s_add_u32 s68, %[wo0], s72\n"
"s_add_u32 s68, s68, 1105920\n"
"buffer_load_dwordx4 %[dX], %[rWo], s68 offen offset:0 lds\n"
"s_add_u32 s68, s68, 9216\n"
"s_waitcnt lgkmcnt(6)\n"
"v_mfma_f32_16x16x32_bf16 v[160:163], a[220:223], v[64:67], v[160:163]\n"
"ds_read_b64_tr_b16 a[236:237], v237 offset:8192\n"
"v_mfma_f32_16x16x32_bf16 v[164:167], a[220:223], v[68:71], v[164:167]\n"
"ds_read_b64_tr_b16 a[238:239], v237 offset:10240\n"
"v_mfma_f32_16x16x32_bf16 v[168:171], a[220:223], v[72:75], v[168:171]\n"
"s_waitcnt lgkmcnt(6)\n"
"v_mfma_f32_16x16x32_bf16 v[160:163], a[224:227], v[76:79], v[160:163]\n"
"ds_read_b64_tr_b16 a[216:217], v238 offset:0\n"
"v_mfma_f32_16x16x32_bf16 v[164:167], a[224:227], v[80:83], v[164:167]\n"
"ds_read_b64_tr_b16 a[218:219], v238 offset:2048\n"
"v_mfma_f32_16x16x32_bf16 v[168:171], a[224:227], v[84:87], v[168:171]\n"
"buffer_load_dwordx4 %[dX], %[rWo], s68 offen offset:1024 lds\n"
"s_add_u32 s68, s68, 9216\n"
"s_waitcnt lgkmcnt(6)\n"
"v_mfma_f32_16x16x32_bf16 v[172:175], a[228:231], v[52:55], v[172:175]\n"
"ds_read_b64_tr_b16 a[220:221], v238 offset:4096\n"
"v_mfma_f32_16x16x32_bf16 v[176:179], a[228:231], v[56:59], v[176:179]\n"
"ds_read_b64_tr_b16 a[222:223], v238 offset:6144\n"
"v_mfma_f32_16x16x32_bf16 v[180:183], a[228:231], v[60:63], v[180:183]\n"
"s_waitcnt lgkmcnt(6)\n"
"v_mfma_f32_16x16x32_bf16 v[172:175], a[232:235], v[64:67], v[172:175]\n"
"ds_read_b64_tr_b16 a[224:225], v238 offset:8192\n"
"v_mfma_f32_16x16x32_bf16 v[176:179], a[232:235], v[68:71], v[176:179]\n"
"ds_read_b64_tr_b16 a[226:227], v238 offset:10240\n"
"v_mfma_f32_16x16x32_bf16 v[180:183], a[232:235], v[72:75], v[180:183]\n"
"buffer_load_dwordx4 %[dX], %[rWo], s68 offen offset:2048 lds\n"
"s_add_u32 m0, s61, 3072\n"
"s_add_u32 s68, s68, 12288\n"
"s_waitcnt lgkmcnt(6)\n"
"v_mfma_f32_16x16x32_bf16 v[172:175], a[236:239], v[76:79], v[172:175]\n"
"ds_read_b64_tr_b16 a[228:229], v239 offset:0\n"
"v_mfma_f32_16x16x32_bf16 v[176:179], a[236:239], v[80:83], v[176:179]\n"
"ds_read_b64_tr_b16 a[230:231], v239 offset:2048\n"
"v_mfma_f32_16x16x32_bf16 v[180:183], a[236:239], v[84:87], v[180:183]\n"
"s_waitcnt lgkmcnt(6)\n"
"v_mfma_f32_16x16x32_bf16 v[184:187], a[216:219], v[52:55], v[184:187]\n"
"ds_read_b64_tr_b16 a[232:233], v239 offset:4096\n"
"v_mfma_f32_16x16x32_bf16 v[188:191], a[216:219], v[56:59], v[188:191]\n"
"ds_read_b64_tr_b16 a[234:235], v239 offset:6144\n"
"v_mfma_f32_16x16x32_bf16 v[192:195], a[216:219], v[60:63], v[192:195]\n"
"buffer_load_dwordx4 %[dX], %[rWo], s68 offen offset:0 lds\n"
"s_add_u32 s68, s68, 9216\n"
"s_waitcnt lgkmcnt(6)\n"
"v_mfma_f32_16x16x32_bf16 v[184:187], a[220:223], v[64:67], v[184:187]\n"
"ds_read_b64_tr_b16 a[236:237], v239 offset:8192\n"
"v_mfma_f32_16x16x32_bf16 v[188:191], a[220:223], v[68:71], v[188:191]\n"
"ds_read_b64_tr_b16 a[238:239], v239 offset:10240\n"
"v_mfma_f32_16x16x32_bf16 v[192:195], a[220:223], v[72:75], v[192:195]\n"
"buffer_load_dwordx4 %[dX], %[rWo], s68 offen offset:1024 lds\n"
"s_add_u32 s68, s68, 9216\n"
"s_waitcnt lgkmcnt(6)\n"
"v_mfma_f32_16x16x32_bf16 v[184:187], a[224:227], v[76:79], v[184:187]\n"
"s_add_u32 s61, %[m0b], s60\n"
"s_add_u32 s60, s60, 12288\n"
"s_cmp_eq_u32 s60, 61440\n"
"s_cselect_b32 s60, 0, s60\n"
"v_add_u32 v236, s60, %[rdO]\n"
"v_mfma_f32_16x16x32_bf16 v[188:191], a[224:227], v[80:83], v[188:191]\n"
"v_xor_b32 v237, 32, v236\n"
"v_xor_b32 v238, 64, v236\n"
"v_xor_b32 v239, 96, v236\n"
"v_mfma_f32_16x16x32_bf16 v[192:195], a[224:227], v[84:87], v[192:195]\n"
"s_waitcnt vmcnt(23) lgkmcnt(0)\n"
"s_barrier\n"
"ds_read_b64_tr_b16 a[216:217], v236 offset:0\n"
"ds_read_b64_tr_b16 a[218:219], v236 offset:2048\n"
"v_mfma_f32_16x16x32_bf16 v[196:199], a[228:231], v[52:55], v[196:199]\n"
"ds_read_b64_tr_b16 a[220:221], v236 offset:4096\n"
"v_mfma_f32_16x16x32_bf16 v[200:203], a[228:231], v[56:59], v[200:203]\n"
"ds_read_b64_tr_b16 a[222:223], v236 offset:6144\n"
"v_mfma_f32_16x16x32_bf16 v[204:207], a[228:231], v[60:63], v[204:207]\n"
"v_mfma_f32_16x16x32_bf16 v[196:199], a[232:235], v[64:67], v[196:199]\n"
"ds_read_b64_tr_b16 a[224:225], v236 offset:8192\n"
"v_mfma_f32_16x16x32_bf16 v[200:203], a[232:235], v[68:71], v[200:203]\n"
"ds_read_b64_tr_b16 a[226:227], v236 offset:10240\n"
"v_mfma_f32_16x16x32_bf16 v[204:207], a[232:235], v[72:75], v[204:207]\n"
"buffer_load_dwordx4 %[dX], %[rWo], s68 offen offset:2048 lds\n"
"v_mfma_f32_16x16x32_bf16 v[196:199], a[236:239], v[76:79], v[196:199]\n"
"ds_read_b64_tr_b16 a[228:229], v237 offset:0\n"
"v_mfma_f32_16x16x32_bf16 v[200:203], a[236:239], v[80:83], v[200:203]\n"
"ds_read_b64_tr_b16 a[230:231], v237 offset:2048\n"
"v_mfma_f32_16x16x32_bf16 v[204:207], a[236:239], v[84:87], v[204:207]\n"
"s_waitcnt lgkmcnt(6)\n"
"v_mfma_f32_16x16x32_bf16 v[160:163], a[216:219], v[88:91], v[160:163]\n"
"ds_read_b64_tr_b16 a[232:233], v237 offset:4096\n"
"v_mfma_f32_16x16x32_bf16 v[164:167], a[216:219], v[92:95], v[164:167]\n"
"ds_read_b64_tr_b16 a[234:235], v237 offset:6144\n"
"v_mfma_f32_16x16x32_bf16 v[168:171], a[216:219], v[96:99], v[168:171]\n"
"s_mov_b32 m0, s61\n"
"s_add_u32 s68, %[wo1], s72\n"
"s_add_u32 s68, s68, 983040\n"
"buffer_load_dwordx4 %[dX], %[rWo], s68 offen offset:0 lds\n"
"s_add_u32 s68, s68, 9216\n"
"s_waitcnt lgkmcnt(6)\n"
"v_mfma_f32_16x16x32_bf16 v[160:163], a[220:223], v[100:103], v[160:163]\n"
"ds_read_b64_tr_b16 a[236:237], v237 offset:8192\n"
"v_mfma_f32_16x16x32_bf16 v[164:167], a[220:223], v[104:107], v[164:167]\n"
"ds_read_b64_tr_b16 a[238:239], v237 offset:10240\n"
"v_mfma_f32_16x16x32_bf16 v[168:171], a[220:223], v[108:111], v[168:171]\n"
"s_waitcnt lgkmcnt(6)\n"
"v_mfma_f32_16x16x32_bf16 v[160:163], a[224:227], v[112:115], v[160:163]\n"
"ds_read_b64_tr_b16 a[216:217], v238 offset:0\n"
"v_mfma_f32_16x16x32_bf16 v[164:167], a[224:227], v[116:119], v[164:167]\n"
"ds_read_b64_tr_b16 a[218:219], v238 offset:2048\n"
"v_mfma_f32_16x16x32_bf16 v[168:171], a[224:227], v[120:123], v[168:171]\n"
"buffer_load_dwordx4 %[dX], %[rWo], s68 offen offset:1024 lds\n"
"s_add_u32 s68, s68, 9216\n"
"s_waitcnt lgkmcnt(6)\n"
"v_mfma_f32_16x16x32_bf16 v[172:175], a[228:231], v[88:91], v[172:175]\n"
"ds_read_b64_tr_b16 a[220:221], v238 offset:4096\n"
"v_mfma_f32_16x16x32_bf16 v[176:179], a[228:231], v[92:95], v[176:179]\n"
"ds_read_b64_tr_b16 a[222:223], v238 offset:6144\n"
"v_mfma_f32_16x16x32_bf16 v[180:183], a[228:231], v[96:99], v[180:183]\n"
"s_waitcnt lgkmcnt(6)\n"
"v_mfma_f32_16x16x32_bf16 v[172:175], a[232:235], v[100:103], v[172:175]\n"
"ds_read_b64_tr_b16 a[224:225], v238 offset:8192\n"
"v_mfma_f32_16x16x32_bf16 v[176:179], a[232:235], v[104:107], v[176:179]\n"
"ds_read_b64_tr_b16 a[226:227], v238 offset:10240\n"
"v_mfma_f32_16x16x32_bf16 v[180:183], a[232:235], v[108:111], v[180:183]\n"
"buffer_load_dwordx4 %[dX], %[rWo], s68 offen offset:2048 lds\n"
"s_add_u32 m0, s61, 3072\n"
"s_add_u32 s68, s68, 12288\n"
"s_waitcnt lgkmcnt(6)\n"
"v_mfma_f32_16x16x32_bf16 v[172:175], a[236:239], v[112:115], v[172:175]\n"
"ds_read_b64_tr_b16 a[228:229], v239 offset:0\n"
"v_mfma_f32_16x16x32_bf16 v[176:179], a[236:239], v[116:119], v[176:179]\n"
"ds_read_b64_tr_b16 a[230:231], v239 offset:2048\n"
"v_mfma_f32_16x16x32_bf16 v[180:183], a[236:239], v[120:123], v[180:183]\n"
"s_waitcnt lgkmcnt(6)\n"
"v_mfma_f32_16x16x32_bf16 v[184:187], a[216:219], v[88:91], v[184:187]\n"
"ds_read_b64_tr_b16 a[232:233], v239 offset:4096\n"
"v_mfma_f32_16x16x32_bf16 v[188:191], a[216:219], v[92:95], v[188:191]\n"
"ds_read_b64_tr_b16 a[234:235], v239 offset:6144\n"
"v_mfma_f32_16x16x32_bf16 v[192:195], a[216:219], v[96:99], v[192:195]\n"
"buffer_load_dwordx4 %[dX], %[rWo], s68 offen offset:0 lds\n"
"s_add_u32 s68, s68, 9216\n"
"s_waitcnt lgkmcnt(6)\n"
"v_mfma_f32_16x16x32_bf16 v[184:187], a[220:223], v[100:103], v[184:187]\n"
"ds_read_b64_tr_b16 a[236:237], v239 offset:8192\n"
"v_mfma_f32_16x16x32_bf16 v[188:191], a[220:223], v[104:107], v[188:191]\n"
"ds_read_b64_tr_b16 a[238:239], v239 offset:10240\n"
"v_mfma_f32_16x16x32_bf16 v[192:195], a[220:223], v[108:111], v[192:195]\n"
"buffer_load_dwordx4 %[dX], %[rWo], s68 offen offset:1024 lds\n"
"s_add_u32 s68, s68, 9216\n"
"s_waitcnt lgkmcnt(6)\n"
"v_mfma_f32_16x16x32_bf16 v[184:187], a[224:227], v[112:115], v[184:187]\n"
"s_add_u32 s61, %[m0b], s60\n"
"s_add_u32 s60, s60, 12288\n"
"s_cmp_eq_u32 s60, 61440\n"
"s_cselect_b32 s60, 0, s60\n"
"v_add_u32 v236, s60, %[rdO]\n"
"v_mfma_f32_16x16x32_bf16 v[188:191], a[224:227], v[116:119], v[188:191]\n"
"v_xor_b32 v237, 32, v236\n"
"v_xor_b32 v238, 64, v236\n"
"v_xor_b32 v239, 96, v236\n"
"v_mfma_f32_16x16x32_bf16 v[192:195], a[224:227], v[120:123], v[192:195]\n"
"s_waitcnt vmcnt(23) lgkmcnt(0)\n"
"s_barrier\n"
"ds_read_b64_tr_b16 a[216:217], v236 offset:0\n"
"ds_read_b64_tr_b16 a[218:219], v236 offset:2048\n"
"v_mfma_f32_16x16x32_bf16 v[196:199], a[228:231], v[88:91], v[196:199]\n"
"ds_read_b64_tr_b16 a[220:221], v236 offset:4096\n"
"v_mfma_f32_16x16x32_bf16 v[200:203], a[228:231], v[92:95], v[200:203]\n"
"ds_read_b64_tr_b16 a[222:223], v236 offset:6144\n"
"v_mfma_f32_16x16x32_bf16 v[204:207], a[228:231], v[96:99], v[204:207]\n"
"v_mfma_f32_16x16x32_bf16 v[196:199], a[232:235], v[100:103], v[196:199]\n"
"ds_read_b64_tr_b16 a[224:225], v236 offset:8192\n"
"v_mfma_f32_16x16x32_bf16 v[200:203], a[232:235], v[104:107], v[200:203]\n"
"ds_read_b64_tr_b16 a[226:227], v236 offset:10240\n"
"v_mfma_f32_16x16x32_bf16 v[204:207], a[232:235], v[108:111], v[204:207]\n"
"buffer_load_dwordx4 %[dX], %[rWo], s68 offen offset:2048 lds\n"
"v_mfma_f32_16x16x32_bf16 v[196:199], a[236:239], v[112:115], v[196:199]\n"
"ds_read_b64_tr_b16 a[228:229], v237 offset:0\n"
"v_mfma_f32_16x16x32_bf16 v[200:203], a[236:239], v[116:119], v[200:203]\n"
"ds_read_b64_tr_b16 a[230:231], v237 offset:2048\n"
"v_mfma_f32_16x16x32_bf16 v[204:207], a[236:239], v[120:123], v[204:207]\n"
"s_waitcnt lgkmcnt(6)\n"
"v_mfma_f32_16x16x32_bf16 v[160:163], a[216:219], v[124:127], v[160:163]\n"
"ds_read_b64_tr_b16 a[232:233], v237 offset:4096\n"
"v_mfma_f32_16x16x32_bf16 v[164:167], a[216:219], v[128:131], v[164:167]\n"
"ds_read_b64_tr_b16 a[234:235], v237 offset:6144\n"
"v_mfma_f32_16x16x32_bf16 v[168:171], a[216:219], v[132:135], v[168:171]\n"
"s_mov_b32 m0, s61\n"
"s_add_u32 s68, %[wo1], s72\n"
"s_add_u32 s68, s68, 1105920\n"
"buffer_load_dwordx4 %[dX], %[rWo], s68 offen offset:0 lds\n"
"s_add_u32 s68, s68, 9216\n"
"s_waitcnt lgkmcnt(6)\n"
"v_mfma_f32_16x16x32_bf16 v[160:163], a[220:223], v[136:139], v[160:163]\n"
"ds_read_b64_tr_b16 a[236:237], v237 offset:8192\n"
"v_mfma_f32_16x16x32_bf16 v[164:167], a[220:223], v[140:143], v[164:167]\n"
"ds_read_b64_tr_b16 a[238:239], v237 offset:10240\n"
"v_mfma_f32_16x16x32_bf16 v[168:171], a[220:223], v[144:147], v[168:171]\n"
"s_waitcnt lgkmcnt(6)\n"
"v_mfma_f32_16x16x32_bf16 v[160:163], a[224:227], v[148:151], v[160:163]\n"
"ds_read_b64_tr_b16 a[216:217], v238 offset:0\n"
"v_mfma_f32_16x16x32_bf16 v[164:167], a[224:227], v[152:155], v[164:167]\n"
"ds_read_b64_tr_b16 a[218:219], v238 offset:2048\n"
"v_mfma_f32_16x16x32_bf16 v[168:171], a[224:227], v[156:159], v[168:171]\n"
"buffer_load_dwordx4 %[dX], %[rWo], s68 offen offset:1024 lds\n"
"s_add_u32 s68, s68, 9216\n"
"s_waitcnt lgkmcnt(6)\n"
"v_mfma_f32_16x16x32_bf16 v[172:175], a[228:231], v[124:127], v[172:175]\n"
"ds_read_b64_tr_b16 a[220:221], v238 offset:4096\n"
"v_mfma_f32_16x16x32_bf16 v[176:179], a[228:231], v[128:131], v[176:179]\n"
"ds_read_b64_tr_b16 a[222:223], v238 offset:6144\n"
"v_mfma_f32_16x16x32_bf16 v[180:183], a[228:231], v[132:135], v[180:183]\n"
"s_waitcnt lgkmcnt(6)\n"
"v_mfma_f32_16x16x32_bf16 v[172:175], a[232:235], v[136:139], v[172:175]\n"
"ds_read_b64_tr_b16 a[224:225], v238 offset:8192\n"
"v_mfma_f32_16x16x32_bf16 v[176:179], a[232:235], v[140:143], v[176:179]\n"
"ds_read_b64_tr_b16 a[226:227], v238 offset:10240\n"
"v_mfma_f32_16x16x32_bf16 v[180:183], a[232:235], v[144:147], v[180:183]\n"
"buffer_load_dwordx4 %[dX], %[rWo], s68 offen offset:2048 lds\n"
"s_add_u32 m0, s61, 3072\n"
"s_add_u32 s68, s68, 12288\n"
"s_waitcnt lgkmcnt(6)\n"
"v_mfma_f32_16x16x32_bf16 v[172:175], a[236:239], v[148:151], v[172:175]\n"
"ds_read_b64_tr_b16 a[228:229], v239 offset:0\n"
"v_mfma_f32_16x16x32_bf16 v[176:179], a[236:239], v[152:155], v[176:179]\n"
"ds_read_b64_tr_b16 a[230:231], v239 offset:2048\n"
"v_mfma_f32_16x16x32_bf16 v[180:183], a[236:239], v[156:159], v[180:183]\n"
"s_waitcnt lgkmcnt(6)\n"
"v_mfma_f32_16x16x32_bf16 v[184:187], a[216:219], v[124:127], v[184:187]\n"
"ds_read_b64_tr_b16 a[232:233], v239 offset:4096\n"
"v_mfma_f32_16x16x32_bf16 v[188:191], a[216:219], v[128:131], v[188:191]\n"
"ds_read_b64_tr_b16 a[234:235], v239 offset:6144\n"
"v_mfma_f32_16x16x32_bf16 v[192:195], a[216:219], v[132:135], v[192:195]\n"
"buffer_load_dwordx4 %[dX], %[rWo], s68 offen offset:0 lds\n"
"s_add_u32 s68, s68, 9216\n"
"s_waitcnt lgkmcnt(6)\n"
"v_mfma_f32_16x16x32_bf16 v[184:187], a[220:223], v[136:139], v[184:187]\n"
"ds_read_b64_tr_b16 a[236:237], v239 offset:8192\n"
"v_mfma_f32_16x16x32_bf16 v[188:191], a[220:223], v[140:143], v[188:191]\n"
"ds_read_b64_tr_b16 a[238:239], v239 offset:10240\n"
"v_mfma_f32_16x16x32_bf16 v[192:195], a[220:223], v[144:147], v[192:195]\n"
"buffer_load_dwordx4 %[dX], %[rWo], s68 offen offset:1024 lds\n"
"s_add_u32 s68, s68, 9216\n"
"s_waitcnt lgkmcnt(6)\n"
"v_mfma_f32_16x16x32_bf16 v[184:187], a[224:227], v[148:151], v[184:187]\n"
"s_add_u32 s61, %[m0b], s60\n"
"s_add_u32 s60, s60, 12288\n"
"s_cmp_eq_u32 s60, 61440\n"
"s_cselect_b32 s60, 0, s60\n"
"v_add_u32 v236, s60, %[rdO]\n"
"v_mfma_f32_16x16x32_bf16 v[188:191], a[224:227], v[152:155], v[188:191]\n"
"v_xor_b32 v237, 32, v236\n"
"v_xor_b32 v238, 64, v236\n"
"v_xor_b32 v239, 96, v236\n"
"v_mfma_f32_16x16x32_bf16 v[192:195], a[224:227], v[156:159], v[192:195]\n"
"s_waitcnt vmcnt(20) lgkmcnt(0)\n"
"s_barrier\n"
"ds_read_b64_tr_b16 a[216:217], v236 offset:0\n"
"ds_read_b64_tr_b16 a[218:219], v236 offset:2048\n"
"v_mfma_f32_16x16x32_bf16 v[196:199], a[228:231], v[124:127], v[196:199]\n"
"ds_read_b64_tr_b16 a[220:221], v236 offset:4096\n"
"v_mfma_f32_16x16x32_bf16 v[200:203], a[228:231], v[128:131], v[200:203]\n"
"ds_read_b64_tr_b16 a[222:223], v236 offset:6144\n"
"v_mfma_f32_16x16x32_bf16 v[204:207], a[228:231], v[132:135], v[204:207]\n"
"v_mfma_f32_16x16x32_bf16 v[196:199], a[232:235], v[136:139], v[196:199]\n"
"ds_read_b64_tr_b16 a[224:225], v236 offset:8192\n"
"v_mfma_f32_16x16x32_bf16 v[200:203], a[232:235], v[140:143], v[200:203]\n"
"ds_read_b64_tr_b16 a[226:227], v236 offset:10240\n"
"v_mfma_f32_16x16x32_bf16 v[204:207], a[232:235], v[144:147], v[204:207]\n"
"buffer_load_dwordx4 %[dX], %[rWo], s68 offen offset:2048 lds\n"
"s_add_u32 s72, s72, 128\n"
"s_cmp_eq_u32 s72, 640\n"
"s_cselect_b32 s72, 0, s72\n"
"v_mfma_f32_16x16x32_bf16 v[196:199], a[236:239], v[148:151], v[196:199]\n"
"ds_read_b64_tr_b16 a[228:229], v237 offset:0\n"
"v_mfma_f32_16x16x32_bf16 v[200:203], a[236:239], v[152:155], v[200:203]\n"
"ds_read_b64_tr_b16 a[230:231], v237 offset:2048\n"
"v_mfma_f32_16x16x32_bf16 v[204:207], a[236:239], v[156:159], v[204:207]\n"
"s_waitcnt lgkmcnt(6)\n"
"v_mfma_f32_16x16x32_bf16 v[160:163], a[216:219], a[144:147], v[160:163]\n"
"ds_read_b64_tr_b16 a[232:233], v237 offset:4096\n"
"v_mfma_f32_16x16x32_bf16 v[164:167], a[216:219], a[148:151], v[164:167]\n"
"ds_read_b64_tr_b16 a[234:235], v237 offset:6144\n"
"v_mfma_f32_16x16x32_bf16 v[168:171], a[216:219], a[152:155], v[168:171]\n"
"s_mov_b32 m0, s61\n"
"s_add_u32 s68, %[wo0], s72\n"
"buffer_load_dwordx4 %[dX], %[rWo], s68 offen offset:0 lds\n"
"s_add_u32 s68, s68, 9216\n"
"s_waitcnt lgkmcnt(6)\n"
"v_mfma_f32_16x16x32_bf16 v[160:163], a[220:223], a[156:159], v[160:163]\n"
"ds_read_b64_tr_b16 a[236:237], v237 offset:8192\n"
"v_mfma_f32_16x16x32_bf16 v[164:167], a[220:223], a[160:163], v[164:167]\n"
"ds_read_b64_tr_b16 a[238:239], v237 offset:10240\n"
"v_mfma_f32_16x16x32_bf16 v[168:171], a[220:223], a[164:167], v[168:171]\n"
"s_waitcnt lgkmcnt(6)\n"
"v_mfma_f32_16x16x32_bf16 v[160:163], a[224:227], a[168:171], v[160:163]\n"
"ds_read_b64_tr_b16 a[216:217], v238 offset:0\n"
"v_mfma_f32_16x16x32_bf16 v[164:167], a[224:227], a[172:175], v[164:167]\n"
"ds_read_b64_tr_b16 a[218:219], v238 offset:2048\n"
"v_mfma_f32_16x16x32_bf16 v[168:171], a[224:227], a[176:179], v[168:171]\n"
"buffer_load_dwordx4 %[dX], %[rWo], s68 offen offset:1024 lds\n"
"s_add_u32 s68, s68, 9216\n"
"s_waitcnt lgkmcnt(6)\n"
"v_mfma_f32_16x16x32_bf16 v[172:175], a[228:231], a[144:147], v[172:175]\n"
"ds_read_b64_tr_b16 a[220:221], v238 offset:4096\n"
"v_mfma_f32_16x16x32_bf16 v[176:179], a[228:231], a[148:151], v[176:179]\n"
"ds_read_b64_tr_b16 a[222:223], v238 offset:6144\n"
"v_mfma_f32_16x16x32_bf16 v[180:183], a[228:231], a[152:155], v[180:183]\n"
"s_waitcnt lgkmcnt(6)\n"
"v_mfma_f32_16x16x32_bf16 v[172:175], a[232:235], a[156:159], v[172:175]\n"
"ds_read_b64_tr_b16 a[224:225], v238 offset:8192\n"
"v_mfma_f32_16x16x32_bf16 v[176:179], a[232:235], a[160:163], v[176:179]\n"
"ds_read_b64_tr_b16 a[226:227], v238 offset:10240\n"
"v_mfma_f32_16x16x32_bf16 v[180:183], a[232:235], a[164:167], v[180:183]\n"
"buffer_load_dwordx4 %[dX], %[rWo], s68 offen offset:2048 lds\n"
"s_add_u32 m0, s61, 3072\n"
"s_add_u32 s68, s68, 12288\n"
"s_waitcnt lgkmcnt(6)\n"
"v_mfma_f32_16x16x32_bf16 v[172:175], a[236:239], a[168:171], v[172:175]\n"
"ds_read_b64_tr_b16 a[228:229], v239 offset:0\n"
"v_mfma_f32_16x16x32_bf16 v[176:179], a[236:239], a[172:175], v[176:179]\n"
"ds_read_b64_tr_b16 a[230:231], v239 offset:2048\n"
"v_mfma_f32_16x16x32_bf16 v[180:183], a[236:239], a[176:179], v[180:183]\n"
"s_waitcnt lgkmcnt(6)\n"
"v_mfma_f32_16x16x32_bf16 v[184:187], a[216:219], a[144:147], v[184:187]\n"
"ds_read_b64_tr_b16 a[232:233], v239 offset:4096\n"
"v_mfma_f32_16x16x32_bf16 v[188:191], a[216:219], a[148:151], v[188:191]\n"
"ds_read_b64_tr_b16 a[234:235], v239 offset:6144\n"
"v_mfma_f32_16x16x32_bf16 v[192:195], a[216:219], a[152:155], v[192:195]\n"
"buffer_load_dwordx4 %[dX], %[rWo], s68 offen offset:0 lds\n"
"s_add_u32 s68, s68, 9216\n"
"s_waitcnt lgkmcnt(6)\n"
"v_mfma_f32_16x16x32_bf16 v[184:187], a[220:223], a[156:159], v[184:187]\n"
"ds_read_b64_tr_b16 a[236:237], v239 offset:8192\n"
"v_mfma_f32_16x16x32_bf16 v[188:191], a[220:223], a[160:163], v[188:191]\n"
"ds_read_b64_tr_b16 a[238:239], v239 offset:10240\n"
"v_mfma_f32_16x16x32_bf16 v[192:195], a[220:223], a[164:167], v[192:195]\n"
"buffer_load_dwordx4 %[dX], %[rWo], s68 offen offset:1024 lds\n"
"s_add_u32 s68, s68, 9216\n"
"s_waitcnt lgkmcnt(6)\n"
"v_mfma_f32_16x16x32_bf16 v[184:187], a[224:227], a[168:171], v[184:187]\n"
"s_add_u32 s61, %[m0b], s60\n"
"s_add_u32 s60, s60, 12288\n"
"s_cmp_eq_u32 s60, 61440\n"
"s_cselect_b32 s60, 0, s60\n"
"v_add_u32 v236, s60, %[rdO]\n"
"v_mfma_f32_16x16x32_bf16 v[188:191], a[224:227], a[172:175], v[188:191]\n"
"v_xor_b32 v237, 32, v236\n"
"v_xor_b32 v238, 64, v236\n"
"v_xor_b32 v239, 96, v236\n"
"v_mfma_f32_16x16x32_bf16 v[192:195], a[224:227], a[176:179], v[192:195]\n"
"s_waitcnt vmcnt(17) lgkmcnt(0)\n"
"s_barrier\n"
"ds_read_b64_tr_b16 a[216:217], v236 offset:0\n"
"ds_read_b64_tr_b16 a[218:219], v236 offset:2048\n"
"v_mfma_f32_16x16x32_bf16 v[196:199], a[228:231], a[144:147], v[196:199]\n"
"ds_read_b64_tr_b16 a[220:221], v236 offset:4096\n"
"v_mfma_f32_16x16x32_bf16 v[200:203], a[228:231], a[148:151], v[200:203]\n"
"ds_read_b64_tr_b16 a[222:223], v236 offset:6144\n"
"v_mfma_f32_16x16x32_bf16 v[204:207], a[228:231], a[152:155], v[204:207]\n"
"v_mfma_f32_16x16x32_bf16 v[196:199], a[232:235], a[156:159], v[196:199]\n"
"ds_read_b64_tr_b16 a[224:225], v236 offset:8192\n"
"v_mfma_f32_16x16x32_bf16 v[200:203], a[232:235], a[160:163], v[200:203]\n"
"ds_read_b64_tr_b16 a[226:227], v236 offset:10240\n"
"v_mfma_f32_16x16x32_bf16 v[204:207], a[232:235], a[164:167], v[204:207]\n"
"buffer_load_dwordx4 %[dX], %[rWo], s68 offen offset:2048 lds\n"
"v_mfma_f32_16x16x32_bf16 v[196:199], a[236:239], a[168:171], v[196:199]\n"
"ds_read_b64_tr_b16 a[228:229], v237 offset:0\n"
"v_mfma_f32_16x16x32_bf16 v[200:203], a[236:239], a[172:175], v[200:203]\n"
"ds_read_b64_tr_b16 a[230:231], v237 offset:2048\n"
"v_mfma_f32_16x16x32_bf16 v[204:207], a[236:239], a[176:179], v[204:207]\n"
"s_waitcnt lgkmcnt(6)\n"
"v_mfma_f32_16x16x32_bf16 v[160:163], a[216:219], a[180:183], v[160:163]\n"
"ds_read_b64_tr_b16 a[232:233], v237 offset:4096\n"
"v_mfma_f32_16x16x32_bf16 v[164:167], a[216:219], a[184:187], v[164:167]\n"
"ds_read_b64_tr_b16 a[234:235], v237 offset:6144\n"
"v_mfma_f32_16x16x32_bf16 v[168:171], a[216:219], a[188:191], v[168:171]\n"
"s_mov_b32 m0, s61\n"
"s_add_u32 s68, %[wo0], s72\n"
"s_add_u32 s68, s68, 122880\n"
"buffer_load_dwordx4 %[dX], %[rWo], s68 offen offset:0 lds\n"
"s_add_u32 s68, s68, 9216\n"
"s_waitcnt lgkmcnt(6)\n"
"v_mfma_f32_16x16x32_bf16 v[160:163], a[220:223], a[192:195], v[160:163]\n"
"ds_read_b64_tr_b16 a[236:237], v237 offset:8192\n"
"v_mfma_f32_16x16x32_bf16 v[164:167], a[220:223], a[196:199], v[164:167]\n"
"ds_read_b64_tr_b16 a[238:239], v237 offset:10240\n"
"v_mfma_f32_16x16x32_bf16 v[168:171], a[220:223], a[200:203], v[168:171]\n"
"s_waitcnt lgkmcnt(6)\n"
"v_mfma_f32_16x16x32_bf16 v[160:163], a[224:227], a[204:207], v[160:163]\n"
"ds_read_b64_tr_b16 a[216:217], v238 offset:0\n"
"v_mfma_f32_16x16x32_bf16 v[164:167], a[224:227], a[208:211], v[164:167]\n"
"ds_read_b64_tr_b16 a[218:219], v238 offset:2048\n"
"v_mfma_f32_16x16x32_bf16 v[168:171], a[224:227], a[212:215], v[168:171]\n"
"buffer_load_dwordx4 %[dX], %[rWo], s68 offen offset:1024 lds\n"
"s_add_u32 s68, s68, 9216\n"
"s_waitcnt lgkmcnt(6)\n"
"v_mfma_f32_16x16x32_bf16 v[172:175], a[228:231], a[180:183], v[172:175]\n"
"ds_read_b64_tr_b16 a[220:221], v238 offset:4096\n"
"v_mfma_f32_16x16x32_bf16 v[176:179], a[228:231], a[184:187], v[176:179]\n"
"ds_read_b64_tr_b16 a[222:223], v238 offset:6144\n"
"v_mfma_f32_16x16x32_bf16 v[180:183], a[228:231], a[188:191], v[180:183]\n"
"s_waitcnt lgkmcnt(6)\n"
"v_mfma_f32_16x16x32_bf16 v[172:175], a[232:235], a[192:195], v[172:175]\n"
"ds_read_b64_tr_b16 a[224:225], v238 offset:8192\n"
"v_mfma_f32_16x16x32_bf16 v[176:179], a[232:235], a[196:199], v[176:179]\n"
"ds_read_b64_tr_b16 a[226:227], v238 offset:10240\n"
"v_mfma_f32_16x16x32_bf16 v[180:183], a[232:235], a[200:203], v[180:183]\n"
"buffer_load_dwordx4 %[dX], %[rWo], s68 offen offset:2048 lds\n"
"s_add_u32 m0, s61, 3072\n"
"s_add_u32 s68, s68, 12288\n"
"s_waitcnt lgkmcnt(6)\n"
"v_mfma_f32_16x16x32_bf16 v[172:175], a[236:239], a[204:207], v[172:175]\n"
"ds_read_b64_tr_b16 a[228:229], v239 offset:0\n"
"v_mfma_f32_16x16x32_bf16 v[176:179], a[236:239], a[208:211], v[176:179]\n"
"ds_read_b64_tr_b16 a[230:231], v239 offset:2048\n"
"v_mfma_f32_16x16x32_bf16 v[180:183], a[236:239], a[212:215], v[180:183]\n"
"s_waitcnt lgkmcnt(6)\n"
"v_mfma_f32_16x16x32_bf16 v[184:187], a[216:219], a[180:183], v[184:187]\n"
"ds_read_b64_tr_b16 a[232:233], v239 offset:4096\n"
"v_mfma_f32_16x16x32_bf16 v[188:191], a[216:219], a[184:187], v[188:191]\n"
"ds_read_b64_tr_b16 a[234:235], v239 offset:6144\n"
"v_mfma_f32_16x16x32_bf16 v[192:195], a[216:219], a[188:191], v[192:195]\n"
"buffer_load_dwordx4 %[dX], %[rWo], s68 offen offset:0 lds\n"
"s_add_u32 s68, s68, 9216\n"
"s_waitcnt lgkmcnt(6)\n"
"v_mfma_f32_16x16x32_bf16 v[184:187], a[220:223], a[192:195], v[184:187]\n"
"ds_read_b64_tr_b16 a[236:237], v239 offset:8192\n"
"v_mfma_f32_16x16x32_bf16 v[188:191], a[220:223], a[196:199], v[188:191]\n"
"ds_read_b64_tr_b16 a[238:239], v239 offset:10240\n"
"v_mfma_f32_16x16x32_bf16 v[192:195], a[220:223], a[200:203], v[192:195]\n"
"buffer_load_dwordx4 %[dX], %[rWo], s68 offen offset:1024 lds\n"
"s_add_u32 s68, s68, 9216\n"
"s_waitcnt lgkmcnt(6)\n"
"v_mfma_f32_16x16x32_bf16 v[184:187], a[224:227], a[204:207], v[184:187]\n"
"s_add_u32 s61, %[m0b], s60\n"
"s_add_u32 s60, s60, 12288\n"
"s_cmp_eq_u32 s60, 61440\n"
"s_cselect_b32 s60, 0, s60\n"
"v_add_u32 v236, s60, %[rdO]\n"
"v_mfma_f32_16x16x32_bf16 v[188:191], a[224:227], a[208:211], v[188:191]\n"
"v_xor_b32 v237, 32, v236\n"
"v_xor_b32 v238, 64, v236\n"
"v_xor_b32 v239, 96, v236\n"
"v_mfma_f32_16x16x32_bf16 v[192:195], a[224:227], a[212:215], v[192:195]\n"
"s_waitcnt vmcnt(17) lgkmcnt(0)\n"
"s_barrier\n"
"ds_read_b64_tr_b16 a[216:217], v236 offset:0\n"
"ds_read_b64_tr_b16 a[218:219], v236 offset:2048\n"
"v_mfma_f32_16x16x32_bf16 v[196:199], a[228:231], a[180:183], v[196:199]\n"
"ds_read_b64_tr_b16 a[220:221], v236 offset:4096\n"
"v_mfma_f32_16x16x32_bf16 v[200:203], a[228:231], a[184:187], v[200:203]\n"
"ds_read_b64_tr_b16 a[222:223], v236 offset:6144\n"
"v_mfma_f32_16x16x32_bf16 v[204:207], a[228:231], a[188:191], v[204:207]\n"
"v_mfma_f32_16x16x32_bf16 v[196:199], a[232:235], a[192:195], v[196:199]\n"
"ds_read_b64_tr_b16 a[224:225], v236 offset:8192\n"
"v_mfma_f32_16x16x32_bf16 v[200:203], a[232:235], a[196:199], v[200:203]\n"
"ds_read_b64_tr_b16 a[226:227], v236 offset:10240\n"
"v_mfma_f32_16x16x32_bf16 v[204:207], a[232:235], a[200:203], v[204:207]\n"
"buffer_load_dwordx4 %[dX], %[rWo], s68 offen offset:2048 lds\n"
"v_mfma_f32_16x16x32_bf16 v[196:199], a[236:239], a[204:207], v[196:199]\n"
"ds_read_b64_tr_b16 a[228:229], v237 offset:0\n"
"v_mfma_f32_16x16x32_bf16 v[200:203], a[236:239], a[208:211], v[200:203]\n"
"ds_read_b64_tr_b16 a[230:231], v237 offset:2048\n"
"v_mfma_f32_16x16x32_bf16 v[204:207], a[236:239], a[212:215], v[204:207]\n"
"s_waitcnt lgkmcnt(6)\n"
"v_mfma_f32_16x16x32_bf16 v[160:163], a[216:219], a[0:3], v[160:163]\n"
"ds_read_b64_tr_b16 a[232:233], v237 offset:4096\n"
"v_mfma_f32_16x16x32_bf16 v[164:167], a[216:219], a[4:7], v[164:167]\n"
"ds_read_b64_tr_b16 a[234:235], v237 offset:6144\n"
"v_mfma_f32_16x16x32_bf16 v[168:171], a[216:219], a[8:11], v[168:171]\n"
"s_mov_b32 m0, s61\n"
"s_add_u32 s68, %[wo1], s72\n"
"buffer_load_dwordx4 %[dX], %[rWo], s68 offen offset:0 lds\n"
"s_add_u32 s68, s68, 9216\n"
"s_waitcnt lgkmcnt(6)\n"
"v_mfma_f32_16x16x32_bf16 v[160:163], a[220:223], a[12:15], v[160:163]\n"
"ds_read_b64_tr_b16 a[236:237], v237 offset:8192\n"
"v_mfma_f32_16x16x32_bf16 v[164:167], a[220:223], a[16:19], v[164:167]\n"
"ds_read_b64_tr_b16 a[238:239], v237 offset:10240\n"
"v_mfma_f32_16x16x32_bf16 v[168:171], a[220:223], a[20:23], v[168:171]\n"
"s_waitcnt lgkmcnt(6)\n"
"v_mfma_f32_16x16x32_bf16 v[160:163], a[224:227], a[24:27], v[160:163]\n"
"ds_read_b64_tr_b16 a[216:217], v238 offset:0\n"
"v_mfma_f32_16x16x32_bf16 v[164:167], a[224:227], a[28:31], v[164:167]\n"
"ds_read_b64_tr_b16 a[218:219], v238 offset:2048\n"
"v_mfma_f32_16x16x32_bf16 v[168:171], a[224:227], a[32:35], v[168:171]\n"
"buffer_load_dwordx4 %[dX], %[rWo], s68 offen offset:1024 lds\n"
"s_add_u32 s68, s68, 9216\n"
"s_waitcnt lgkmcnt(6)\n"
"v_mfma_f32_16x16x32_bf16 v[172:175], a[228:231], a[0:3], v[172:175]\n"
"ds_read_b64_tr_b16 a[220:221], v238 offset:4096\n"
"v_mfma_f32_16x16x32_bf16 v[176:179], a[228:231], a[4:7], v[176:179]\n"
"ds_read_b64_tr_b16 a[222:223], v238 offset:6144\n"
"v_mfma_f32_16x16x32_bf16 v[180:183], a[228:231], a[8:11], v[180:183]\n"
"s_waitcnt lgkmcnt(6)\n"
"v_mfma_f32_16x16x32_bf16 v[172:175], a[232:235], a[12:15], v[172:175]\n"
"ds_read_b64_tr_b16 a[224:225], v238 offset:8192\n"
"v_mfma_f32_16x16x32_bf16 v[176:179], a[232:235], a[16:19], v[176:179]\n"
"ds_read_b64_tr_b16 a[226:227], v238 offset:10240\n"
"v_mfma_f32_16x16x32_bf16 v[180:183], a[232:235], a[20:23], v[180:183]\n"
"buffer_load_dwordx4 %[dX], %[rWo], s68 offen offset:2048 lds\n"
"s_add_u32 m0, s61, 3072\n"
"s_add_u32 s68, s68, 12288\n"
"s_waitcnt lgkmcnt(6)\n"
"v_mfma_f32_16x16x32_bf16 v[172:175], a[236:239], a[24:27], v[172:175]\n"
"ds_read_b64_tr_b16 a[228:229], v239 offset:0\n"
"v_mfma_f32_16x16x32_bf16 v[176:179], a[236:239], a[28:31], v[176:179]\n"
"ds_read_b64_tr_b16 a[230:231], v239 offset:2048\n"
"v_mfma_f32_16x16x32_bf16 v[180:183], a[236:239], a[32:35], v[180:183]\n"
"s_waitcnt lgkmcnt(6)\n"
"v_mfma_f32_16x16x32_bf16 v[184:187], a[216:219], a[0:3], v[184:187]\n"
"ds_read_b64_tr_b16 a[232:233], v239 offset:4096\n"
"v_mfma_f32_16x16x32_bf16 v[188:191], a[216:219], a[4:7], v[188:191]\n"
"ds_read_b64_tr_b16 a[234:235], v239 offset:6144\n"
"v_mfma_f32_16x16x32_bf16 v[192:195], a[216:219], a[8:11], v[192:195]\n"
"buffer_load_dwordx4 %[dX], %[rWo], s68 offen offset:0 lds\n"
"s_add_u32 s68, s68, 9216\n"
"s_waitcnt lgkmcnt(6)\n"
"v_mfma_f32_16x16x32_bf16 v[184:187], a[220:223], a[12:15], v[184:187]\n"
"ds_read_b64_tr_b16 a[236:237], v239 offset:8192\n"
"v_mfma_f32_16x16x32_bf16 v[188:191], a[220:223], a[16:19], v[188:191]\n"
"ds_read_b64_tr_b16 a[238:239], v239 offset:10240\n"
"v_mfma_f32_16x16x32_bf16 v[192:195], a[220:223], a[20:23], v[192:195]\n"
"buffer_load_dwordx4 %[dX], %[rWo], s68 offen offset:1024 lds\n"
"s_add_u32 s68, s68, 9216\n"
"s_waitcnt lgkmcnt(6)\n"
"v_mfma_f32_16x16x32_bf16 v[184:187], a[224:227], a[24:27], v[184:187]\n"
"s_add_u32 s61, %[m0b], s60\n"
"s_add_u32 s60, s60, 12288\n"
"s_cmp_eq_u32 s60, 61440\n"
"s_cselect_b32 s60, 0, s60\n"
"v_add_u32 v236, s60, %[rdO]\n"
"v_mfma_f32_16x16x32_bf16 v[188:191], a[224:227], a[28:31], v[188:191]\n"
"v_xor_b32 v237, 32, v236\n"
"v_xor_b32 v238, 64, v236\n"
"v_xor_b32 v239, 96, v236\n"
"v_mfma_f32_16x16x32_bf16 v[192:195], a[224:227], a[32:35], v[192:195]\n"
"s_waitcnt vmcnt(17) lgkmcnt(0)\n"
"s_barrier\n"
"ds_read_b64_tr_b16 a[216:217], v236 offset:0\n"
"ds_read_b64_tr_b16 a[218:219], v236 offset:2048\n"
"v_mfma_f32_16x16x32_bf16 v[196:199], a[228:231], a[0:3], v[196:199]\n"
"ds_read_b64_tr_b16 a[220:221], v236 offset:4096\n"
"v_mfma_f32_16x16x32_bf16 v[200:203], a[228:231], a[4:7], v[200:203]\n"
"ds_read_b64_tr_b16 a[222:223], v236 offset:6144\n"
"v_mfma_f32_16x16x32_bf16 v[204:207], a[228:231], a[8:11], v[204:207]\n"
"v_mfma_f32_16x16x32_bf16 v[196:199], a[232:235], a[12:15], v[196:199]\n"
"ds_read_b64_tr_b16 a[224:225], v236 offset:8192\n"
"v_mfma_f32_16x16x32_bf16 v[200:203], a[232:235], a[16:19], v[200:203]\n"
"ds_read_b64_tr_b16 a[226:227], v236 offset:10240\n"
"v_mfma_f32_16x16x32_bf16 v[204:207], a[232:235], a[20:23], v[204:207]\n"
"buffer_load_dwordx4 %[dX], %[rWo], s68 offen offset:2048 lds\n"
"v_mfma_f32_16x16x32_bf16 v[196:199], a[236:239], a[24:27], v[196:199]\n"
"ds_read_b64_tr_b16 a[228:229], v237 offset:0\n"
"v_mfma_f32_16x16x32_bf16 v[200:203], a[236:239], a[28:31], v[200:203]\n"
"ds_read_b64_tr_b16 a[230:231], v237 offset:2048\n"
"v_mfma_f32_16x16x32_bf16 v[204:207], a[236:239], a[32:35], v[204:207]\n"
"s_waitcnt lgkmcnt(6)\n"
"v_mfma_f32_16x16x32_bf16 v[160:163], a[216:219], a[36:39], v[160:163]\n"
"ds_read_b64_tr_b16 a[232:233], v237 offset:4096\n"
"v_mfma_f32_16x16x32_bf16 v[164:167], a[216:219], a[40:43], v[164:167]\n"
"ds_read_b64_tr_b16 a[234:235], v237 offset:6144\n"
"v_mfma_f32_16x16x32_bf16 v[168:171], a[216:219], a[44:47], v[168:171]\n"
"s_mov_b32 m0, s61\n"
"s_add_u32 s68, %[wo1], s72\n"
"s_add_u32 s68, s68, 122880\n"
"buffer_load_dwordx4 %[dX], %[rWo], s68 offen offset:0 lds\n"
"s_add_u32 s68, s68, 9216\n"
"s_waitcnt lgkmcnt(6)\n"
"v_mfma_f32_16x16x32_bf16 v[160:163], a[220:223], a[48:51], v[160:163]\n"
"ds_read_b64_tr_b16 a[236:237], v237 offset:8192\n"
"v_mfma_f32_16x16x32_bf16 v[164:167], a[220:223], a[52:55], v[164:167]\n"
"ds_read_b64_tr_b16 a[238:239], v237 offset:10240\n"
"v_mfma_f32_16x16x32_bf16 v[168:171], a[220:223], a[56:59], v[168:171]\n"
"s_waitcnt lgkmcnt(6)\n"
"v_mfma_f32_16x16x32_bf16 v[160:163], a[224:227], a[60:63], v[160:163]\n"
"ds_read_b64_tr_b16 a[216:217], v238 offset:0\n"
"v_mfma_f32_16x16x32_bf16 v[164:167], a[224:227], a[64:67], v[164:167]\n"
"ds_read_b64_tr_b16 a[218:219], v238 offset:2048\n"
"v_mfma_f32_16x16x32_bf16 v[168:171], a[224:227], a[68:71], v[168:171]\n"
"buffer_load_dwordx4 %[dX], %[rWo], s68 offen offset:1024 lds\n"
"s_add_u32 s68, s68, 9216\n"
"s_waitcnt lgkmcnt(6)\n"
"v_mfma_f32_16x16x32_bf16 v[172:175], a[228:231], a[36:39], v[172:175]\n"
"ds_read_b64_tr_b16 a[220:221], v238 offset:4096\n"
"v_mfma_f32_16x16x32_bf16 v[176:179], a[228:231], a[40:43], v[176:179]\n"
"ds_read_b64_tr_b16 a[222:223], v238 offset:6144\n"
"v_mfma_f32_16x16x32_bf16 v[180:183], a[228:231], a[44:47], v[180:183]\n"
"s_waitcnt lgkmcnt(6)\n"
"v_mfma_f32_16x16x32_bf16 v[172:175], a[232:235], a[48:51], v[172:175]\n"
"ds_read_b64_tr_b16 a[224:225], v238 offset:8192\n"
"v_cvt_pk_bf16_f32 v208, v160, v161\n"
"v_mfma_f32_16x16x32_bf16 v[176:179], a[232:235], a[52:55], v[176:179]\n"
"ds_read_b64_tr_b16 a[226:227], v238 offset:10240\n"
"v_cvt_pk_bf16_f32 v209, v162, v163\n"
"v_cvt_pk_bf16_f32 v212, v164, v165\n"
"v_mfma_f32_16x16x32_bf16 v[180:183], a[232:235], a[56:59], v[180:183]\n"
"buffer_load_dwordx4 %[dX], %[rWo], s68 offen offset:2048 lds\n"
"s_add_u32 m0, s61, 3072\n"
"s_add_u32 s68, s68, 12288\n"
"s_waitcnt lgkmcnt(6)\n"
"v_mfma_f32_16x16x32_bf16 v[172:175], a[236:239], a[60:63], v[172:175]\n"
"ds_read_b64_tr_b16 a[228:229], v239 offset:0\n"
"v_mfma_f32_16x16x32_bf16 v[176:179], a[236:239], a[64:67], v[176:179]\n"
"ds_read_b64_tr_b16 a[230:231], v239 offset:2048\n"
"v_cvt_pk_bf16_f32 v213, v166, v167\n"
"v_mfma_f32_16x16x32_bf16 v[180:183], a[236:239], a[68:71], v[180:183]\n"
"v_cvt_pk_bf16_f32 v216, v168, v169\n"
"v_cvt_pk_bf16_f32 v217, v170, v171\n"
"s_waitcnt lgkmcnt(6)\n"
"v_mfma_f32_16x16x32_bf16 v[184:187], a[216:219], a[36:39], v[184:187]\n"
"ds_read_b64_tr_b16 a[232:233], v239 offset:4096\n"
"v_mfma_f32_16x16x32_bf16 v[188:191], a[216:219], a[40:43], v[188:191]\n"
"ds_read_b64_tr_b16 a[234:235], v239 offset:6144\n"
"v_mfma_f32_16x16x32_bf16 v[192:195], a[216:219], a[44:47], v[192:195]\n"
"buffer_load_dwordx4 %[dX], %[rWo], s68 offen offset:0 lds\n"
"s_add_u32 s68, s68, 9216\n"
"s_waitcnt lgkmcnt(6)\n"
"v_mfma_f32_16x16x32_bf16 v[184:187], a[220:223], a[48:51], v[184:187]\n"
"ds_read_b64_tr_b16 a[236:237], v239 offset:8192\n"
"v_cvt_pk_bf16_f32 v210, v172, v173\n"
"v_cvt_pk_bf16_f32 v211, v174, v175\n"
"v_cvt_pk_bf16_f32 v214, v176, v177\n"
"v_mfma_f32_16x16x32_bf16 v[188:191], a[220:223], a[52:55], v[188:191]\n"
"ds_read_b64_tr_b16 a[238:239], v239 offset:10240\n"
"v_cvt_pk_bf16_f32 v215, v178, v179\n"
"v_cvt_pk_bf16_f32 v218, v180, v181\n"
"v_cvt_pk_bf16_f32 v219, v182, v183\n"
"v_mfma_f32_16x16x32_bf16 v[192:195], a[220:223], a[56:59], v[192:195]\n"
"buffer_load_dwordx4 %[dX], %[rWo], s68 offen offset:1024 lds\n"
"s_add_u32 s68, s68, 9216\n"
"s_waitcnt lgkmcnt(6)\n"
"v_mfma_f32_16x16x32_bf16 v[184:187], a[224:227], a[60:63], v[184:187]\n"
"s_add_u32 s61, %[m0b], s60\n"
"s_add_u32 s60, s60, 12288\n"
"s_cmp_eq_u32 s60, 61440\n"
"s_cselect_b32 s60, 0, s60\n"
"v_add_u32 v236, s60, %[rdO]\n"
"v_mfma_f32_16x16x32_bf16 v[188:191], a[224:227], a[64:67], v[188:191]\n"
"v_xor_b32 v237, 32, v236\n"
"v_xor_b32 v238, 64, v236\n"
"v_xor_b32 v239, 96, v236\n"
"v_mfma_f32_16x16x32_bf16 v[192:195], a[224:227], a[68:71], v[192:195]\n"
"s_waitcnt vmcnt(17) lgkmcnt(0)\n"
"s_barrier\n"
"ds_read_b64_tr_b16 a[216:217], v236 offset:0\n"
"ds_read_b64_tr_b16 a[218:219], v236 offset:2048\n"
"v_mfma_f32_16x16x32_bf16 v[196:199], a[228:231], a[36:39], v[196:199]\n"
"ds_read_b64_tr_b16 a[220:221], v236 offset:4096\n"
"v_mfma_f32_16x16x32_bf16 v[200:203], a[228:231], a[40:43], v[200:203]\n"
"ds_read_b64_tr_b16 a[222:223], v236 offset:6144\n"
"v_mfma_f32_16x16x32_bf16 v[204:207], a[228:231], a[44:47], v[204:207]\n"
"v_mfma_f32_16x16x32_bf16 v[196:199], a[232:235], a[48:51], v[196:199]\n"
"ds_read_b64_tr_b16 a[224:225], v236 offset:8192\n"
"v_mfma_f32_16x16x32_bf16 v[200:203], a[232:235], a[52:55], v[200:203]\n"
"ds_read_b64_tr_b16 a[226:227], v236 offset:10240\n"
"v_mfma_f32_16x16x32_bf16 v[204:207], a[232:235], a[56:59], v[204:207]\n"
"buffer_load_dwordx4 %[dX], %[rWo], s68 offen offset:2048 lds\n"
"v_cvt_pk_bf16_f32 v220, v184, v185\n"
"v_cvt_pk_bf16_f32 v221, v186, v187\n"
"v_mfma_f32_16x16x32_bf16 v[196:199], a[236:239], a[60:63], v[196:199]\n"
"ds_read_b64_tr_b16 a[228:229], v237 offset:0\n"
"buffer_store_dwordx4 v[208:211], %[stv], %[rWs], s73 offen offset:0 sc1\n"
"v_cvt_pk_bf16_f32 v224, v188, v189\n"
"v_cvt_pk_bf16_f32 v225, v190, v191\n"
"v_mfma_f32_16x16x32_bf16 v[200:203], a[236:239], a[64:67], v[200:203]\n"
"ds_read_b64_tr_b16 a[230:231], v237 offset:2048\n"
"buffer_store_dwordx4 v[212:215], %[stv], %[rWs], s73 offen offset:1024 sc1\n"
"v_cvt_pk_bf16_f32 v228, v192, v193\n"
"v_mfma_f32_16x16x32_bf16 v[204:207], a[236:239], a[68:71], v[204:207]\n"
"buffer_store_dwordx4 v[216:219], %[stv], %[rWs], s73 offen offset:2048 sc1\n"
"s_add_u32 s73, s73, 3072\n"
"v_cvt_pk_bf16_f32 v229, v194, v195\n"
"s_sub_u32 s69, s69, 1\n"
"s_cmp_lg_u32 s69, 0\n"
"s_cbranch_scc1 Ld_%=\n"
"s_waitcnt lgkmcnt(6)\n"
"v_mfma_f32_16x16x32_bf16 v[160:163], a[216:219], v[16:19], 0\n"
"ds_read_b64_tr_b16 a[232:233], v237 offset:4096\n"
"v_mfma_f32_16x16x32_bf16 v[164:167], a[216:219], v[20:23], 0\n"
"ds_read_b64_tr_b16 a[234:235], v237 offset:6144\n"
"v_mfma_f32_16x16x32_bf16 v[168:171], a[216:219], v[24:27], 0\n"
"s_mov_b32 m0, s61\n"
"s_add_u32 s68, %[wo0], s72\n"
"s_add_u32 s68, s68, 983040\n"
"buffer_load_dwordx4 %[dX], %[rWo], s68 offen offset:0 lds\n"
"s_add_u32 s68, s68, 9216\n"
"s_waitcnt lgkmcnt(6)\n"
"v_mfma_f32_16x16x32_bf16 v[160:163], a[220:223], v[28:31], v[160:163]\n"
"ds_read_b64_tr_b16 a[236:237], v237 offset:8192\n"
"v_cvt_pk_bf16_f32 v222, v196, v197\n"
"v_mfma_f32_16x16x32_bf16 v[164:167], a[220:223], v[32:35], v[164:167]\n"
"ds_read_b64_tr_b16 a[238:239], v237 offset:10240\n"
"v_cvt_pk_bf16_f32 v223, v198, v199\n"
"v_mfma_f32_16x16x32_bf16 v[168:171], a[220:223], v[36:39], v[168:171]\n"
"v_cvt_pk_bf16_f32 v226, v200, v201\n"
"v_cvt_pk_bf16_f32 v227, v202, v203\n"
"v_cvt_pk_bf16_f32 v230, v204, v205\n"
"s_waitcnt lgkmcnt(6)\n"
"v_mfma_f32_16x16x32_bf16 v[160:163], a[224:227], v[40:43], v[160:163]\n"
"ds_read_b64_tr_b16 a[216:217], v238 offset:0\n"
"v_cvt_pk_bf16_f32 v231, v206, v207\n"
"v_mfma_f32_16x16x32_bf16 v[164:167], a[224:227], v[44:47], v[164:167]\n"
"ds_read_b64_tr_b16 a[218:219], v238 offset:2048\n"
"v_mfma_f32_16x16x32_bf16 v[168:171], a[224:227], v[48:51], v[168:171]\n"
"buffer_load_dwordx4 %[dX], %[rWo], s68 offen offset:1024 lds\n"
"s_add_u32 s68, s68, 9216\n"
"s_waitcnt lgkmcnt(6)\n"
"v_mfma_f32_16x16x32_bf16 v[172:175], a[228:231], v[16:19], 0\n"
"ds_read_b64_tr_b16 a[220:221], v238 offset:4096\n"
"v_mfma_f32_16x16x32_bf16 v[176:179], a[228:231], v[20:23], 0\n"
"ds_read_b64_tr_b16 a[222:223], v238 offset:6144\n"
"v_mfma_f32_16x16x32_bf16 v[180:183], a[228:231], v[24:27], 0\n"
"s_waitcnt lgkmcnt(6)\n"
"v_mfma_f32_16x16x32_bf16 v[172:175], a[232:235], v[28:31], v[172:175]\n"
"ds_read_b64_tr_b16 a[224:225], v238 offset:8192\n"
"v_mfma_f32_16x16x32_bf16 v[176:179], a[232:235], v[32:35], v[176:179]\n"
"ds_read_b64_tr_b16 a[226:227], v238 offset:10240\n"
"v_mfma_f32_16x16x32_bf16 v[180:183], a[232:235], v[36:39], v[180:183]\n"
"buffer_load_dwordx4 %[dX], %[rWo], s68 offen offset:2048 lds\n"
"s_add_u32 m0, s61, 3072\n"
"s_add_u32 s68, s68, 12288\n"
"s_waitcnt lgkmcnt(6)\n"
"v_mfma_f32_16x16x32_bf16 v[172:175], a[236:239], v[40:43], v[172:175]\n"
"ds_read_b64_tr_b16 a[228:229], v239 offset:0\n"
"v_mfma_f32_16x16x32_bf16 v[176:179], a[236:239], v[44:47], v[176:179]\n"
"ds_read_b64_tr_b16 a[230:231], v239 offset:2048\n"
"v_mfma_f32_16x16x32_bf16 v[180:183], a[236:239], v[48:51], v[180:183]\n"
"s_waitcnt lgkmcnt(6)\n"
"v_mfma_f32_16x16x32_bf16 v[184:187], a[216:219], v[16:19], 0\n"
"ds_read_b64_tr_b16 a[232:233], v239 offset:4096\n"
"v_mfma_f32_16x16x32_bf16 v[188:191], a[216:219], v[20:23], 0\n"
"ds_read_b64_tr_b16 a[234:235], v239 offset:6144\n"
"v_mfma_f32_16x16x32_bf16 v[192:195], a[216:219], v[24:27], 0\n"
"buffer_load_dwordx4 %[dX], %[rWo], s68 offen offset:0 lds\n"
"s_add_u32 s68, s68, 9216\n"
"s_waitcnt lgkmcnt(6)\n"
"v_mfma_f32_16x16x32_bf16 v[184:187], a[220:223], v[28:31], v[184:187]\n"
"ds_read_b64_tr_b16 a[236:237], v239 offset:8192\n"
"v_mfma_f32_16x16x32_bf16 v[188:191], a[220:223], v[32:35], v[188:191]\n"
"ds_read_b64_tr_b16 a[238:239], v239 offset:10240\n"
"v_mfma_f32_16x16x32_bf16 v[192:195], a[220:223], v[36:39], v[192:195]\n"
"buffer_load_dwordx4 %[dX], %[rWo], s68 offen offset:1024 lds\n"
"s_add_u32 s68, s68, 9216\n"
"s_waitcnt lgkmcnt(6)\n"
"v_mfma_f32_16x16x32_bf16 v[184:187], a[224:227], v[40:43], v[184:187]\n"
"s_add_u32 s61, %[m0b], s60\n"
"s_add_u32 s60, s60, 12288\n"
"s_cmp_eq_u32 s60, 61440\n"
"s_cselect_b32 s60, 0, s60\n"
"v_add_u32 v236, s60, %[rdO]\n"
"v_mfma_f32_16x16x32_bf16 v[188:191], a[224:227], v[44:47], v[188:191]\n"
"v_xor_b32 v237, 32, v236\n"
"v_xor_b32 v238, 64, v236\n"
"v_xor_b32 v239, 96, v236\n"
"v_mfma_f32_16x16x32_bf16 v[192:195], a[224:227], v[48:51], v[192:195]\n"
"s_waitcnt vmcnt(20) lgkmcnt(0)\n"
"s_barrier\n"
"ds_read_b64_tr_b16 a[216:217], v236 offset:0\n"
"ds_read_b64_tr_b16 a[218:219], v236 offset:2048\n"
"v_mfma_f32_16x16x32_bf16 v[196:199], a[228:231], v[16:19], 0\n"
"ds_read_b64_tr_b16 a[220:221], v236 offset:4096\n"
"v_mfma_f32_16x16x32_bf16 v[200:203], a[228:231], v[20:23], 0\n"
"ds_read_b64_tr_b16 a[222:223], v236 offset:6144\n"
"v_mfma_f32_16x16x32_bf16 v[204:207], a[228:231], v[24:27], 0\n"
"v_mfma_f32_16x16x32_bf16 v[196:199], a[232:235], v[28:31], v[196:199]\n"
"ds_read_b64_tr_b16 a[224:225], v236 offset:8192\n"
"v_mfma_f32_16x16x32_bf16 v[200:203], a[232:235], v[32:35], v[200:203]\n"
"ds_read_b64_tr_b16 a[226:227], v236 offset:10240\n"
"v_mfma_f32_16x16x32_bf16 v[204:207], a[232:235], v[36:39], v[204:207]\n"
"buffer_load_dwordx4 %[dX], %[rWo], s68 offen offset:2048 lds\n"
"v_mfma_f32_16x16x32_bf16 v[196:199], a[236:239], v[40:43], v[196:199]\n"
"ds_read_b64_tr_b16 a[228:229], v237 offset:0\n"
"buffer_store_dwordx4 v[220:223], %[stv], %[rWs], s73 offen offset:0 sc1\n"
"v_mfma_f32_16x16x32_bf16 v[200:203], a[236:239], v[44:47], v[200:203]\n"
"ds_read_b64_tr_b16 a[230:231], v237 offset:2048\n"
"buffer_store_dwordx4 v[224:227], %[stv], %[rWs], s73 offen offset:1024 sc1\n"
"buffer_store_dwordx4 v[228:231], %[stv], %[rWs], s73 offen offset:2048 sc1\n"
"v_mfma_f32_16x16x32_bf16 v[204:207], a[236:239], v[48:51], v[204:207]\n"
"s_add_u32 s73, s73, 3072\n"
"s_cmp_eq_u32 s73, s58\n"
"s_cselect_b32 s73, %[st0], s73\n"
"s_waitcnt lgkmcnt(6)\n"
"v_mfma_f32_16x16x32_bf16 v[160:163], a[216:219], v[52:55], v[160:163]\n"
"ds_read_b64_tr_b16 a[232:233], v237 offset:4096\n"
"v_mfma_f32_16x16x32_bf16 v[164:167], a[216:219], v[56:59], v[164:167]\n"
"ds_read_b64_tr_b16 a[234:235], v237 offset:6144\n"
"v_mfma_f32_16x16x32_bf16 v[168:171], a[216:219], v[60:63], v[168:171]\n"
"s_mov_b32 m0, s61\n"
"s_add_u32 s68, %[wo0], s72\n"
"s_add_u32 s68, s68, 1105920\n"
"buffer_load_dwordx4 %[dX], %[rWo], s68 offen offset:0 lds\n"
"s_add_u32 s68, s68, 9216\n"
"s_waitcnt lgkmcnt(6)\n"
"v_mfma_f32_16x16x32_bf16 v[160:163], a[220:223], v[64:67], v[160:163]\n"
"ds_read_b64_tr_b16 a[236:237], v237 offset:8192\n"
"v_mfma_f32_16x16x32_bf16 v[164:167], a[220:223], v[68:71], v[164:167]\n"
"ds_read_b64_tr_b16 a[238:239], v237 offset:10240\n"
"v_mfma_f32_16x16x32_bf16 v[168:171], a[220:223], v[72:75], v[168:171]\n"
"s_waitcnt lgkmcnt(6)\n"
"v_mfma_f32_16x16x32_bf16 v[160:163], a[224:227], v[76:79], v[160:163]\n"
"ds_read_b64_tr_b16 a[216:217], v238 offset:0\n"
"v_mfma_f32_16x16x32_bf16 v[164:167], a[224:227], v[80:83], v[164:167]\n"
"ds_read_b64_tr_b16 a[218:219], v238 offset:2048\n"
"v_mfma_f32_16x16x32_bf16 v[168:171], a[224:227], v[84:87], v[168:171]\n"
"buffer_load_dwordx4 %[dX], %[rWo], s68 offen offset:1024 lds\n"
"s_add_u32 s68, s68, 9216\n"
"s_waitcnt lgkmcnt(6)\n"
"v_mfma_f32_16x16x32_bf16 v[172:175], a[228:231], v[52:55], v[172:175]\n"
"ds_read_b64_tr_b16 a[220:221], v238 offset:4096\n"
"v_mfma_f32_16x16x32_bf16 v[176:179], a[228:231], v[56:59], v[176:179]\n"
"ds_read_b64_tr_b16 a[222:223], v238 offset:6144\n"
"v_mfma_f32_16x16x32_bf16 v[180:183], a[228:231], v[60:63], v[180:183]\n"
"s_waitcnt lgkmcnt(6)\n"
"v_mfma_f32_16x16x32_bf16 v[172:175], a[232:235], v[64:67], v[172:175]\n"
"ds_read_b64_tr_b16 a[224:225], v238 offset:8192\n"
"v_mfma_f32_16x16x32_bf16 v[176:179], a[232:235], v[68:71], v[176:179]\n"
"ds_read_b64_tr_b16 a[226:227], v238 offset:10240\n"
"v_mfma_f32_16x16x32_bf16 v[180:183], a[232:235], v[72:75], v[180:183]\n"
"buffer_load_dwordx4 %[dX], %[rWo], s68 offen offset:2048 lds\n"
"s_add_u32 m0, s61, 3072\n"
"s_add_u32 s68, s68, 12288\n"
"s_waitcnt lgkmcnt(6)\n"
"v_mfma_f32_16x16x32_bf16 v[172:175], a[236:239], v[76:79], v[172:175]\n"
"ds_read_b64_tr_b16 a[228:229], v239 offset:0\n"
"v_mfma_f32_16x16x32_bf16 v[176:179], a[236:239], v[80:83], v[176:179]\n"
"ds_read_b64_tr_b16 a[230:231], v239 offset:2048\n"
"v_mfma_f32_16x16x32_bf16 v[180:183], a[236:239], v[84:87], v[180:183]\n"
"s_waitcnt lgkmcnt(6)\n"
"v_mfma_f32_16x16x32_bf16 v[184:187], a[216:219], v[52:55], v[184:187]\n"
"ds_read_b64_tr_b16 a[232:233], v239 offset:4096\n"
"v_mfma_f32_16x16x32_bf16 v[188:191], a[216:219], v[56:59], v[188:191]\n"
"ds_read_b64_tr_b16 a[234:235], v239 offset:6144\n"
"v_mfma_f32_16x16x32_bf16 v[192:195], a[216:219], v[60:63], v[192:195]\n"
"buffer_load_dwordx4 %[dX], %[rWo], s68 offen offset:0 lds\n"
"s_add_u32 s68, s68, 9216\n"
"s_waitcnt lgkmcnt(6)\n"
"v_mfma_f32_16x16x32_bf16 v[184:187], a[220:223], v[64:67], v[184:187]\n"
"ds_read_b64_tr_b16 a[236:237], v239 offset:8192\n"
"v_mfma_f32_16x16x32_bf16 v[188:191], a[220:223], v[68:71], v[188:191]\n"
"ds_read_b64_tr_b16 a[238:239], v239 offset:10240\n"
"v_mfma_f32_16x16x32_bf16 v[192:195], a[220:223], v[72:75], v[192:195]\n"
"buffer_load_dwordx4 %[dX], %[rWo], s68 offen offset:1024 lds\n"
"s_add_u32 s68, s68, 9216\n"
"s_waitcnt lgkmcnt(6)\n"
"v_mfma_f32_16x16x32_bf16 v[184:187], a[224:227], v[76:79], v[184:187]\n"
"s_add_u32 s61, %[m0b], s60\n"
"s_add_u32 s60, s60, 12288\n"
"s_cmp_eq_u32 s60, 61440\n"
"s_cselect_b32 s60, 0, s60\n"
"v_add_u32 v236, s60, %[rdO]\n"
"v_mfma_f32_16x16x32_bf16 v[188:191], a[224:227], v[80:83], v[188:191]\n"
"v_xor_b32 v237, 32, v236\n"
"v_xor_b32 v238, 64, v236\n"
"v_xor_b32 v239, 96, v236\n"
"v_mfma_f32_16x16x32_bf16 v[192:195], a[224:227], v[84:87], v[192:195]\n"
"s_waitcnt vmcnt(23) lgkmcnt(0)\n"
"s_barrier\n"
"ds_read_b64_tr_b16 a[216:217], v236 offset:0\n"
"ds_read_b64_tr_b16 a[218:219], v236 offset:2048\n"
"v_mfma_f32_16x16x32_bf16 v[196:199], a[228:231], v[52:55], v[196:199]\n"
"ds_read_b64_tr_b16 a[220:221], v236 offset:4096\n"
"v_mfma_f32_16x16x32_bf16 v[200:203], a[228:231], v[56:59], v[200:203]\n"
"ds_read_b64_tr_b16 a[222:223], v236 offset:6144\n"
"v_mfma_f32_16x16x32_bf16 v[204:207], a[228:231], v[60:63], v[204:207]\n"
"v_mfma_f32_16x16x32_bf16 v[196:199], a[232:235], v[64:67], v[196:199]\n"
"ds_read_b64_tr_b16 a[224:225], v236 offset:8192\n"
"v_mfma_f32_16x16x32_bf16 v[200:203], a[232:235], v[68:71], v[200:203]\n"
"ds_read_b64_tr_b16 a[226:227], v236 offset:10240\n"
"v_mfma_f32_16x16x32_bf16 v[204:207], a[232:235], v[72:75], v[204:207]\n"
"buffer_load_dwordx4 %[dX], %[rWo], s68 offen offset:2048 lds\n"
"v_mfma_f32_16x16x32_bf16 v[196:199], a[236:239], v[76:79], v[196:199]\n"
"ds_read_b64_tr_b16 a[228:229], v237 offset:0\n"
"v_mfma_f32_16x16x32_bf16 v[200:203], a[236:239], v[80:83], v[200:203]\n"
"ds_read_b64_tr_b16 a[230:231], v237 offset:2048\n"
"v_mfma_f32_16x16x32_bf16 v[204:207], a[236:239], v[84:87], v[204:207]\n"
"s_waitcnt lgkmcnt(6)\n"
"v_mfma_f32_16x16x32_bf16 v[160:163], a[216:219], v[88:91], v[160:163]\n"
"ds_read_b64_tr_b16 a[232:233], v237 offset:4096\n"
"v_mfma_f32_16x16x32_bf16 v[164:167], a[216:219], v[92:95], v[164:167]\n"
"ds_read_b64_tr_b16 a[234:235], v237 offset:6144\n"
"v_mfma_f32_16x16x32_bf16 v[168:171], a[216:219], v[96:99], v[168:171]\n"
"s_mov_b32 m0, s61\n"
"s_add_u32 s68, %[wo1], s72\n"
"s_add_u32 s68, s68, 983040\n"
"buffer_load_dwordx4 %[dX], %[rWo], s68 offen offset:0 lds\n"
"s_add_u32 s68, s68, 9216\n"
"s_waitcnt lgkmcnt(6)\n"
"v_mfma_f32_16x16x32_bf16 v[160:163], a[220:223], v[100:103], v[160:163]\n"
"ds_read_b64_tr_b16 a[236:237], v237 offset:8192\n"
"v_mfma_f32_16x16x32_bf16 v[164:167], a[220:223], v[104:107], v[164:167]\n"
"ds_read_b64_tr_b16 a[238:239], v237 offset:10240\n"
"v_mfma_f32_16x16x32_bf16 v[168:171], a[220:223], v[108:111], v[168:171]\n"
"s_waitcnt lgkmcnt(6)\n"
"v_mfma_f32_16x16x32_bf16 v[160:163], a[224:227], v[112:115], v[160:163]\n"
"ds_read_b64_tr_b16 a[216:217], v238 offset:0\n"
"v_mfma_f32_16x16x32_bf16 v[164:167], a[224:227], v[116:119], v[164:167]\n"
"ds_read_b64_tr_b16 a[218:219], v238 offset:2048\n"
"v_mfma_f32_16x16x32_bf16 v[168:171], a[224:227], v[120:123], v[168:171]\n"
"buffer_load_dwordx4 %[dX], %[rWo], s68 offen offset:1024 lds\n"
"s_add_u32 s68, s68, 9216\n"
"s_waitcnt lgkmcnt(6)\n"
"v_mfma_f32_16x16x32_bf16 v[172:175], a[228:231], v[88:91], v[172:175]\n"
"ds_read_b64_tr_b16 a[220:221], v238 offset:4096\n"
"v_mfma_f32_16x16x32_bf16 v[176:179], a[228:231], v[92:95], v[176:179]\n"
"ds_read_b64_tr_b16 a[222:223], v238 offset:6144\n"
"v_mfma_f32_16x16x32_bf16 v[180:183], a[228:231], v[96:99], v[180:183]\n"
"s_waitcnt lgkmcnt(6)\n"
"v_mfma_f32_16x16x32_bf16 v[172:175], a[232:235], v[100:103], v[172:175]\n"
"ds_read_b64_tr_b16 a[224:225], v238 offset:8192\n"
"v_mfma_f32_16x16x32_bf16 v[176:179], a[232:235], v[104:107], v[176:179]\n"
"ds_read_b64_tr_b16 a[226:227], v238 offset:10240\n"
"v_mfma_f32_16x16x32_bf16 v[180:183], a[232:235], v[108:111], v[180:183]\n"
"buffer_load_dwordx4 %[dX], %[rWo], s68 offen offset:2048 lds\n"
"s_add_u32 m0, s61, 3072\n"
"s_add_u32 s68, s68, 12288\n"
"s_waitcnt lgkmcnt(6)\n"
"v_mfma_f32_16x16x32_bf16 v[172:175], a[236:239], v[112:115], v[172:175]\n"
"ds_read_b64_tr_b16 a[228:229], v239 offset:0\n"
"v_mfma_f32_16x16x32_bf16 v[176:179], a[236:239], v[116:119], v[176:179]\n"
"ds_read_b64_tr_b16 a[230:231], v239 offset:2048\n"
"v_mfma_f32_16x16x32_bf16 v[180:183], a[236:239], v[120:123], v[180:183]\n"
"s_waitcnt lgkmcnt(6)\n"
"v_mfma_f32_16x16x32_bf16 v[184:187], a[216:219], v[88:91], v[184:187]\n"
"ds_read_b64_tr_b16 a[232:233], v239 offset:4096\n"
"v_mfma_f32_16x16x32_bf16 v[188:191], a[216:219], v[92:95], v[188:191]\n"
"ds_read_b64_tr_b16 a[234:235], v239 offset:6144\n"
"v_mfma_f32_16x16x32_bf16 v[192:195], a[216:219], v[96:99], v[192:195]\n"
"buffer_load_dwordx4 %[dX], %[rWo], s68 offen offset:0 lds\n"
"s_add_u32 s68, s68, 9216\n"
"s_waitcnt lgkmcnt(6)\n"
"v_mfma_f32_16x16x32_bf16 v[184:187], a[220:223], v[100:103], v[184:187]\n"
"ds_read_b64_tr_b16 a[236:237], v239 offset:8192\n"
"v_mfma_f32_16x16x32_bf16 v[188:191], a[220:223], v[104:107], v[188:191]\n"
"ds_read_b64_tr_b16 a[238:239], v239 offset:10240\n"
"v_mfma_f32_16x16x32_bf16 v[192:195], a[220:223], v[108:111], v[192:195]\n"
"buffer_load_dwordx4 %[dX], %[rWo], s68 offen offset:1024 lds\n"
"s_add_u32 s68, s68, 9216\n"
"s_waitcnt lgkmcnt(6)\n"
"v_mfma_f32_16x16x32_bf16 v[184:187], a[224:227], v[112:115], v[184:187]\n"
"s_add_u32 s61, %[m0b], s60\n"
"s_add_u32 s60, s60, 12288\n"
"s_cmp_eq_u32 s60, 61440\n"
"s_cselect_b32 s60, 0, s60\n"
"v_add_u32 v236, s60, %[rdO]\n"
"v_mfma_f32_16x16x32_bf16 v[188:191], a[224:227], v[116:119], v[188:191]\n"
"v_xor_b32 v237, 32, v236\n"
"v_xor_b32 v238, 64, v236\n"
"v_xor_b32 v239, 96, v236\n"
"v_mfma_f32_16x16x32_bf16 v[192:195], a[224:227], v[120:123], v[192:195]\n"
"s_waitcnt vmcnt(23) lgkmcnt(0)\n"
"s_barrier\n"
"ds_read_b64_tr_b16 a[216:217], v236 offset:0\n"
"ds_read_b64_tr_b16 a[218:219], v236 offset:2048\n"
"v_mfma_f32_16x16x32_bf16 v[196:199], a[228:231], v[88:91], v[196:199]\n"
"ds_read_b64_tr_b16 a[220:221], v236 offset:4096\n"
"v_mfma_f32_16x16x32_bf16 v[200:203], a[228:231], v[92:95], v[200:203]\n"
"ds_read_b64_tr_b16 a[222:223], v236 offset:6144\n"
"v_mfma_f32_16x16x32_bf16 v[204:207], a[228:231], v[96:99], v[204:207]\n"
"v_mfma_f32_16x16x32_bf16 v[196:199], a[232:235], v[100:103], v[196:199]\n"
"ds_read_b64_tr_b16 a[224:225], v236 offset:8192\n"
"v_mfma_f32_16x16x32_bf16 v[200:203], a[232:235], v[104:107], v[200:203]\n"
"ds_read_b64_tr_b16 a[226:227], v236 offset:10240\n"
"v_mfma_f32_16x16x32_bf16 v[204:207], a[232:235], v[108:111], v[204:207]\n"
"buffer_load_dwordx4 %[dX], %[rWo], s68 offen offset:2048 lds\n"
"v_mfma_f32_16x16x32_bf16 v[196:199], a[236:239], v[112:115], v[196:199]\n"
"ds_read_b64_tr_b16 a[228:229], v237 offset:0\n"
"v_mfma_f32_16x16x32_bf16 v[200:203], a[236:239], v[116:119], v[200:203]\n"
"ds_read_b64_tr_b16 a[230:231], v237 offset:2048\n"
"v_mfma_f32_16x16x32_bf16 v[204:207], a[236:239], v[120:123], v[204:207]\n"
"s_waitcnt lgkmcnt(6)\n"
"v_mfma_f32_16x16x32_bf16 v[160:163], a[216:219], v[124:127], v[160:163]\n"
"ds_read_b64_tr_b16 a[232:233], v237 offset:4096\n"
"v_mfma_f32_16x16x32_bf16 v[164:167], a[216:219], v[128:131], v[164:167]\n"
"ds_read_b64_tr_b16 a[234:235], v237 offset:6144\n"
"v_mfma_f32_16x16x32_bf16 v[168:171], a[216:219], v[132:135], v[168:171]\n"
"s_mov_b32 m0, s61\n"
"s_add_u32 s68, %[wo1], s72\n"
"s_add_u32 s68, s68, 1105920\n"
"buffer_load_dwordx4 %[dX], %[rWo], s68 offen offset:0 lds\n"
"s_add_u32 s68, s68, 9216\n"
"s_waitcnt lgkmcnt(6)\n"
"v_mfma_f32_16x16x32_bf16 v[160:163], a[220:223], v[136:139], v[160:163]\n"
"ds_read_b64_tr_b16 a[236:237], v237 offset:8192\n"
"v_mfma_f32_16x16x32_bf16 v[164:167], a[220:223], v[140:143], v[164:167]\n"
"ds_read_b64_tr_b16 a[238:239], v237 offset:10240\n"
"v_mfma_f32_16x16x32_bf16 v[168:171], a[220:223], v[144:147], v[168:171]\n"
"s_waitcnt lgkmcnt(6)\n"
"v_mfma_f32_16x16x32_bf16 v[160:163], a[224:227], v[148:151], v[160:163]\n"
"ds_read_b64_tr_b16 a[216:217], v238 offset:0\n"
"v_mfma_f32_16x16x32_bf16 v[164:167], a[224:227], v[152:155], v[164:167]\n"
"ds_read_b64_tr_b16 a[218:219], v238 offset:2048\n"
"v_mfma_f32_16x16x32_bf16 v[168:171], a[224:227], v[156:159], v[168:171]\n"
"buffer_load_dwordx4 %[dX], %[rWo], s68 offen offset:1024 lds\n"
"s_add_u32 s68, s68, 9216\n"
"s_waitcnt lgkmcnt(6)\n"
"v_mfma_f32_16x16x32_bf16 v[172:175], a[228:231], v[124:127], v[172:175]\n"
"ds_read_b64_tr_b16 a[220:221], v238 offset:4096\n"
"v_mfma_f32_16x16x32_bf16 v[176:179], a[228:231], v[128:131], v[176:179]\n"
"ds_read_b64_tr_b16 a[222:223], v238 offset:6144\n"
"v_mfma_f32_16x16x32_bf16 v[180:183], a[228:231], v[132:135], v[180:183]\n"
"s_waitcnt lgkmcnt(6)\n"
"v_mfma_f32_16x16x32_bf16 v[172:175], a[232:235], v[136:139], v[172:175]\n"
"ds_read_b64_tr_b16 a[224:225], v238 offset:8192\n"
"v_mfma_f32_16x16x32_bf16 v[176:179], a[232:235], v[140:143], v[176:179]\n"
"ds_read_b64_tr_b16 a[226:227], v238 offset:10240\n"
"v_mfma_f32_16x16x32_bf16 v[180:183], a[232:235], v[144:147], v[180:183]\n"
"buffer_load_dwordx4 %[dX], %[rWo], s68 offen offset:2048 lds\n"
"s_add_u32 m0, s61, 3072\n"
"s_add_u32 s68, s68, 12288\n"
"s_waitcnt lgkmcnt(6)\n"
"v_mfma_f32_16x16x32_bf16 v[172:175], a[236:239], v[148:151], v[172:175]\n"
"ds_read_b64_tr_b16 a[228:229], v239 offset:0\n"
"v_mfma_f32_16x16x32_bf16 v[176:179], a[236:239], v[152:155], v[176:179]\n"
"ds_read_b64_tr_b16 a[230:231], v239 offset:2048\n"
"v_mfma_f32_16x16x32_bf16 v[180:183], a[236:239], v[156:159], v[180:183]\n"
"s_waitcnt lgkmcnt(6)\n"
"v_mfma_f32_16x16x32_bf16 v[184:187], a[216:219], v[124:127], v[184:187]\n"
"ds_read_b64_tr_b16 a[232:233], v239 offset:4096\n"
"v_mfma_f32_16x16x32_bf16 v[188:191], a[216:219], v[128:131], v[188:191]\n"
"ds_read_b64_tr_b16 a[234:235], v239 offset:6144\n"
"v_mfma_f32_16x16x32_bf16 v[192:195], a[216:219], v[132:135], v[192:195]\n"
"buffer_load_dwordx4 %[dX], %[rWo], s68 offen offset:0 lds\n"
"s_add_u32 s68, s68, 9216\n"
"s_waitcnt lgkmcnt(6)\n"
"v_mfma_f32_16x16x32_bf16 v[184:187], a[220:223], v[136:139], v[184:187]\n"
"ds_read_b64_tr_b16 a[236:237], v239 offset:8192\n"
"v_mfma_f32_16x16x32_bf16 v[188:191], a[220:223], v[140:143], v[188:191]\n"
"ds_read_b64_tr_b16 a[238:239], v239 offset:10240\n"
"v_mfma_f32_16x16x32_bf16 v[192:195], a[220:223], v[144:147], v[192:195]\n"
"buffer_load_dwordx4 %[dX], %[rWo], s68 offen offset:1024 lds\n"
"s_add_u32 s68, s68, 9216\n"
"s_waitcnt lgkmcnt(6)\n"
"v_mfma_f32_16x16x32_bf16 v[184:187], a[224:227], v[148:151], v[184:187]\n"
"s_add_u32 s61, %[m0b], s60\n"
"s_add_u32 s60, s60, 12288\n"
"s_cmp_eq_u32 s60, 61440\n"
"s_cselect_b32 s60, 0, s60\n"
"v_add_u32 v236, s60, %[rdO]\n"
"v_mfma_f32_16x16x32_bf16 v[188:191], a[224:227], v[152:155], v[188:191]\n"
"v_xor_b32 v237, 32, v236\n"
"v_xor_b32 v238, 64, v236\n"
"v_xor_b32 v239, 96, v236\n"
"v_mfma_f32_16x16x32_bf16 v[192:195], a[224:227], v[156:159], v[192:195]\n"
"s_waitcnt vmcnt(20) lgkmcnt(0)\n"
"s_barrier\n"
"ds_read_b64_tr_b16 a[216:217], v236 offset:0\n"
"ds_read_b64_tr_b16 a[218:219], v236 offset:2048\n"
"v_mfma_f32_16x16x32_bf16 v[196:199], a[228:231], v[124:127], v[196:199]\n"
"ds_read_b64_tr_b16 a[220:221], v236 offset:4096\n"
"v_mfma_f32_16x16x32_bf16 v[200:203], a[228:231], v[128:131], v[200:203]\n"
"ds_read_b64_tr_b16 a[222:223], v236 offset:6144\n"
"v_mfma_f32_16x16x32_bf16 v[204:207], a[228:231], v[132:135], v[204:207]\n"
"v_mfma_f32_16x16x32_bf16 v[196:199], a[232:235], v[136:139], v[196:199]\n"
"ds_read_b64_tr_b16 a[224:225], v236 offset:8192\n"
"v_mfma_f32_16x16x32_bf16 v[200:203], a[232:235], v[140:143], v[200:203]\n"
"ds_read_b64_tr_b16 a[226:227], v236 offset:10240\n"
"v_mfma_f32_16x16x32_bf16 v[204:207], a[232:235], v[144:147], v[204:207]\n"
"buffer_load_dwordx4 %[dX], %[rWo], s68 offen offset:2048 lds\n"
"s_add_u32 s72, s72, 128\n"
"s_cmp_eq_u32 s72, 640\n"
"s_cselect_b32 s72, 0, s72\n"
"v_mfma_f32_16x16x32_bf16 v[196:199], a[236:239], v[148:151], v[196:199]\n"
"ds_read_b64_tr_b16 a[228:229], v237 offset:0\n"
"v_mfma_f32_16x16x32_bf16 v[200:203], a[236:239], v[152:155], v[200:203]\n"
"ds_read_b64_tr_b16 a[230:231], v237 offset:2048\n"
"v_mfma_f32_16x16x32_bf16 v[204:207], a[236:239], v[156:159], v[204:207]\n"
"s_waitcnt lgkmcnt(6)\n"
"v_mfma_f32_16x16x32_bf16 v[160:163], a[216:219], a[144:147], v[160:163]\n"
"ds_read_b64_tr_b16 a[232:233], v237 offset:4096\n"
"v_mfma_f32_16x16x32_bf16 v[164:167], a[216:219], a[148:151], v[164:167]\n"
"ds_read_b64_tr_b16 a[234:235], v237 offset:6144\n"
"v_mfma_f32_16x16x32_bf16 v[168:171], a[216:219], a[152:155], v[168:171]\n"
"s_mov_b32 m0, s61\n"
"s_add_u32 s68, %[wo0], s72\n"
"buffer_load_dwordx4 %[dX], %[rWo], s68 offen offset:0 lds\n"
"s_add_u32 s68, s68, 9216\n"
"s_waitcnt lgkmcnt(6)\n"
"v_mfma_f32_16x16x32_bf16 v[160:163], a[220:223], a[156:159], v[160:163]\n"
"ds_read_b64_tr_b16 a[236:237], v237 offset:8192\n"
"v_mfma_f32_16x16x32_bf16 v[164:167], a[220:223], a[160:163], v[164:167]\n"
"ds_read_b64_tr_b16 a[238:239], v237 offset:10240\n"
"v_mfma_f32_16x16x32_bf16 v[168:171], a[220:223], a[164:167], v[168:171]\n"
"s_waitcnt lgkmcnt(6)\n"
"v_mfma_f32_16x16x32_bf16 v[160:163], a[224:227], a[168:171], v[160:163]\n"
"ds_read_b64_tr_b16 a[216:217], v238 offset:0\n"
"v_mfma_f32_16x16x32_bf16 v[164:167], a[224:227], a[172:175], v[164:167]\n"
"ds_read_b64_tr_b16 a[218:219], v238 offset:2048\n"
"v_mfma_f32_16x16x32_bf16 v[168:171], a[224:227], a[176:179], v[168:171]\n"
"buffer_load_dwordx4 %[dX], %[rWo], s68 offen offset:1024 lds\n"
"s_add_u32 s68, s68, 9216\n"
"s_waitcnt lgkmcnt(6)\n"
"v_mfma_f32_16x16x32_bf16 v[172:175], a[228:231], a[144:147], v[172:175]\n"
"ds_read_b64_tr_b16 a[220:221], v238 offset:4096\n"
"v_mfma_f32_16x16x32_bf16 v[176:179], a[228:231], a[148:151], v[176:179]\n"
"ds_read_b64_tr_b16 a[222:223], v238 offset:6144\n"
"v_mfma_f32_16x16x32_bf16 v[180:183], a[228:231], a[152:155], v[180:183]\n"
"s_waitcnt lgkmcnt(6)\n"
"v_mfma_f32_16x16x32_bf16 v[172:175], a[232:235], a[156:159], v[172:175]\n"
"ds_read_b64_tr_b16 a[224:225], v238 offset:8192\n"
"v_mfma_f32_16x16x32_bf16 v[176:179], a[232:235], a[160:163], v[176:179]\n"
"ds_read_b64_tr_b16 a[226:227], v238 offset:10240\n"
"v_mfma_f32_16x16x32_bf16 v[180:183], a[232:235], a[164:167], v[180:183]\n"
"buffer_load_dwordx4 %[dX], %[rWo], s68 offen offset:2048 lds\n"
"s_add_u32 m0, s61, 3072\n"
"s_add_u32 s68, s68, 12288\n"
"s_waitcnt lgkmcnt(6)\n"
"v_mfma_f32_16x16x32_bf16 v[172:175], a[236:239], a[168:171], v[172:175]\n"
"ds_read_b64_tr_b16 a[228:229], v239 offset:0\n"
"v_mfma_f32_16x16x32_bf16 v[176:179], a[236:239], a[172:175], v[176:179]\n"
"ds_read_b64_tr_b16 a[230:231], v239 offset:2048\n"
"v_mfma_f32_16x16x32_bf16 v[180:183], a[236:239], a[176:179], v[180:183]\n"
"s_waitcnt lgkmcnt(6)\n"
"v_mfma_f32_16x16x32_bf16 v[184:187], a[216:219], a[144:147], v[184:187]\n"
"ds_read_b64_tr_b16 a[232:233], v239 offset:4096\n"
"v_mfma_f32_16x16x32_bf16 v[188:191], a[216:219], a[148:151], v[188:191]\n"
"ds_read_b64_tr_b16 a[234:235], v239 offset:6144\n"
"v_mfma_f32_16x16x32_bf16 v[192:195], a[216:219], a[152:155], v[192:195]\n"
"buffer_load_dwordx4 %[dX], %[rWo], s68 offen offset:0 lds\n"
"s_add_u32 s68, s68, 9216\n"
"s_waitcnt lgkmcnt(6)\n"
"v_mfma_f32_16x16x32_bf16 v[184:187], a[220:223], a[156:159], v[184:187]\n"
"ds_read_b64_tr_b16 a[236:237], v239 offset:8192\n"
"v_mfma_f32_16x16x32_bf16 v[188:191], a[220:223], a[160:163], v[188:191]\n"
"ds_read_b64_tr_b16 a[238:239], v239 offset:10240\n"
"v_mfma_f32_16x16x32_bf16 v[192:195], a[220:223], a[164:167], v[192:195]\n"
"buffer_load_dwordx4 %[dX], %[rWo], s68 offen offset:1024 lds\n"
"s_add_u32 s68, s68, 9216\n"
"s_waitcnt lgkmcnt(6)\n"
"v_mfma_f32_16x16x32_bf16 v[184:187], a[224:227], a[168:171], v[184:187]\n"
"s_add_u32 s61, %[m0b], s60\n"
"s_add_u32 s60, s60, 12288\n"
"s_cmp_eq_u32 s60, 61440\n"
"s_cselect_b32 s60, 0, s60\n"
"v_add_u32 v236, s60, %[rdO]\n"
"v_mfma_f32_16x16x32_bf16 v[188:191], a[224:227], a[172:175], v[188:191]\n"
"v_xor_b32 v237, 32, v236\n"
"v_xor_b32 v238, 64, v236\n"
"v_xor_b32 v239, 96, v236\n"
"v_mfma_f32_16x16x32_bf16 v[192:195], a[224:227], a[176:179], v[192:195]\n"
"s_waitcnt vmcnt(17) lgkmcnt(0)\n"
"s_barrier\n"
"ds_read_b64_tr_b16 a[216:217], v236 offset:0\n"
"ds_read_b64_tr_b16 a[218:219], v236 offset:2048\n"
"v_mfma_f32_16x16x32_bf16 v[196:199], a[228:231], a[144:147], v[196:199]\n"
"ds_read_b64_tr_b16 a[220:221], v236 offset:4096\n"
"v_mfma_f32_16x16x32_bf16 v[200:203], a[228:231], a[148:151], v[200:203]\n"
"ds_read_b64_tr_b16 a[222:223], v236 offset:6144\n"
"v_lshrrev_b32 v254, 2, %[stv]\n"
"v_mfma_f32_16x16x32_bf16 v[204:207], a[228:231], a[152:155], v[204:207]\n"
"v_add_u32 v255, 1, %[y]\n"
"v_mfma_f32_16x16x32_bf16 v[196:199], a[232:235], a[156:159], v[196:199]\n"
"ds_read_b64_tr_b16 a[224:225], v236 offset:8192\n"
"s_lshl_b32 exec_lo, %[fl1], 8\n"
"s_mov_b32 exec_hi, 0\n"
"buffer_store_dword v255, v254, %[rWs], %[flg] offen sc1\n"
"s_mov_b64 exec, -1\n"
"v_mfma_f32_16x16x32_bf16 v[200:203], a[232:235], a[160:163], v[200:203]\n"
"ds_read_b64_tr_b16 a[226:227], v236 offset:10240\n"
"v_mfma_f32_16x16x32_bf16 v[204:207], a[232:235], a[164:167], v[204:207]\n"
"buffer_load_dwordx4 %[dX], %[rWo], s68 offen offset:2048 lds\n"
"v_mfma_f32_16x16x32_bf16 v[196:199], a[236:239], a[168:171], v[196:199]\n"
"ds_read_b64_tr_b16 a[228:229], v237 offset:0\n"
"v_mfma_f32_16x16x32_bf16 v[200:203], a[236:239], a[172:175], v[200:203]\n"
"ds_read_b64_tr_b16 a[230:231], v237 offset:2048\n"
"v_mfma_f32_16x16x32_bf16 v[204:207], a[236:239], a[176:179], v[204:207]\n"
"s_waitcnt lgkmcnt(6)\n"
"v_mfma_f32_16x16x32_bf16 v[160:163], a[216:219], a[180:183], v[160:163]\n"
"ds_read_b64_tr_b16 a[232:233], v237 offset:4096\n"
"v_mfma_f32_16x16x32_bf16 v[164:167], a[216:219], a[184:187], v[164:167]\n"
"ds_read_b64_tr_b16 a[234:235], v237 offset:6144\n"
"v_mfma_f32_16x16x32_bf16 v[168:171], a[216:219], a[188:191], v[168:171]\n"
"s_mov_b32 m0, s61\n"
"s_add_u32 s68, %[wo0], s72\n"
"s_add_u32 s68, s68, 122880\n"
"buffer_load_dwordx4 %[dX], %[rWo], s68 offen offset:0 lds\n"
"s_add_u32 s68, s68, 9216\n"
"s_waitcnt lgkmcnt(6)\n"
"v_mfma_f32_16x16x32_bf16 v[160:163], a[220:223], a[192:195], v[160:163]\n"
"ds_read_b64_tr_b16 a[236:237], v237 offset:8192\n"
"v_mfma_f32_16x16x32_bf16 v[164:167], a[220:223], a[196:199], v[164:167]\n"
"ds_read_b64_tr_b16 a[238:239], v237 offset:10240\n"
"v_mfma_f32_16x16x32_bf16 v[168:171], a[220:223], a[200:203], v[168:171]\n"
"s_waitcnt lgkmcnt(6)\n"
"v_mfma_f32_16x16x32_bf16 v[160:163], a[224:227], a[204:207], v[160:163]\n"
"ds_read_b64_tr_b16 a[216:217], v238 offset:0\n"
"v_mfma_f32_16x16x32_bf16 v[164:167], a[224:227], a[208:211], v[164:167]\n"
"ds_read_b64_tr_b16 a[218:219], v238 offset:2048\n"
"v_mfma_f32_16x16x32_bf16 v[168:171], a[224:227], a[212:215], v[168:171]\n"
"buffer_load_dwordx4 %[dX], %[rWo], s68 offen offset:1024 lds\n"
"s_add_u32 s68, s68, 9216\n"
"s_waitcnt lgkmcnt(6)\n"
"v_mfma_f32_16x16x32_bf16 v[172:175], a[228:231], a[180:183], v[172:175]\n"
"ds_read_b64_tr_b16 a[220:221], v238 offset:4096\n"
"v_mfma_f32_16x16x32_bf16 v[176:179], a[228:231], a[184:187], v[176:179]\n"
"ds_read_b64_tr_b16 a[222:223], v238 offset:6144\n"
"v_mfma_f32_16x16x32_bf16 v[180:183], a[228:231], a[188:191], v[180:183]\n"
"s_waitcnt lgkmcnt(6)\n"
"v_mfma_f32_16x16x32_bf16 v[172:175], a[232:235], a[192:195], v[172:175]\n"
"ds_read_b64_tr_b16 a[224:225], v238 offset:8192\n"
"v_mfma_f32_16x16x32_bf16 v[176:179], a[232:235], a[196:199], v[176:179]\n"
"ds_read_b64_tr_b16 a[226:227], v238 offset:10240\n"
"v_mfma_f32_16x16x32_bf16 v[180:183], a[232:235], a[200:203], v[180:183]\n"
"buffer_load_dwordx4 %[dX], %[rWo], s68 offen offset:2048 lds\n"
"s_add_u32 m0, s61, 3072\n"
"s_add_u32 s68, s68, 12288\n"
"s_waitcnt lgkmcnt(6)\n"
"v_mfma_f32_16x16x32_bf16 v[172:175], a[236:239], a[204:207], v[172:175]\n"
"ds_read_b64_tr_b16 a[228:229], v239 offset:0\n"
"v_mfma_f32_16x16x32_bf16 v[176:179], a[236:239], a[208:211], v[176:179]\n"
"ds_read_b64_tr_b16 a[230:231], v239 offset:2048\n"
"v_mfma_f32_16x16x32_bf16 v[180:183], a[236:239], a[212:215], v[180:183]\n"
"s_waitcnt lgkmcnt(6)\n"
"v_mfma_f32_16x16x32_bf16 v[184:187], a[216:219], a[180:183], v[184:187]\n"
"ds_read_b64_tr_b16 a[232:233], v239 offset:4096\n"
"v_mfma_f32_16x16x32_bf16 v[188:191], a[216:219], a[184:187], v[188:191]\n"
"ds_read_b64_tr_b16 a[234:235], v239 offset:6144\n"
"v_mfma_f32_16x16x32_bf16 v[192:195], a[216:219], a[188:191], v[192:195]\n"
"buffer_load_dwordx4 %[dX], %[rWo], s68 offen offset:0 lds\n"
"s_add_u32 s68, s68, 9216\n"
"s_waitcnt lgkmcnt(6)\n"
"v_mfma_f32_16x16x32_bf16 v[184:187], a[220:223], a[192:195], v[184:187]\n"
"ds_read_b64_tr_b16 a[236:237], v239 offset:8192\n"
"v_mfma_f32_16x16x32_bf16 v[188:191], a[220:223], a[196:199], v[188:191]\n"
"ds_read_b64_tr_b16 a[238:239], v239 offset:10240\n"
"v_mfma_f32_16x16x32_bf16 v[192:195], a[220:223], a[200:203], v[192:195]\n"
"buffer_load_dwordx4 %[dX], %[rWo], s68 offen offset:1024 lds\n"
"s_add_u32 s68, s68, 9216\n"
"s_waitcnt lgkmcnt(6)\n"
"v_mfma_f32_16x16x32_bf16 v[184:187], a[224:227], a[204:207], v[184:187]\n"
"s_add_u32 s61, %[m0b], s60\n"
"s_add_u32 s60, s60, 12288\n"
"s_cmp_eq_u32 s60, 61440\n"
"s_cselect_b32 s60, 0, s60\n"
"v_add_u32 v236, s60, %[rdO]\n"
"v_mfma_f32_16x16x32_bf16 v[188:191], a[224:227], a[208:211], v[188:191]\n"
"v_xor_b32 v237, 32, v236\n"
"v_xor_b32 v238, 64, v236\n"
"v_xor_b32 v239, 96, v236\n"
"v_mfma_f32_16x16x32_bf16 v[192:195], a[224:227], a[212:215], v[192:195]\n"
"s_waitcnt vmcnt(18) lgkmcnt(0)\n"
"s_barrier\n"
"ds_read_b64_tr_b16 a[216:217], v236 offset:0\n"
"ds_read_b64_tr_b16 a[218:219], v236 offset:2048\n"
"v_mfma_f32_16x16x32_bf16 v[196:199], a[228:231], a[180:183], v[196:199]\n"
"ds_read_b64_tr_b16 a[220:221], v236 offset:4096\n"
"v_mfma_f32_16x16x32_bf16 v[200:203], a[228:231], a[184:187], v[200:203]\n"
"ds_read_b64_tr_b16 a[222:223], v236 offset:6144\n"
"v_mfma_f32_16x16x32_bf16 v[204:207], a[228:231], a[188:191], v[204:207]\n"
"v_mfma_f32_16x16x32_bf16 v[196:199], a[232:235], a[192:195], v[196:199]\n"
"ds_read_b64_tr_b16 a[224:225], v236 offset:8192\n"
"v_mfma_f32_16x16x32_bf16 v[200:203], a[232:235], a[196:199], v[200:203]\n"
"ds_read_b64_tr_b16 a[226:227], v236 offset:10240\n"
"v_mfma_f32_16x16x32_bf16 v[204:207], a[232:235], a[200:203], v[204:207]\n"
"buffer_load_dwordx4 %[dX], %[rWo], s68 offen offset:2048 lds\n"
"v_mfma_f32_16x16x32_bf16 v[196:199], a[236:239], a[204:207], v[196:199]\n"
"ds_read_b64_tr_b16 a[228:229], v237 offset:0\n"
"v_mfma_f32_16x16x32_bf16 v[200:203], a[236:239], a[208:211], v[200:203]\n"
"ds_read_b64_tr_b16 a[230:231], v237 offset:2048\n"
"v_mfma_f32_16x16x32_bf16 v[204:207], a[236:239], a[212:215], v[204:207]\n"
"s_waitcnt lgkmcnt(6)\n"
"v_mfma_f32_16x16x32_bf16 v[160:163], a[216:219], a[0:3], v[160:163]\n"
"ds_read_b64_tr_b16 a[232:233], v237 offset:4096\n"
"v_mfma_f32_16x16x32_bf16 v[164:167], a[216:219], a[4:7], v[164:167]\n"
"ds_read_b64_tr_b16 a[234:235], v237 offset:6144\n"
"v_mfma_f32_16x16x32_bf16 v[168:171], a[216:219], a[8:11], v[168:171]\n"
"s_mov_b32 m0, s61\n"
"s_add_u32 s68, %[wo1], s72\n"
"buffer_load_dwordx4 %[dX], %[rWo], s68 offen offset:0 lds\n"
"s_add_u32 s68, s68, 9216\n"
"s_waitcnt lgkmcnt(6)\n"
"v_mfma_f32_16x16x32_bf16 v[160:163], a[220:223], a[12:15], v[160:163]\n"
"ds_read_b64_tr_b16 a[236:237], v237 offset:8192\n"
"v_mfma_f32_16x16x32_bf16 v[164:167], a[220:223], a[16:19], v[164:167]\n"
"ds_read_b64_tr_b16 a[238:239], v237 offset:10240\n"
"v_mfma_f32_16x16x32_bf16 v[168:171], a[220:223], a[20:23], v[168:171]\n"
"s_waitcnt lgkmcnt(6)\n"
"v_mfma_f32_16x16x32_bf16 v[160:163], a[224:227], a[24:27], v[160:163]\n"
"ds_read_b64_tr_b16 a[216:217], v238 offset:0\n"
"v_mfma_f32_16x16x32_bf16 v[164:167], a[224:227], a[28:31], v[164:167]\n"
"ds_read_b64_tr_b16 a[218:219], v238 offset:2048\n"
"v_mfma_f32_16x16x32_bf16 v[168:171], a[224:227], a[32:35], v[168:171]\n"
"buffer_load_dwordx4 %[dX], %[rWo], s68 offen offset:1024 lds\n"
"s_add_u32 s68, s68, 9216\n"
"s_waitcnt lgkmcnt(6)\n"
"v_mfma_f32_16x16x32_bf16 v[172:175], a[228:231], a[0:3], v[172:175]\n"
"ds_read_b64_tr_b16 a[220:221], v238 offset:4096\n"
"v_mfma_f32_16x16x32_bf16 v[176:179], a[228:231], a[4:7], v[176:179]\n"
"ds_read_b64_tr_b16 a[222:223], v238 offset:6144\n"
"v_mfma_f32_16x16x32_bf16 v[180:183], a[228:231], a[8:11], v[180:183]\n"
"s_waitcnt lgkmcnt(6)\n"
"v_mfma_f32_16x16x32_bf16 v[172:175], a[232:235], a[12:15], v[172:175]\n"
"ds_read_b64_tr_b16 a[224:225], v238 offset:8192\n"
"v_mfma_f32_16x16x32_bf16 v[176:179], a[232:235], a[16:19], v[176:179]\n"
"ds_read_b64_tr_b16 a[226:227], v238 offset:10240\n"
"v_mfma_f32_16x16x32_bf16 v[180:183], a[232:235], a[20:23], v[180:183]\n"
"buffer_load_dwordx4 %[dX], %[rWo], s68 offen offset:2048 lds\n"
"s_add_u32 m0, s61, 3072\n"
"s_add_u32 s68, s68, 12288\n"
"s_waitcnt lgkmcnt(6)\n"
"v_mfma_f32_16x16x32_bf16 v[172:175], a[236:239], a[24:27], v[172:175]\n"
"ds_read_b64_tr_b16 a[228:229], v239 offset:0\n"
"v_mfma_f32_16x16x32_bf16 v[176:179], a[236:239], a[28:31], v[176:179]\n"
"ds_read_b64_tr_b16 a[230:231], v239 offset:2048\n"
"v_mfma_f32_16x16x32_bf16 v[180:183], a[236:239], a[32:35], v[180:183]\n"
"s_waitcnt lgkmcnt(6)\n"
"v_mfma_f32_16x16x32_bf16 v[184:187], a[216:219], a[0:3], v[184:187]\n"
"ds_read_b64_tr_b16 a[232:233], v239 offset:4096\n"
"v_mfma_f32_16x16x32_bf16 v[188:191], a[216:219], a[4:7], v[188:191]\n"
"ds_read_b64_tr_b16 a[234:235], v239 offset:6144\n"
"v_mfma_f32_16x16x32_bf16 v[192:195], a[216:219], a[8:11], v[192:195]\n"
"buffer_load_dwordx4 %[dX], %[rWo], s68 offen offset:0 lds\n"
"s_add_u32 s68, s68, 9216\n"
"s_waitcnt lgkmcnt(6)\n"
"v_mfma_f32_16x16x32_bf16 v[184:187], a[220:223], a[12:15], v[184:187]\n"
"ds_read_b64_tr_b16 a[236:237], v239 offset:8192\n"
"v_mfma_f32_16x16x32_bf16 v[188:191], a[220:223], a[16:19], v[188:191]\n"
"ds_read_b64_tr_b16 a[238:239], v239 offset:10240\n"
"v_mfma_f32_16x16x32_bf16 v[192:195], a[220:223], a[20:23], v[192:195]\n"
"buffer_load_dwordx4 %[dX], %[rWo], s68 offen offset:1024 lds\n"
"s_add_u32 s68, s68, 9216\n"
"s_waitcnt lgkmcnt(6)\n"
"v_mfma_f32_16x16x32_bf16 v[184:187], a[224:227], a[24:27], v[184:187]\n"
"s_add_u32 s61, %[m0b], s60\n"
"s_add_u32 s60, s60, 12288\n"
"s_cmp_eq_u32 s60, 61440\n"
"s_cselect_b32 s60, 0, s60\n"
"v_add_u32 v236, s60, %[rdO]\n"
"v_mfma_f32_16x16x32_bf16 v[188:191], a[224:227], a[28:31], v[188:191]\n"
"v_xor_b32 v237, 32, v236\n"
"v_xor_b32 v238, 64, v236\n"
"v_xor_b32 v239, 96, v236\n"
"v_mfma_f32_16x16x32_bf16 v[192:195], a[224:227], a[32:35], v[192:195]\n"
"s_waitcnt vmcnt(18) lgkmcnt(0)\n"
"s_barrier\n"
"ds_read_b64_tr_b16 a[216:217], v236 offset:0\n"
"ds_read_b64_tr_b16 a[218:219], v236 offset:2048\n"
"v_mfma_f32_16x16x32_bf16 v[196:199], a[228:231], a[0:3], v[196:199]\n"
"ds_read_b64_tr_b16 a[220:221], v236 offset:4096\n"
"v_mfma_f32_16x16x32_bf16 v[200:203], a[228:231], a[4:7], v[200:203]\n"
"ds_read_b64_tr_b16 a[222:223], v236 offset:6144\n"
"v_mfma_f32_16x16x32_bf16 v[204:207], a[228:231], a[8:11], v[204:207]\n"
"v_mfma_f32_16x16x32_bf16 v[196:199], a[232:235], a[12:15], v[196:199]\n"
"ds_read_b64_tr_b16 a[224:225], v236 offset:8192\n"
"v_mfma_f32_16x16x32_bf16 v[200:203], a[232:235], a[16:19], v[200:203]\n"
"ds_read_b64_tr_b16 a[226:227], v236 offset:10240\n"
"v_mfma_f32_16x16x32_bf16 v[204:207], a[232:235], a[20:23], v[204:207]\n"
"buffer_load_dwordx4 %[dX], %[rWo], s68 offen offset:2048 lds\n"
"v_mfma_f32_16x16x32_bf16 v[196:199], a[236:239], a[24:27], v[196:199]\n"
"ds_read_b64_tr_b16 a[228:229], v237 offset:0\n"
"v_mfma_f32_16x16x32_bf16 v[200:203], a[236:239], a[28:31], v[200:203]\n"
"ds_read_b64_tr_b16 a[230:231], v237 offset:2048\n"
"v_mfma_f32_16x16x32_bf16 v[204:207], a[236:239], a[32:35], v[204:207]\n"
"s_waitcnt lgkmcnt(6)\n"
"v_mfma_f32_16x16x32_bf16 v[160:163], a[216:219], a[36:39], v[160:163]\n"
"ds_read_b64_tr_b16 a[232:233], v237 offset:4096\n"
"v_mfma_f32_16x16x32_bf16 v[164:167], a[216:219], a[40:43], v[164:167]\n"
"ds_read_b64_tr_b16 a[234:235], v237 offset:6144\n"
"v_mfma_f32_16x16x32_bf16 v[168:171], a[216:219], a[44:47], v[168:171]\n"
"s_mov_b32 m0, s61\n"
"s_add_u32 s68, %[wo1], s72\n"
"s_add_u32 s68, s68, 122880\n"
"buffer_load_dwordx4 %[dX], %[rWo], s68 offen offset:0 lds\n"
"s_add_u32 s68, s68, 9216\n"
"s_waitcnt lgkmcnt(6)\n"
"v_mfma_f32_16x16x32_bf16 v[160:163], a[220:223], a[48:51], v[160:163]\n"
"ds_read_b64_tr_b16 a[236:237], v237 offset:8192\n"
"v_mfma_f32_16x16x32_bf16 v[164:167], a[220:223], a[52:55], v[164:167]\n"
"ds_read_b64_tr_b16 a[238:239], v237 offset:10240\n"
"v_mfma_f32_16x16x32_bf16 v[168:171], a[220:223], a[56:59], v[168:171]\n"
"s_waitcnt lgkmcnt(6)\n"
"v_mfma_f32_16x16x32_bf16 v[160:163], a[224:227], a[60:63], v[160:163]\n"
"ds_read_b64_tr_b16 a[216:217], v238 offset:0\n"
"v_mfma_f32_16x16x32_bf16 v[164:167], a[224:227], a[64:67], v[164:167]\n"
"ds_read_b64_tr_b16 a[218:219], v238 offset:2048\n"
"v_mfma_f32_16x16x32_bf16 v[168:171], a[224:227], a[68:71], v[168:171]\n"
"buffer_load_dwordx4 %[dX], %[rWo], s68 offen offset:1024 lds\n"
"s_add_u32 s68, s68, 9216\n"
"s_waitcnt lgkmcnt(6)\n"
"v_mfma_f32_16x16x32_bf16 v[172:175], a[228:231], a[36:39], v[172:175]\n"
"ds_read_b64_tr_b16 a[220:221], v238 offset:4096\n"
"v_mfma_f32_16x16x32_bf16 v[176:179], a[228:231], a[40:43], v[176:179]\n"
"ds_read_b64_tr_b16 a[222:223], v238 offset:6144\n"
"v_mfma_f32_16x16x32_bf16 v[180:183], a[228:231], a[44:47], v[180:183]\n"
"s_waitcnt lgkmcnt(6)\n"
"v_mfma_f32_16x16x32_bf16 v[172:175], a[232:235], a[48:51], v[172:175]\n"
"ds_read_b64_tr_b16 a[224:225], v238 offset:8192\n"
"v_cvt_pk_bf16_f32 v208, v160, v161\n"
"v_mfma_f32_16x16x32_bf16 v[176:179], a[232:235], a[52:55], v[176:179]\n"
"ds_read_b64_tr_b16 a[226:227], v238 offset:10240\n"
"v_cvt_pk_bf16_f32 v209, v162, v163\n"
"v_cvt_pk_bf16_f32 v212, v164, v165\n"
"v_mfma_f32_16x16x32_bf16 v[180:183], a[232:235], a[56:59], v[180:183]\n"
"buffer_load_dwordx4 %[dX], %[rWo], s68 offen offset:2048 lds\n"
"s_add_u32 m0, s61, 3072\n"
"s_add_u32 s68, s68, 12288\n"
"s_waitcnt lgkmcnt(6)\n"
"v_mfma_f32_16x16x32_bf16 v[172:175], a[236:239], a[60:63], v[172:175]\n"
"ds_read_b64_tr_b16 a[228:229], v239 offset:0\n"
"v_mfma_f32_16x16x32_bf16 v[176:179], a[236:239], a[64:67], v[176:179]\n"
"ds_read_b64_tr_b16 a[230:231], v239 offset:2048\n"
"v_cvt_pk_bf16_f32 v213, v166, v167\n"
"v_mfma_f32_16x16x32_bf16 v[180:183], a[236:239], a[68:71], v[180:183]\n"
"v_cvt_pk_bf16_f32 v216, v168, v169\n"
"v_cvt_pk_bf16_f32 v217, v170, v171\n"
"s_waitcnt lgkmcnt(6)\n"
"v_mfma_f32_16x16x32_bf16 v[184:187], a[216:219], a[36:39], v[184:187]\n"
"ds_read_b64_tr_b16 a[232:233], v239 offset:4096\n"
"v_mfma_f32_16x16x32_bf16 v[188:191], a[216:219], a[40:43], v[188:191]\n"
"ds_read_b64_tr_b16 a[234:235], v239 offset:6144\n"
"v_mfma_f32_16x16x32_bf16 v[192:195], a[216:219], a[44:47], v[192:195]\n"
"buffer_load_dwordx4 %[dX], %[rWo], s68 offen offset:0 lds\n"
"s_add_u32 s68, s68, 9216\n"
"s_waitcnt lgkmcnt(6)\n"
"v_mfma_f32_16x16x32_bf16 v[184:187], a[220:223], a[48:51], v[184:187]\n"
"ds_read_b64_tr_b16 a[236:237], v239 offset:8192\n"
"v_cvt_pk_bf16_f32 v210, v172, v173\n"
"v_cvt_pk_bf16_f32 v211, v174, v175\n"
"v_cvt_pk_bf16_f32 v214, v176, v177\n"
"v_mfma_f32_16x16x32_bf16 v[188:191], a[220:223], a[52:55], v[188:191]\n"
"ds_read_b64_tr_b16 a[238:239], v239 offset:10240\n"
"v_cvt_pk_bf16_f32 v215, v178, v179\n"
"v_cvt_pk_bf16_f32 v218, v180, v181\n"
"v_cvt_pk_bf16_f32 v219, v182, v183\n"
"v_mfma_f32_16x16x32_bf16 v[192:195], a[220:223], a[56:59], v[192:195]\n"
"buffer_load_dwordx4 %[dX], %[rWo], s68 offen offset:1024 lds\n"
"s_add_u32 s68, s68, 9216\n"
"s_waitcnt lgkmcnt(6)\n"
"v_mfma_f32_16x16x32_bf16 v[184:187], a[224:227], a[60:63], v[184:187]\n"
"s_add_u32 s61, %[m0b], s60\n"
"s_add_u32 s60, s60, 12288\n"
"s_cmp_eq_u32 s60, 61440\n"
"s_cselect_b32 s60, 0, s60\n"
"v_add_u32 v236, s60, %[rdO]\n"
"v_mfma_f32_16x16x32_bf16 v[188:191], a[224:227], a[64:67], v[188:191]\n"
"v_xor_b32 v237, 32, v236\n"
"v_xor_b32 v238, 64, v236\n"
"v_xor_b32 v239, 96, v236\n"
"v_mfma_f32_16x16x32_bf16 v[192:195], a[224:227], a[68:71], v[192:195]\n"
"s_waitcnt vmcnt(17) lgkmcnt(0)\n"
"s_barrier\n"
"ds_read_b64_tr_b16 a[216:217], v236 offset:0\n"
"ds_read_b64_tr_b16 a[218:219], v236 offset:2048\n"
"v_mfma_f32_16x16x32_bf16 v[196:199], a[228:231], a[36:39], v[196:199]\n"
"ds_read_b64_tr_b16 a[220:221], v236 offset:4096\n"
"v_mfma_f32_16x16x32_bf16 v[200:203], a[228:231], a[40:43], v[200:203]\n"
"ds_read_b64_tr_b16 a[222:223], v236 offset:6144\n"
"v_mfma_f32_16x16x32_bf16 v[204:207], a[228:231], a[44:47], v[204:207]\n"
"v_mfma_f32_16x16x32_bf16 v[196:199], a[232:235], a[48:51], v[196:199]\n"
"ds_read_b64_tr_b16 a[224:225], v236 offset:8192\n"
"v_mfma_f32_16x16x32_bf16 v[200:203], a[232:235], a[52:55], v[200:203]\n"
"ds_read_b64_tr_b16 a[226:227], v236 offset:10240\n"
"v_mfma_f32_16x16x32_bf16 v[204:207], a[232:235], a[56:59], v[204:207]\n"
"buffer_load_dwordx4 %[dX], %[rWo], s68 offen offset:2048 lds\n"
"v_cvt_pk_bf16_f32 v220, v184, v185\n"
"v_cvt_pk_bf16_f32 v221, v186, v187\n"
"v_mfma_f32_16x16x32_bf16 v[196:199], a[236:239], a[60:63], v[196:199]\n"
"ds_read_b64_tr_b16 a[228:229], v237 offset:0\n"
"buffer_store_dwordx4 v[208:211], %[stv], %[rWs], s73 offen offset:0 sc1\n"
"v_cvt_pk_bf16_f32 v224, v188, v189\n"
"v_cvt_pk_bf16_f32 v225, v190, v191\n"
"v_mfma_f32_16x16x32_bf16 v[200:203], a[236:239], a[64:67], v[200:203]\n"
"ds_read_b64_tr_b16 a[230:231], v237 offset:2048\n"
"buffer_store_dwordx4 v[212:215], %[stv], %[rWs], s73 offen offset:1024 sc1\n"
"v_cvt_pk_bf16_f32 v228, v192, v193\n"
"v_mfma_f32_16x16x32_bf16 v[204:207], a[236:239], a[68:71], v[204:207]\n"
"buffer_store_dwordx4 v[216:219], %[stv], %[rWs], s73 offen offset:2048 sc1\n"
"s_add_u32 s73, s73, 3072\n"
"v_cvt_pk_bf16_f32 v229, v194, v195\n"
"s_waitcnt lgkmcnt(6)\n"
"v_mfma_f32_16x16x32_bf16 v[160:163], a[216:219], v[16:19], 0\n"
"ds_read_b64_tr_b16 a[232:233], v237 offset:4096\n"
"v_mfma_f32_16x16x32_bf16 v[164:167], a[216:219], v[20:23], 0\n"
"ds_read_b64_tr_b16 a[234:235], v237 offset:6144\n"
"v_mfma_f32_16x16x32_bf16 v[168:171], a[216:219], v[24:27], 0\n"
"s_mov_b32 m0, s61\n"
"s_add_u32 s68, %[wo0], s72\n"
"s_add_u32 s68, s68, 983040\n"
"buffer_load_dwordx4 %[dX], %[rWo], s68 offen offset:0 lds\n"
"s_add_u32 s68, s68, 9216\n"
"s_waitcnt lgkmcnt(6)\n"
"v_mfma_f32_16x16x32_bf16 v[160:163], a[220:223], v[28:31], v[160:163]\n"
"ds_read_b64_tr_b16 a[236:237], v237 offset:8192\n"
"v_cvt_pk_bf16_f32 v222, v196, v197\n"
"v_mfma_f32_16x16x32_bf16 v[164:167], a[220:223], v[32:35], v[164:167]\n"
"ds_read_b64_tr_b16 a[238:239], v237 offset:10240\n"
"v_cvt_pk_bf16_f32 v223, v198, v199\n"
"v_mfma_f32_16x16x32_bf16 v[168:171], a[220:223], v[36:39], v[168:171]\n"
"v_cvt_pk_bf16_f32 v226, v200, v201\n"
"v_cvt_pk_bf16_f32 v227, v202, v203\n"
"v_cvt_pk_bf16_f32 v230, v204, v205\n"
"s_waitcnt lgkmcnt(6)\n"
"v_mfma_f32_16x16x32_bf16 v[160:163], a[224:227], v[40:43], v[160:163]\n"
"ds_read_b64_tr_b16 a[216:217], v238 offset:0\n"
"v_cvt_pk_bf16_f32 v231, v206, v207\n"
"v_mfma_f32_16x16x32_bf16 v[164:167], a[224:227], v[44:47], v[164:167]\n"
"ds_read_b64_tr_b16 a[218:219], v238 offset:2048\n"
"v_mfma_f32_16x16x32_bf16 v[168:171], a[224:227], v[48:51], v[168:171]\n"
"buffer_load_dwordx4 %[dX], %[rWo], s68 offen offset:1024 lds\n"
"s_add_u32 s68, s68, 9216\n"
"s_waitcnt lgkmcnt(6)\n"
"v_mfma_f32_16x16x32_bf16 v[172:175], a[228:231], v[16:19], 0\n"
"ds_read_b64_tr_b16 a[220:221], v238 offset:4096\n"
"v_mfma_f32_16x16x32_bf16 v[176:179], a[228:231], v[20:23], 0\n"
"ds_read_b64_tr_b16 a[222:223], v238 offset:6144\n"
"v_mfma_f32_16x16x32_bf16 v[180:183], a[228:231], v[24:27], 0\n"
"s_waitcnt lgkmcnt(6)\n"
"v_mfma_f32_16x16x32_bf16 v[172:175], a[232:235], v[28:31], v[172:175]\n"
"ds_read_b64_tr_b16 a[224:225], v238 offset:8192\n"
"v_mfma_f32_16x16x32_bf16 v[176:179], a[232:235], v[32:35], v[176:179]\n"
"ds_read_b64_tr_b16 a[226:227], v238 offset:10240\n"
"v_mfma_f32_16x16x32_bf16 v[180:183], a[232:235], v[36:39], v[180:183]\n"
"buffer_load_dwordx4 %[dX], %[rWo], s68 offen offset:2048 lds\n"
"s_add_u32 m0, s61, 3072\n"
"s_add_u32 s68, s68, 12288\n"
"s_waitcnt lgkmcnt(6)\n"
"v_mfma_f32_16x16x32_bf16 v[172:175], a[236:239], v[40:43], v[172:175]\n"
"ds_read_b64_tr_b16 a[228:229], v239 offset:0\n"
"v_mfma_f32_16x16x32_bf16 v[176:179], a[236:239], v[44:47], v[176:179]\n"
"ds_read_b64_tr_b16 a[230:231], v239 offset:2048\n"
"v_mfma_f32_16x16x32_bf16 v[180:183], a[236:239], v[48:51], v[180:183]\n"
"s_waitcnt lgkmcnt(6)\n"
"v_mfma_f32_16x16x32_bf16 v[184:187], a[216:219], v[16:19], 0\n"
"ds_read_b64_tr_b16 a[232:233], v239 offset:4096\n"
"v_mfma_f32_16x16x32_bf16 v[188:191], a[216:219], v[20:23], 0\n"
"ds_read_b64_tr_b16 a[234:235], v239 offset:6144\n"
"v_mfma_f32_16x16x32_bf16 v[192:195], a[216:219], v[24:27], 0\n"
"buffer_load_dwordx4 %[dX], %[rWo], s68 offen offset:0 lds\n"
"s_add_u32 s68, s68, 9216\n"
"s_waitcnt lgkmcnt(6)\n"
"v_mfma_f32_16x16x32_bf16 v[184:187], a[220:223], v[28:31], v[184:187]\n"
"ds_read_b64_tr_b16 a[236:237], v239 offset:8192\n"
"v_mfma_f32_16x16x32_bf16 v[188:191], a[220:223], v[32:35], v[188:191]\n"
"ds_read_b64_tr_b16 a[238:239], v239 offset:10240\n"
"v_mfma_f32_16x16x32_bf16 v[192:195], a[220:223], v[36:39], v[192:195]\n"
"buffer_load_dwordx4 %[dX], %[rWo], s68 offen offset:1024 lds\n"
"s_add_u32 s68, s68, 9216\n"
"s_waitcnt lgkmcnt(6)\n"
"v_mfma_f32_16x16x32_bf16 v[184:187], a[224:227], v[40:43], v[184:187]\n"
"s_add_u32 s61, %[m0b], s60\n"
"s_add_u32 s60, s60, 12288\n"
"s_cmp_eq_u32 s60, 61440\n"
"s_cselect_b32 s60, 0, s60\n"
"v_add_u32 v236, s60, %[rdO]\n"
"v_mfma_f32_16x16x32_bf16 v[188:191], a[224:227], v[44:47], v[188:191]\n"
"v_xor_b32 v237, 32, v236\n"
"v_xor_b32 v238, 64, v236\n"
"v_xor_b32 v239, 96, v236\n"
"v_mfma_f32_16x16x32_bf16 v[192:195], a[224:227], v[48:51], v[192:195]\n"
"s_waitcnt vmcnt(20) lgkmcnt(0)\n"
"s_barrier\n"
"ds_read_b64_tr_b16 a[216:217], v236 offset:0\n"
"ds_read_b64_tr_b16 a[218:219], v236 offset:2048\n"
"v_mfma_f32_16x16x32_bf16 v[196:199], a[228:231], v[16:19], 0\n"
"ds_read_b64_tr_b16 a[220:221], v236 offset:4096\n"
"v_mfma_f32_16x16x32_bf16 v[200:203], a[228:231], v[20:23], 0\n"
"ds_read_b64_tr_b16 a[222:223], v236 offset:6144\n"
"v_mfma_f32_16x16x32_bf16 v[204:207], a[228:231], v[24:27], 0\n"
"v_mfma_f32_16x16x32_bf16 v[196:199], a[232:235], v[28:31], v[196:199]\n"
"ds_read_b64_tr_b16 a[224:225], v236 offset:8192\n"
"v_mfma_f32_16x16x32_bf16 v[200:203], a[232:235], v[32:35], v[200:203]\n"
"ds_read_b64_tr_b16 a[226:227], v236 offset:10240\n"
"v_mfma_f32_16x16x32_bf16 v[204:207], a[232:235], v[36:39], v[204:207]\n"
"buffer_load_dwordx4 %[dX], %[rWo], s68 offen offset:2048 lds\n"
"v_mfma_f32_16x16x32_bf16 v[196:199], a[236:239], v[40:43], v[196:199]\n"
"ds_read_b64_tr_b16 a[228:229], v237 offset:0\n"
"buffer_store_dwordx4 v[220:223], %[stv], %[rWs], s73 offen offset:0 sc1\n"
"v_mfma_f32_16x16x32_bf16 v[200:203], a[236:239], v[44:47], v[200:203]\n"
"ds_read_b64_tr_b16 a[230:231], v237 offset:2048\n"
"buffer_store_dwordx4 v[224:227], %[stv], %[rWs], s73 offen offset:1024 sc1\n"
"buffer_store_dwordx4 v[228:231], %[stv], %[rWs], s73 offen offset:2048 sc1\n"
"v_mfma_f32_16x16x32_bf16 v[204:207], a[236:239], v[48:51], v[204:207]\n"
"s_add_u32 s73, s73, 3072\n"
"s_cmp_eq_u32 s73, s58\n"
"s_cselect_b32 s73, %[st0], s73\n"
"s_waitcnt lgkmcnt(6)\n"
"v_mfma_f32_16x16x32_bf16 v[160:163], a[216:219], v[52:55], v[160:163]\n"
"ds_read_b64_tr_b16 a[232:233], v237 offset:4096\n"
"v_mfma_f32_16x16x32_bf16 v[164:167], a[216:219], v[56:59], v[164:167]\n"
"ds_read_b64_tr_b16 a[234:235], v237 offset:6144\n"
"v_mfma_f32_16x16x32_bf16 v[168:171], a[216:219], v[60:63], v[168:171]\n"
"s_mov_b32 m0, s61\n"
"s_add_u32 s68, %[wo0], s72\n"
"s_add_u32 s68, s68, 1105920\n"
"buffer_load_dwordx4 %[dX], %[rWo], s68 offen offset:0 lds\n"
"s_add_u32 s68, s68, 9216\n"
"s_waitcnt lgkmcnt(6)\n"
"v_mfma_f32_16x16x32_bf16 v[160:163], a[220:223], v[64:67], v[160:163]\n"
"ds_read_b64_tr_b16 a[236:237], v237 offset:8192\n"
"v_mfma_f32_16x16x32_bf16 v[164:167], a[220:223], v[68:71], v[164:167]\n"
"ds_read_b64_tr_b16 a[238:239], v237 offset:10240\n"
"v_mfma_f32_16x16x32_bf16 v[168:171], a[220:223], v[72:75], v[168:171]\n"
"s_waitcnt lgkmcnt(6)\n"
"v_mfma_f32_16x16x32_bf16 v[160:163], a[224:227], v[76:79], v[160:163]\n"
"ds_read_b64_tr_b16 a[216:217], v238 offset:0\n"
"v_mfma_f32_16x16x32_bf16 v[164:167], a[224:227], v[80:83], v[164:167]\n"
"ds_read_b64_tr_b16 a[218:219], v238 offset:2048\n"
"v_mfma_f32_16x16x32_bf16 v[168:171], a[224:227], v[84:87], v[168:171]\n"
"buffer_load_dwordx4 %[dX], %[rWo], s68 offen offset:1024 lds\n"
"s_add_u32 s68, s68, 9216\n"
"s_waitcnt lgkmcnt(6)\n"
"v_mfma_f32_16x16x32_bf16 v[172:175], a[228:231], v[52:55], v[172:175]\n"
"ds_read_b64_tr_b16 a[220:221], v238 offset:4096\n"
"v_mfma_f32_16x16x32_bf16 v[176:179], a[228:231], v[56:59], v[176:179]\n"
"ds_read_b64_tr_b16 a[222:223], v238 offset:6144\n"
"v_mfma_f32_16x16x32_bf16 v[180:183], a[228:231], v[60:63], v[180:183]\n"
"s_waitcnt lgkmcnt(6)\n"
"v_mfma_f32_16x16x32_bf16 v[172:175], a[232:235], v[64:67], v[172:175]\n"
"ds_read_b64_tr_b16 a[224:225], v238 offset:8192\n"
"v_mfma_f32_16x16x32_bf16 v[176:179], a[232:235], v[68:71], v[176:179]\n"
"ds_read_b64_tr_b16 a[226:227], v238 offset:10240\n"
"v_mfma_f32_16x16x32_bf16 v[180:183], a[232:235], v[72:75], v[180:183]\n"
"buffer_load_dwordx4 %[dX], %[rWo], s68 offen offset:2048 lds\n"
"s_add_u32 m0, s61, 3072\n"
"s_add_u32 s68, s68, 12288\n"
"s_waitcnt lgkmcnt(6)\n"
"v_mfma_f32_16x16x32_bf16 v[172:175], a[236:239], v[76:79], v[172:175]\n"
"ds_read_b64_tr_b16 a[228:229], v239 offset:0\n"
"v_mfma_f32_16x16x32_bf16 v[176:179], a[236:239], v[80:83], v[176:179]\n"
"ds_read_b64_tr_b16 a[230:231], v239 offset:2048\n"
"v_mfma_f32_16x16x32_bf16 v[180:183], a[236:239], v[84:87], v[180:183]\n"
"s_waitcnt lgkmcnt(6)\n"
"v_mfma_f32_16x16x32_bf16 v[184:187], a[216:219], v[52:55], v[184:187]\n"
"ds_read_b64_tr_b16 a[232:233], v239 offset:4096\n"
"v_mfma_f32_16x16x32_bf16 v[188:191], a[216:219], v[56:59], v[188:191]\n"
"ds_read_b64_tr_b16 a[234:235], v239 offset:6144\n"
"v_mfma_f32_16x16x32_bf16 v[192:195], a[216:219], v[60:63], v[192:195]\n"
"buffer_load_dwordx4 %[dX], %[rWo], s68 offen offset:0 lds\n"
"s_add_u32 s68, s68, 9216\n"
"s_waitcnt lgkmcnt(6)\n"
"v_mfma_f32_16x16x32_bf16 v[184:187], a[220:223], v[64:67], v[184:187]\n"
"ds_read_b64_tr_b16 a[236:237], v239 offset:8192\n"
"v_mfma_f32_16x16x32_bf16 v[188:191], a[220:223], v[68:71], v[188:191]\n"
"ds_read_b64_tr_b16 a[238:239], v239 offset:10240\n"
"v_mfma_f32_16x16x32_bf16 v[192:195], a[220:223], v[72:75], v[192:195]\n"
"buffer_load_dwordx4 %[dX], %[rWo], s68 offen offset:1024 lds\n"
"s_add_u32 s68, s68, 9216\n"
"s_waitcnt lgkmcnt(6)\n"
"v_mfma_f32_16x16x32_bf16 v[184:187], a[224:227], v[76:79], v[184:187]\n"
"s_add_u32 s61, %[m0b], s60\n"
"s_add_u32 s60, s60, 12288\n"
"s_cmp_eq_u32 s60, 61440\n"
"s_cselect_b32 s60, 0, s60\n"
"v_add_u32 v236, s60, %[rdO]\n"
"v_mfma_f32_16x16x32_bf16 v[188:191], a[224:227], v[80:83], v[188:191]\n"
"v_xor_b32 v237, 32, v236\n"
"v_xor_b32 v238, 64, v236\n"
"v_xor_b32 v239, 96, v236\n"
"v_mfma_f32_16x16x32_bf16 v[192:195], a[224:227], v[84:87], v[192:195]\n"
"s_waitcnt vmcnt(23) lgkmcnt(0)\n"
"s_barrier\n"
"ds_read_b64_tr_b16 a[216:217], v236 offset:0\n"
"ds_read_b64_tr_b16 a[218:219], v236 offset:2048\n"
"v_mfma_f32_16x16x32_bf16 v[196:199], a[228:231], v[52:55], v[196:199]\n"
"ds_read_b64_tr_b16 a[220:221], v236 offset:4096\n"
"v_mfma_f32_16x16x32_bf16 v[200:203], a[228:231], v[56:59], v[200:203]\n"
"ds_read_b64_tr_b16 a[222:223], v236 offset:6144\n"
"v_mfma_f32_16x16x32_bf16 v[204:207], a[228:231], v[60:63], v[204:207]\n"
"v_mfma_f32_16x16x32_bf16 v[196:199], a[232:235], v[64:67], v[196:199]\n"
"ds_read_b64_tr_b16 a[224:225], v236 offset:8192\n"
"v_mfma_f32_16x16x32_bf16 v[200:203], a[232:235], v[68:71], v[200:203]\n"
"ds_read_b64_tr_b16 a[226:227], v236 offset:10240\n"
"v_mfma_f32_16x16x32_bf16 v[204:207], a[232:235], v[72:75], v[204:207]\n"
"buffer_load_dwordx4 %[dX], %[rWo], s68 offen offset:2048 lds\n"
"v_mfma_f32_16x16x32_bf16 v[196:199], a[236:239], v[76:79], v[196:199]\n"
"ds_read_b64_tr_b16 a[228:229], v237 offset:0\n"
"buffer_load_dword %[rdK], v254, %[rWs], %[flg] offen sc1\n"
"v_mfma_f32_16x16x32_bf16 v[200:203], a[236:239], v[80:83], v[200:203]\n"
"ds_read_b64_tr_b16 a[230:231], v237 offset:2048\n"
"v_mfma_f32_16x16x32_bf16 v[204:207], a[236:239], v[84:87], v[204:207]\n"
"s_waitcnt lgkmcnt(6)\n"
"v_mfma_f32_16x16x32_bf16 v[160:163], a[216:219], v[88:91], v[160:163]\n"
"ds_read_b64_tr_b16 a[232:233], v237 offset:4096\n"
"v_mfma_f32_16x16x32_bf16 v[164:167], a[216:219], v[92:95], v[164:167]\n"
"ds_read_b64_tr_b16 a[234:235], v237 offset:6144\n"
"v_mfma_f32_16x16x32_bf16 v[168:171], a[216:219], v[96:99], v[168:171]\n"
"s_mov_b32 m0, s61\n"
"s_add_u32 s68, %[wo1], s72\n"
"s_add_u32 s68, s68, 983040\n"
"buffer_load_dwordx4 %[dX], %[rWo], s68 offen offset:0 lds\n"
"s_add_u32 s68, s68, 9216\n"
"s_waitcnt lgkmcnt(6)\n"
"v_mfma_f32_16x16x32_bf16 v[160:163], a[220:223], v[100:103], v[160:163]\n"
"ds_read_b64_tr_b16 a[236:237], v237 offset:8192\n"
"v_mfma_f32_16x16x32_bf16 v[164:167], a[220:223], v[104:107], v[164:167]\n"
"ds_read_b64_tr_b16 a[238:239], v237 offset:10240\n"
"v_mfma_f32_16x16x32_bf16 v[168:171], a[220:223], v[108:111], v[168:171]\n"
"s_waitcnt lgkmcnt(6)\n"
"v_mfma_f32_16x16x32_bf16 v[160:163], a[224:227], v[112:115], v[160:163]\n"
"ds_read_b64_tr_b16 a[216:217], v238 offset:0\n"
"v_mfma_f32_16x16x32_bf16 v[164:167], a[224:227], v[116:119], v[164:167]\n"
"ds_read_b64_tr_b16 a[218:219], v238 offset:2048\n"
"v_mfma_f32_16x16x32_bf16 v[168:171], a[224:227], v[120:123], v[168:171]\n"
"buffer_load_dwordx4 %[dX], %[rWo], s68 offen offset:1024 lds\n"
"s_add_u32 s68, s68, 9216\n"
"s_waitcnt lgkmcnt(6)\n"
"v_mfma_f32_16x16x32_bf16 v[172:175], a[228:231], v[88:91], v[172:175]\n"
"ds_read_b64_tr_b16 a[220:221], v238 offset:4096\n"
"v_mfma_f32_16x16x32_bf16 v[176:179], a[228:231], v[92:95], v[176:179]\n"
"ds_read_b64_tr_b16 a[222:223], v238 offset:6144\n"
"v_mfma_f32_16x16x32_bf16 v[180:183], a[228:231], v[96:99], v[180:183]\n"
"s_waitcnt lgkmcnt(6)\n"
"v_mfma_f32_16x16x32_bf16 v[172:175], a[232:235], v[100:103], v[172:175]\n"
"ds_read_b64_tr_b16 a[224:225], v238 offset:8192\n"
"v_mfma_f32_16x16x32_bf16 v[176:179], a[232:235], v[104:107], v[176:179]\n"
"ds_read_b64_tr_b16 a[226:227], v238 offset:10240\n"
"v_mfma_f32_16x16x32_bf16 v[180:183], a[232:235], v[108:111], v[180:183]\n"
"buffer_load_dwordx4 %[dX], %[rWo], s68 offen offset:2048 lds\n"
"s_add_u32 m0, s61, 3072\n"
"s_add_u32 s68, s68, 12288\n"
"s_waitcnt lgkmcnt(6)\n"
"v_mfma_f32_16x16x32_bf16 v[172:175], a[236:239], v[112:115], v[172:175]\n"
"ds_read_b64_tr_b16 a[228:229], v239 offset:0\n"
"v_mfma_f32_16x16x32_bf16 v[176:179], a[236:239], v[116:119], v[176:179]\n"
"ds_read_b64_tr_b16 a[230:231], v239 offset:2048\n"
"v_mfma_f32_16x16x32_bf16 v[180:183], a[236:239], v[120:123], v[180:183]\n"
"s_waitcnt lgkmcnt(6)\n"
"v_mfma_f32_16x16x32_bf16 v[184:187], a[216:219], v[88:91], v[184:187]\n"
"ds_read_b64_tr_b16 a[232:233], v239 offset:4096\n"
"v_mfma_f32_16x16x32_bf16 v[188:191], a[216:219], v[92:95], v[188:191]\n"
"ds_read_b64_tr_b16 a[234:235], v239 offset:6144\n"
"v_mfma_f32_16x16x32_bf16 v[192:195], a[216:219], v[96:99], v[192:195]\n"
"buffer_load_dwordx4 %[dX], %[rWo], s68 offen offset:0 lds\n"
"s_add_u32 s68, s68, 9216\n"
"s_waitcnt lgkmcnt(6)\n"
"v_mfma_f32_16x16x32_bf16 v[184:187], a[220:223], v[100:103], v[184:187]\n"
"ds_read_b64_tr_b16 a[236:237], v239 offset:8192\n"
"v_mfma_f32_16x16x32_bf16 v[188:191], a[220:223], v[104:107], v[188:191]\n"
"ds_read_b64_tr_b16 a[238:239], v239 offset:10240\n"
"v_mfma_f32_16x16x32_bf16 v[192:195], a[220:223], v[108:111], v[192:195]\n"
"buffer_load_dwordx4 %[dX], %[rWo], s68 offen offset:1024 lds\n"
"s_add_u32 s68, s68, 9216\n"
"s_waitcnt lgkmcnt(6)\n"
"v_mfma_f32_16x16x32_bf16 v[184:187], a[224:227], v[112:115], v[184:187]\n"
"s_add_u32 s61, %[m0b], s60\n"
"s_add_u32 s60, s60, 12288\n"
"s_cmp_eq_u32 s60, 61440\n"
"s_cselect_b32 s60, 0, s60\n"
"v_add_u32 v236, s60, %[rdO]\n"
"v_mfma_f32_16x16x32_bf16 v[188:191], a[224:227], v[116:119], v[188:191]\n"
"v_xor_b32 v237, 32, v236\n"
"v_xor_b32 v238, 64, v236\n"
"v_xor_b32 v239, 96, v236\n"
"v_mfma_f32_16x16x32_bf16 v[192:195], a[224:227], v[120:123], v[192:195]\n"
"s_waitcnt vmcnt(24) lgkmcnt(0)\n"
"s_barrier\n"
"ds_read_b64_tr_b16 a[216:217], v236 offset:0\n"
"ds_read_b64_tr_b16 a[218:219], v236 offset:2048\n"
"v_mfma_f32_16x16x32_bf16 v[196:199], a[228:231], v[88:91], v[196:199]\n"
"ds_read_b64_tr_b16 a[220:221], v236 offset:4096\n"
"v_mfma_f32_16x16x32_bf16 v[200:203], a[228:231], v[92:95], v[200:203]\n"
"ds_read_b64_tr_b16 a[222:223], v236 offset:6144\n"
"v_mfma_f32_16x16x32_bf16 v[204:207], a[228:231], v[96:99], v[204:207]\n"
"v_mfma_f32_16x16x32_bf16 v[196:199], a[232:235], v[100:103], v[196:199]\n"
"ds_read_b64_tr_b16 a[224:225], v236 offset:8192\n"
"v_mfma_f32_16x16x32_bf16 v[200:203], a[232:235], v[104:107], v[200:203]\n"
"ds_read_b64_tr_b16 a[226:227], v236 offset:10240\n"
"v_mfma_f32_16x16x32_bf16 v[204:207], a[232:235], v[108:111], v[204:207]\n"
"buffer_load_dwordx4 %[dX], %[rWo], s68 offen offset:2048 lds\n"
"v_mfma_f32_16x16x32_bf16 v[196:199], a[236:239], v[112:115], v[196:199]\n"
"ds_read_b64_tr_b16 a[228:229], v237 offset:0\n"
"v_mfma_f32_16x16x32_bf16 v[200:203], a[236:239], v[116:119], v[200:203]\n"
"ds_read_b64_tr_b16 a[230:231], v237 offset:2048\n"
"v_mfma_f32_16x16x32_bf16 v[204:207], a[236:239], v[120:123], v[204:207]\n"
"s_waitcnt lgkmcnt(6)\n"
"v_mfma_f32_16x16x32_bf16 v[160:163], a[216:219], v[124:127], v[160:163]\n"
"ds_read_b64_tr_b16 a[232:233], v237 offset:4096\n"
"v_mfma_f32_16x16x32_bf16 v[164:167], a[216:219], v[128:131], v[164:167]\n"
"ds_read_b64_tr_b16 a[234:235], v237 offset:6144\n"
"v_mfma_f32_16x16x32_bf16 v[168:171], a[216:219], v[132:135], v[168:171]\n"
"s_mov_b32 m0, s61\n"
"s_add_u32 s68, %[wo1], s72\n"
"s_add_u32 s68, s68, 1105920\n"
"buffer_load_dwordx4 %[dX], %[rWo], s68 offen offset:0 lds\n"
"s_add_u32 s68, s68, 9216\n"
"s_waitcnt lgkmcnt(6)\n"
"v_mfma_f32_16x16x32_bf16 v[160:163], a[220:223], v[136:139], v[160:163]\n"
"ds_read_b64_tr_b16 a[236:237], v237 offset:8192\n"
"v_mfma_f32_16x16x32_bf16 v[164:167], a[220:223], v[140:143], v[164:167]\n"
"ds_read_b64_tr_b16 a[238:239], v237 offset:10240\n"
"v_mfma_f32_16x16x32_bf16 v[168:171], a[220:223], v[144:147], v[168:171]\n"
"s_waitcnt lgkmcnt(6)\n"
"v_mfma_f32_16x16x32_bf16 v[160:163], a[224:227], v[148:151], v[160:163]\n"
"ds_read_b64_tr_b16 a[216:217], v238 offset:0\n"
"v_mfma_f32_16x16x32_bf16 v[164:167], a[224:227], v[152:155], v[164:167]\n"
"ds_read_b64_tr_b16 a[218:219], v238 offset:2048\n"
"v_mfma_f32_16x16x32_bf16 v[168:171], a[224:227], v[156:159], v[168:171]\n"
"buffer_load_dwordx4 %[dX], %[rWo], s68 offen offset:1024 lds\n"
"s_add_u32 s68, s68, 9216\n"
"s_waitcnt lgkmcnt(6)\n"
"v_mfma_f32_16x16x32_bf16 v[172:175], a[228:231], v[124:127], v[172:175]\n"
"ds_read_b64_tr_b16 a[220:221], v238 offset:4096\n"
"v_mfma_f32_16x16x32_bf16 v[176:179], a[228:231], v[128:131], v[176:179]\n"
"ds_read_b64_tr_b16 a[222:223], v238 offset:6144\n"
"v_mfma_f32_16x16x32_bf16 v[180:183], a[228:231], v[132:135], v[180:183]\n"
"s_waitcnt lgkmcnt(6)\n"
"v_mfma_f32_16x16x32_bf16 v[172:175], a[232:235], v[136:139], v[172:175]\n"
"ds_read_b64_tr_b16 a[224:225], v238 offset:8192\n"
"v_mfma_f32_16x16x32_bf16 v[176:179], a[232:235], v[140:143], v[176:179]\n"
"ds_read_b64_tr_b16 a[226:227], v238 offset:10240\n"
"v_mfma_f32_16x16x32_bf16 v[180:183], a[232:235], v[144:147], v[180:183]\n"
"buffer_load_dwordx4 %[dX], %[rWo], s68 offen offset:2048 lds\n"
"s_add_u32 m0, s61, 3072\n"
"s_add_u32 s68, s68, 12288\n"
"s_waitcnt lgkmcnt(6)\n"
"v_mfma_f32_16x16x32_bf16 v[172:175], a[236:239], v[148:151], v[172:175]\n"
"ds_read_b64_tr_b16 a[228:229], v239 offset:0\n"
"v_mfma_f32_16x16x32_bf16 v[176:179], a[236:239], v[152:155], v[176:179]\n"
"ds_read_b64_tr_b16 a[230:231], v239 offset:2048\n"
"v_mfma_f32_16x16x32_bf16 v[180:183], a[236:239], v[156:159], v[180:183]\n"
"s_waitcnt lgkmcnt(6)\n"
"v_mfma_f32_16x16x32_bf16 v[184:187], a[216:219], v[124:127], v[184:187]\n"
"ds_read_b64_tr_b16 a[232:233], v239 offset:4096\n"
"v_mfma_f32_16x16x32_bf16 v[188:191], a[216:219], v[128:131], v[188:191]\n"
"ds_read_b64_tr_b16 a[234:235], v239 offset:6144\n"
"v_mfma_f32_16x16x32_bf16 v[192:195], a[216:219], v[132:135], v[192:195]\n"
"buffer_load_dwordx4 %[dX], %[rWo], s68 offen offset:0 lds\n"
"s_add_u32 s68, s68, 9216\n"
"s_waitcnt lgkmcnt(6)\n"
"v_mfma_f32_16x16x32_bf16 v[184:187], a[220:223], v[136:139], v[184:187]\n"
"ds_read_b64_tr_b16 a[236:237], v239 offset:8192\n"
"v_mfma_f32_16x16x32_bf16 v[188:191], a[220:223], v[140:143], v[188:191]\n"
"ds_read_b64_tr_b16 a[238:239], v239 offset:10240\n"
"v_mfma_f32_16x16x32_bf16 v[192:195], a[220:223], v[144:147], v[192:195]\n"
"buffer_load_dwordx4 %[dX], %[rWo], s68 offen offset:1024 lds\n"
"s_add_u32 s68, s68, 9216\n"
"s_waitcnt lgkmcnt(6)\n"
"v_mfma_f32_16x16x32_bf16 v[184:187], a[224:227], v[148:151], v[184:187]\n"
"s_add_u32 s61, %[m0b], s60\n"
"s_add_u32 s60, s60, 12288\n"
"s_cmp_eq_u32 s60, 61440\n"
"s_cselect_b32 s60, 0, s60\n"
"v_add_u32 v236, s60, %[rdO]\n"
"v_mfma_f32_16x16x32_bf16 v[188:191], a[224:227], v[152:155], v[188:191]\n"
"v_xor_b32 v237, 32, v236\n"
"v_xor_b32 v238, 64, v236\n"
"v_xor_b32 v239, 96, v236\n"
"v_mfma_f32_16x16x32_bf16 v[192:195], a[224:227], v[156:159], v[192:195]\n"
"s_waitcnt vmcnt(21) lgkmcnt(0)\n"
"s_barrier\n"
"ds_read_b64_tr_b16 a[216:217], v236 offset:0\n"
"ds_read_b64_tr_b16 a[218:219], v236 offset:2048\n"
"v_mfma_f32_16x16x32_bf16 v[196:199], a[228:231], v[124:127], v[196:199]\n"
"ds_read_b64_tr_b16 a[220:221], v236 offset:4096\n"
"v_mfma_f32_16x16x32_bf16 v[200:203], a[228:231], v[128:131], v[200:203]\n"
"ds_read_b64_tr_b16 a[222:223], v236 offset:6144\n"
"v_mfma_f32_16x16x32_bf16 v[204:207], a[228:231], v[132:135], v[204:207]\n"
"v_mfma_f32_16x16x32_bf16 v[196:199], a[232:235], v[136:139], v[196:199]\n"
"ds_read_b64_tr_b16 a[224:225], v236 offset:8192\n"
"v_mfma_f32_16x16x32_bf16 v[200:203], a[232:235], v[140:143], v[200:203]\n"
"ds_read_b64_tr_b16 a[226:227], v236 offset:10240\n"
"v_mfma_f32_16x16x32_bf16 v[204:207], a[232:235], v[144:147], v[204:207]\n"
"buffer_load_dwordx4 %[dX], %[rWo], s68 offen offset:2048 lds\n"
"s_add_u32 s72, s72, 128\n"
"s_cmp_eq_u32 s72, 640\n"
"s_cselect_b32 s72, 0, s72\n"
"v_mfma_f32_16x16x32_bf16 v[196:199], a[236:239], v[148:151], v[196:199]\n"
"ds_read_b64_tr_b16 a[228:229], v237 offset:0\n"
"v_mfma_f32_16x16x32_bf16 v[200:203], a[236:239], v[152:155], v[200:203]\n"
"ds_read_b64_tr_b16 a[230:231], v237 offset:2048\n"
"v_mfma_f32_16x16x32_bf16 v[204:207], a[236:239], v[156:159], v[204:207]\n"
"s_waitcnt lgkmcnt(6)\n"
"v_mfma_f32_16x16x32_bf16 v[160:163], a[216:219], a[144:147], v[160:163]\n"
"ds_read_b64_tr_b16 a[232:233], v237 offset:4096\n"
"v_mfma_f32_16x16x32_bf16 v[164:167], a[216:219], a[148:151], v[164:167]\n"
"ds_read_b64_tr_b16 a[234:235], v237 offset:6144\n"
"v_mfma_f32_16x16x32_bf16 v[168:171], a[216:219], a[152:155], v[168:171]\n"
"s_waitcnt lgkmcnt(6)\n"
"v_mfma_f32_16x16x32_bf16 v[160:163], a[220:223], a[156:159], v[160:163]\n"
"ds_read_b64_tr_b16 a[236:237], v237 offset:8192\n"
"v_mfma_f32_16x16x32_bf16 v[164:167], a[220:223], a[160:163], v[164:167]\n"
"ds_read_b64_tr_b16 a[238:239], v237 offset:10240\n"
"v_mfma_f32_16x16x32_bf16 v[168:171], a[220:223], a[164:167], v[168:171]\n"
"s_waitcnt lgkmcnt(6)\n"
"v_mfma_f32_16x16x32_bf16 v[160:163], a[224:227], a[168:171], v[160:163]\n"
"ds_read_b64_tr_b16 a[216:217], v238 offset:0\n"
"v_mfma_f32_16x16x32_bf16 v[164:167], a[224:227], a[172:175], v[164:167]\n"
"ds_read_b64_tr_b16 a[218:219], v238 offset:2048\n"
"v_mfma_f32_16x16x32_bf16 v[168:171], a[224:227], a[176:179], v[168:171]\n"
"s_waitcnt lgkmcnt(6)\n"
"v_mfma_f32_16x16x32_bf16 v[172:175], a[228:231], a[144:147], v[172:175]\n"
"ds_read_b64_tr_b16 a[220:221], v238 offset:4096\n"
"v_mfma_f32_16x16x32_bf16 v[176:179], a[228:231], a[148:151], v[176:179]\n"
"ds_read_b64_tr_b16 a[222:223], v238 offset:6144\n"
"v_mfma_f32_16x16x32_bf16 v[180:183], a[228:231], a[152:155], v[180:183]\n"
"s_waitcnt lgkmcnt(6)\n"
"v_mfma_f32_16x16x32_bf16 v[172:175], a[232:235], a[156:159], v[172:175]\n"
"ds_read_b64_tr_b16 a[224:225], v238 offset:8192\n"
"v_mfma_f32_16x16x32_bf16 v[176:179], a[232:235], a[160:163], v[176:179]\n"
"ds_read_b64_tr_b16 a[226:227], v238 offset:10240\n"
"v_mfma_f32_16x16x32_bf16 v[180:183], a[232:235], a[164:167], v[180:183]\n"
"s_waitcnt lgkmcnt(6)\n"
"v_mfma_f32_16x16x32_bf16 v[172:175], a[236:239], a[168:171], v[172:175]\n"
"ds_read_b64_tr_b16 a[228:229], v239 offset:0\n"
"v_mfma_f32_16x16x32_bf16 v[176:179], a[236:239], a[172:175], v[176:179]\n"
"ds_read_b64_tr_b16 a[230:231], v239 offset:2048\n"
"v_mfma_f32_16x16x32_bf16 v[180:183], a[236:239], a[176:179], v[180:183]\n"
"s_waitcnt lgkmcnt(6)\n"
"v_mfma_f32_16x16x32_bf16 v[184:187], a[216:219], a[144:147], v[184:187]\n"
"ds_read_b64_tr_b16 a[232:233], v239 offset:4096\n"
"v_mfma_f32_16x16x32_bf16 v[188:191], a[216:219], a[148:151], v[188:191]\n"
"ds_read_b64_tr_b16 a[234:235], v239 offset:6144\n"
"v_mfma_f32_16x16x32_bf16 v[192:195], a[216:219], a[152:155], v[192:195]\n"
"s_waitcnt lgkmcnt(6)\n"
"v_mfma_f32_16x16x32_bf16 v[184:187], a[220:223], a[156:159], v[184:187]\n"
"ds_read_b64_tr_b16 a[236:237], v239 offset:8192\n"
"v_mfma_f32_16x16x32_bf16 v[188:191], a[220:223], a[160:163], v[188:191]\n"
"ds_read_b64_tr_b16 a[238:239], v239 offset:10240\n"
"v_mfma_f32_16x16x32_bf16 v[192:195], a[220:223], a[164:167], v[192:195]\n"
"s_waitcnt lgkmcnt(6)\n"
"v_mfma_f32_16x16x32_bf16 v[184:187], a[224:227], a[168:171], v[184:187]\n"
"s_add_u32 s61, %[m0b], s60\n"
"s_add_u32 s60, s60, 12288\n"
"s_cmp_eq_u32 s60, 61440\n"
"s_cselect_b32 s60, 0, s60\n"
"v_add_u32 v236, s60, %[rdO]\n"
"v_mfma_f32_16x16x32_bf16 v[188:191], a[224:227], a[172:175], v[188:191]\n"
"v_xor_b32 v237, 32, v236\n"
"v_xor_b32 v238, 64, v236\n"
"v_xor_b32 v239, 96, v236\n"
"v_mfma_f32_16x16x32_bf16 v[192:195], a[224:227], a[176:179], v[192:195]\n"
"s_waitcnt vmcnt(12) lgkmcnt(0)\n"
"s_barrier\n"
"ds_read_b64_tr_b16 a[216:217], v236 offset:0\n"
"ds_read_b64_tr_b16 a[218:219], v236 offset:2048\n"
"v_mfma_f32_16x16x32_bf16 v[196:199], a[228:231], a[144:147], v[196:199]\n"
"ds_read_b64_tr_b16 a[220:221], v236 offset:4096\n"
"v_mfma_f32_16x16x32_bf16 v[200:203], a[228:231], a[148:151], v[200:203]\n"
"ds_read_b64_tr_b16 a[222:223], v236 offset:6144\n"
"v_lshrrev_b32 v254, 2, %[stv]\n"
"v_add_u32 v255, 1, %[y]\n"
"v_mfma_f32_16x16x32_bf16 v[204:207], a[228:231], a[152:155], v[204:207]\n"
"s_lshl_b32 exec_lo, %[fl1], 0\n"
"s_mov_b32 exec_hi, 0\n"
"buffer_store_dword v255, v254, %[rWs], %[flg] offen sc1\n"
"s_mov_b64 exec, -1\n"
"v_mfma_f32_16x16x32_bf16 v[196:199], a[232:235], a[156:159], v[196:199]\n"
"ds_read_b64_tr_b16 a[224:225], v236 offset:8192\n"
"s_mov_b32 s69, 0x400000\n"
"v_mfma_f32_16x16x32_bf16 v[200:203], a[232:235], a[160:163], v[200:203]\n"
"ds_read_b64_tr_b16 a[226:227], v236 offset:10240\n"
"Lchk_%=:\n"
"v_cmp_ne_u32 vcc, %[rdK], %[y]\n"
"s_nop 1\n"
"s_lshr_b32 s59, vcc_lo, 16\n"
"s_or_b32 s59, s59, vcc_lo\n"
"s_andn2_b32 s59, 255, s59\n"
"s_cbranch_scc0 Lok_%=\n"
"buffer_load_dword %[rdK], v254, %[rWs], %[flg] offen sc1\n"
"s_waitcnt vmcnt(0)\n"
"s_sub_u32 s69, s69, 1\n"
"s_cbranch_scc0 Lchk_%=\n"
"Lok_%=:\n"
"v_mfma_f32_16x16x32_bf16 v[204:207], a[232:235], a[164:167], v[204:207]\n"
"v_and_b32 v252, 0x80, %[stv]\n"
"v_mad_u32_u24 v253, v252, 23, %[stv]\n"
"v_mfma_f32_16x16x32_bf16 v[196:199], a[236:239], a[168:171], v[196:199]\n"
"ds_read_b64_tr_b16 a[228:229], v237 offset:0\n"
"v_mfma_f32_16x16x32_bf16 v[200:203], a[236:239], a[172:175], v[200:203]\n"
"ds_read_b64_tr_b16 a[230:231], v237 offset:2048\n"
"v_mfma_f32_16x16x32_bf16 v[204:207], a[236:239], a[176:179], v[204:207]\n"
"s_waitcnt lgkmcnt(6)\n"
"v_mfma_f32_16x16x32_bf16 v[160:163], a[216:219], a[180:183], v[160:163]\n"
"ds_read_b64_tr_b16 a[232:233], v237 offset:4096\n"
"s_mov_b32 s59, %[so0]\n"
"v_mfma_f32_16x16x32_bf16 v[164:167], a[216:219], a[184:187], v[164:167]\n"
"ds_read_b64_tr_b16 a[234:235], v237 offset:6144\n"
"buffer_load_dwordx4 v[16:19], v253, %[rWs], s59 offen sc1\n"
"s_add_u32 s59, s59, 122880\n"
"v_mfma_f32_16x16x32_bf16 v[168:171], a[216:219], a[188:191], v[168:171]\n"
"buffer_load_dwordx4 v[20:23], v253, %[rWs], s59 offen sc1\n"
"s_add_u32 s59, s59, 122880\n"
"s_waitcnt lgkmcnt(6)\n"
"v_mfma_f32_16x16x32_bf16 v[160:163], a[220:223], a[192:195], v[160:163]\n"
"ds_read_b64_tr_b16 a[236:237], v237 offset:8192\n"
"buffer_load_dwordx4 v[24:27], v253, %[rWs], s59 offen sc1\n"
"v_mfma_f32_16x16x32_bf16 v[164:167], a[220:223], a[196:199], v[164:167]\n"
"ds_read_b64_tr_b16 a[238:239], v237 offset:10240\n"
"s_add_u32 s59, s59, 122880\n"
"v_mfma_f32_16x16x32_bf16 v[168:171], a[220:223], a[200:203], v[168:171]\n"
"buffer_load_dwordx4 v[28:31], v253, %[rWs], s59 offen sc1\n"
"s_add_u32 s59, s59, 122880\n"
"s_waitcnt lgkmcnt(6)\n"
"v_mfma_f32_16x16x32_bf16 v[160:163], a[224:227], a[204:207], v[160:163]\n"
"ds_read_b64_tr_b16 a[216:217], v238 offset:0\n"
"buffer_load_dwordx4 v[32:35], v253, %[rWs], s59 offen sc1\n"
"v_mfma_f32_16x16x32_bf16 v[164:167], a[224:227], a[208:211], v[164:167]\n"
"ds_read_b64_tr_b16 a[218:219], v238 offset:2048\n"
"s_add_u32 s59, s59, 122880\n"
"v_mfma_f32_16x16x32_bf16 v[168:171], a[224:227], a[212:215], v[168:171]\n"
"buffer_load_dwordx4 v[36:39], v253, %[rWs], s59 offen sc1\n"
"s_add_u32 s59, s59, 122880\n"
"s_waitcnt lgkmcnt(6)\n"
"v_mfma_f32_16x16x32_bf16 v[172:175], a[228:231], a[180:183], v[172:175]\n"
"ds_read_b64_tr_b16 a[220:221], v238 offset:4096\n"
"buffer_load_dwordx4 v[40:43], v253, %[rWs], s59 offen sc1\n"
"v_mfma_f32_16x16x32_bf16 v[176:179], a[228:231], a[184:187], v[176:179]\n"
"ds_read_b64_tr_b16 a[222:223], v238 offset:6144\n"
"s_add_u32 s59, s59, 122880\n"
"buffer_load_dwordx4 v[44:47], v253, %[rWs], s59 offen sc1\n"
"v_mfma_f32_16x16x32_bf16 v[180:183], a[228:231], a[188:191], v[180:183]\n"
"s_add_u32 s59, s59, 122880\n"
"s_mov_b32 s59, %[so1]\n"
"s_waitcnt lgkmcnt(6)\n"
"v_mfma_f32_16x16x32_bf16 v[172:175], a[232:235], a[192:195], v[172:175]\n"
"ds_read_b64_tr_b16 a[224:225], v238 offset:8192\n"
"buffer_load_dwordx4 v[48:51], v253, %[rWs], s59 offen sc1\n"
"v_mfma_f32_16x16x32_bf16 v[176:179], a[232:235], a[196:199], v[176:179]\n"
"ds_read_b64_tr_b16 a[226:227], v238 offset:10240\n"
"s_add_u32 s59, s59, 122880\n"
"v_mfma_f32_16x16x32_bf16 v[180:183], a[232:235], a[200:203], v[180:183]\n"
"buffer_load_dwordx4 v[52:55], v253, %[rWs], s59 offen sc1\n"
"s_add_u32 s59, s59, 122880\n"
"s_waitcnt lgkmcnt(6)\n"
"v_mfma_f32_16x16x32_bf16 v[172:175], a[236:239], a[204:207], v[172:175]\n"
"ds_read_b64_tr_b16 a[228:229], v239 offset:0\n"
"buffer_load_dwordx4 v[56:59], v253, %[rWs], s59 offen sc1\n"
"v_mfma_f32_16x16x32_bf16 v[176:179], a[236:239], a[208:211], v[176:179]\n"
"ds_read_b64_tr_b16 a[230:231], v239 offset:2048\n"
"s_add_u32 s59, s59, 122880\n"
"buffer_load_dwordx4 v[60:63], v253, %[rWs], s59 offen sc1\n"
"v_mfma_f32_16x16x32_bf16 v[180:183], a[236:239], a[212:215], v[180:183]\n"
"s_add_u32 s59, s59, 122880\n"
"buffer_load_dwordx4 v[64:67], v253, %[rWs], s59 offen sc1\n"
"s_waitcnt lgkmcnt(6)\n"
"v_mfma_f32_16x16x32_bf16 v[184:187], a[216:219], a[180:183], v[184:187]\n"
"ds_read_b64_tr_b16 a[232:233], v239 offset:4096\n"
"s_add_u32 s59, s59, 122880\n"
"v_mfma_f32_16x16x32_bf16 v[188:191], a[216:219], a[184:187], v[188:191]\n"
"ds_read_b64_tr_b16 a[234:235], v239 offset:6144\n"
"buffer_load_dwordx4 v[68:71], v253, %[rWs], s59 offen sc1\n"
"v_mfma_f32_16x16x32_bf16 v[192:195], a[216:219], a[188:191], v[192:195]\n"
"s_add_u32 s59, s59, 122880\n"
"buffer_load_dwordx4 v[72:75], v253, %[rWs], s59 offen sc1\n"
"s_waitcnt lgkmcnt(6)\n"
"v_mfma_f32_16x16x32_bf16 v[184:187], a[220:223], a[192:195], v[184:187]\n"
"ds_read_b64_tr_b16 a[236:237], v239 offset:8192\n"
"s_add_u32 s59, s59, 122880\n"
"v_mfma_f32_16x16x32_bf16 v[188:191], a[220:223], a[196:199], v[188:191]\n"
"ds_read_b64_tr_b16 a[238:239], v239 offset:10240\n"
"buffer_load_dwordx4 v[76:79], v253, %[rWs], s59 offen sc1\n"
"v_mfma_f32_16x16x32_bf16 v[192:195], a[220:223], a[200:203], v[192:195]\n"
"s_add_u32 s59, s59, 122880\n"
"s_waitcnt lgkmcnt(6)\n"
"v_mfma_f32_16x16x32_bf16 v[184:187], a[224:227], a[204:207], v[184:187]\n"
"s_add_u32 s61, %[m0b], s60\n"
"s_add_u32 s60, s60, 12288\n"
"s_cmp_eq_u32 s60, 61440\n"
"s_cselect_b32 s60, 0, s60\n"
"v_add_u32 v236, s60, %[rdO]\n"
"v_mfma_f32_16x16x32_bf16 v[188:191], a[224:227], a[208:211], v[188:191]\n"
"v_xor_b32 v237, 32, v236\n"
"v_xor_b32 v238, 64, v236\n"
"v_xor_b32 v239, 96, v236\n"
"v_mfma_f32_16x16x32_bf16 v[192:195], a[224:227], a[212:215], v[192:195]\n"
"s_waitcnt vmcnt(23) lgkmcnt(0)\n"
"s_barrier\n"
"ds_read_b64_tr_b16 a[216:217], v236 offset:0\n"
"ds_read_b64_tr_b16 a[218:219], v236 offset:2048\n"
"v_mfma_f32_16x16x32_bf16 v[196:199], a[228:231], a[180:183], v[196:199]\n"
"ds_read_b64_tr_b16 a[220:221], v236 offset:4096\n"
"v_mfma_f32_16x16x32_bf16 v[200:203], a[228:231], a[184:187], v[200:203]\n"
"ds_read_b64_tr_b16 a[222:223], v236 offset:6144\n"
"v_mfma_f32_16x16x32_bf16 v[204:207], a[228:231], a[188:191], v[204:207]\n"
"v_mfma_f32_16x16x32_bf16 v[196:199], a[232:235], a[192:195], v[196:199]\n"
"ds_read_b64_tr_b16 a[224:225], v236 offset:8192\n"
"v_mfma_f32_16x16x32_bf16 v[200:203], a[232:235], a[196:199], v[200:203]\n"
"ds_read_b64_tr_b16 a[226:227], v236 offset:10240\n"
"v_mfma_f32_16x16x32_bf16 v[204:207], a[232:235], a[200:203], v[204:207]\n"
"v_mfma_f32_16x16x32_bf16 v[196:199], a[236:239], a[204:207], v[196:199]\n"
"ds_read_b64_tr_b16 a[228:229], v237 offset:0\n"
"v_mfma_f32_16x16x32_bf16 v[200:203], a[236:239], a[208:211], v[200:203]\n"
"ds_read_b64_tr_b16 a[230:231], v237 offset:2048\n"
"v_mfma_f32_16x16x32_bf16 v[204:207], a[236:239], a[212:215], v[204:207]\n"
"s_waitcnt lgkmcnt(6)\n"
"v_mfma_f32_16x16x32_bf16 v[160:163], a[216:219], a[0:3], v[160:163]\n"
"ds_read_b64_tr_b16 a[232:233], v237 offset:4096\n"
"v_mfma_f32_16x16x32_bf16 v[164:167], a[216:219], a[4:7], v[164:167]\n"
"ds_read_b64_tr_b16 a[234:235], v237 offset:6144\n"
"v_mfma_f32_16x16x32_bf16 v[168:171], a[216:219], a[8:11], v[168:171]\n"
"s_waitcnt lgkmcnt(6)\n"
"v_mfma_f32_16x16x32_bf16 v[160:163], a[220:223], a[12:15], v[160:163]\n"
"ds_read_b64_tr_b16 a[236:237], v237 offset:8192\n"
"v_mfma_f32_16x16x32_bf16 v[164:167], a[220:223], a[16:19], v[164:167]\n"
"ds_read_b64_tr_b16 a[238:239], v237 offset:10240\n"
"v_mfma_f32_16x16x32_bf16 v[168:171], a[220:223], a[20:23], v[168:171]\n"
"s_waitcnt lgkmcnt(6)\n"
"v_mfma_f32_16x16x32_bf16 v[160:163], a[224:227], a[24:27], v[160:163]\n"
"ds_read_b64_tr_b16 a[216:217], v238 offset:0\n"
"v_mfma_f32_16x16x32_bf16 v[164:167], a[224:227], a[28:31], v[164:167]\n"
"ds_read_b64_tr_b16 a[218:219], v238 offset:2048\n"
"v_mfma_f32_16x16x32_bf16 v[168:171], a[224:227], a[32:35], v[168:171]\n"
"s_waitcnt lgkmcnt(6)\n"
"v_mfma_f32_16x16x32_bf16 v[172:175], a[228:231], a[0:3], v[172:175]\n"
"ds_read_b64_tr_b16 a[220:221], v238 offset:4096\n"
"v_mfma_f32_16x16x32_bf16 v[176:179], a[228:231], a[4:7], v[176:179]\n"
"ds_read_b64_tr_b16 a[222:223], v238 offset:6144\n"
"v_mfma_f32_16x16x32_bf16 v[180:183], a[228:231], a[8:11], v[180:183]\n"
"s_waitcnt lgkmcnt(6)\n"
"v_mfma_f32_16x16x32_bf16 v[172:175], a[232:235], a[12:15], v[172:175]\n"
"ds_read_b64_tr_b16 a[224:225], v238 offset:8192\n"
"v_mfma_f32_16x16x32_bf16 v[176:179], a[232:235], a[16:19], v[176:179]\n"
"ds_read_b64_tr_b16 a[226:227], v238 offset:10240\n"
"v_mfma_f32_16x16x32_bf16 v[180:183], a[232:235], a[20:23], v[180:183]\n"
"s_waitcnt lgkmcnt(6)\n"
"v_mfma_f32_16x16x32_bf16 v[172:175], a[236:239], a[24:27], v[172:175]\n"
"ds_read_b64_tr_b16 a[228:229], v239 offset:0\n"
"v_mfma_f32_16x16x32_bf16 v[176:179], a[236:239], a[28:31], v[176:179]\n"
"ds_read_b64_tr_b16 a[230:231], v239 offset:2048\n"
"v_mfma_f32_16x16x32_bf16 v[180:183], a[236:239], a[32:35], v[180:183]\n"
"s_waitcnt lgkmcnt(6)\n"
"v_mfma_f32_16x16x32_bf16 v[184:187], a[216:219], a[0:3], v[184:187]\n"
"ds_read_b64_tr_b16 a[232:233], v239 offset:4096\n"
"v_mfma_f32_16x16x32_bf16 v[188:191], a[216:219], a[4:7], v[188:191]\n"
"ds_read_b64_tr_b16 a[234:235], v239 offset:6144\n"
"v_mfma_f32_16x16x32_bf16 v[192:195], a[216:219], a[8:11], v[192:195]\n"
"s_waitcnt lgkmcnt(6)\n"
"v_mfma_f32_16x16x32_bf16 v[184:187], a[220:223], a[12:15], v[184:187]\n"
"ds_read_b64_tr_b16 a[236:237], v239 offset:8192\n"
"v_mfma_f32_16x16x32_bf16 v[188:191], a[220:223], a[16:19], v[188:191]\n"
"ds_read_b64_tr_b16 a[238:239], v239 offset:10240\n"
"v_mfma_f32_16x16x32_bf16 v[192:195], a[220:223], a[20:23], v[192:195]\n"
"s_waitcnt lgkmcnt(6)\n"
"v_mfma_f32_16x16x32_bf16 v[184:187], a[224:227], a[24:27], v[184:187]\n"
"s_add_u32 s61, %[m0b], s60\n"
"s_add_u32 s60, s60, 12288\n"
"s_cmp_eq_u32 s60, 61440\n"
"s_cselect_b32 s60, 0, s60\n"
"v_add_u32 v236, s60, %[rdO]\n"
"v_mfma_f32_16x16x32_bf16 v[188:191], a[224:227], a[28:31], v[188:191]\n"
"v_xor_b32 v237, 32, v236\n"
"v_xor_b32 v238, 64, v236\n"
"v_xor_b32 v239, 96, v236\n"
"v_mfma_f32_16x16x32_bf16 v[192:195], a[224:227], a[32:35], v[192:195]\n"
"s_waitcnt vmcnt(17) lgkmcnt(0)\n"
"s_barrier\n"
"ds_read_b64_tr_b16 a[216:217], v236 offset:0\n"
"ds_read_b64_tr_b16 a[218:219], v236 offset:2048\n"
"v_mfma_f32_16x16x32_bf16 v[196:199], a[228:231], a[0:3], v[196:199]\n"
"ds_read_b64_tr_b16 a[220:221], v236 offset:4096\n"
"v_mfma_f32_16x16x32_bf16 v[200:203], a[228:231], a[4:7], v[200:203]\n"
"ds_read_b64_tr_b16 a[222:223], v236 offset:6144\n"
"v_mfma_f32_16x16x32_bf16 v[204:207], a[228:231], a[8:11], v[204:207]\n"
"v_mfma_f32_16x16x32_bf16 v[196:199], a[232:235], a[12:15], v[196:199]\n"
"ds_read_b64_tr_b16 a[224:225], v236 offset:8192\n"
"v_mfma_f32_16x16x32_bf16 v[200:203], a[232:235], a[16:19], v[200:203]\n"
"ds_read_b64_tr_b16 a[226:227], v236 offset:10240\n"
"v_mfma_f32_16x16x32_bf16 v[204:207], a[232:235], a[20:23], v[204:207]\n"
"v_mfma_f32_16x16x32_bf16 v[196:199], a[236:239], a[24:27], v[196:199]\n"
"ds_read_b64_tr_b16 a[228:229], v237 offset:0\n"
"v_mfma_f32_16x16x32_bf16 v[200:203], a[236:239], a[28:31], v[200:203]\n"
"ds_read_b64_tr_b16 a[230:231], v237 offset:2048\n"
"v_mfma_f32_16x16x32_bf16 v[204:207], a[236:239], a[32:35], v[204:207]\n"
"s_waitcnt lgkmcnt(6)\n"
"v_mfma_f32_16x16x32_bf16 v[160:163], a[216:219], a[36:39], v[160:163]\n"
"ds_read_b64_tr_b16 a[232:233], v237 offset:4096\n"
"v_mfma_f32_16x16x32_bf16 v[164:167], a[216:219], a[40:43], v[164:167]\n"
"ds_read_b64_tr_b16 a[234:235], v237 offset:6144\n"
"v_mfma_f32_16x16x32_bf16 v[168:171], a[216:219], a[44:47], v[168:171]\n"
"s_waitcnt lgkmcnt(6)\n"
"v_mfma_f32_16x16x32_bf16 v[160:163], a[220:223], a[48:51], v[160:163]\n"
"ds_read_b64_tr_b16 a[236:237], v237 offset:8192\n"
"v_mfma_f32_16x16x32_bf16 v[164:167], a[220:223], a[52:55], v[164:167]\n"
"ds_read_b64_tr_b16 a[238:239], v237 offset:10240\n"
"v_mfma_f32_16x16x32_bf16 v[168:171], a[220:223], a[56:59], v[168:171]\n"
"s_waitcnt lgkmcnt(6)\n"
"v_mfma_f32_16x16x32_bf16 v[160:163], a[224:227], a[60:63], v[160:163]\n"
"ds_read_b64_tr_b16 a[216:217], v238 offset:0\n"
"v_mfma_f32_16x16x32_bf16 v[164:167], a[224:227], a[64:67], v[164:167]\n"
"ds_read_b64_tr_b16 a[218:219], v238 offset:2048\n"
"v_mfma_f32_16x16x32_bf16 v[168:171], a[224:227], a[68:71], v[168:171]\n"
"s_waitcnt lgkmcnt(6)\n"
"v_mfma_f32_16x16x32_bf16 v[172:175], a[228:231], a[36:39], v[172:175]\n"
"ds_read_b64_tr_b16 a[220:221], v238 offset:4096\n"
"v_mfma_f32_16x16x32_bf16 v[176:179], a[228:231], a[40:43], v[176:179]\n"
"ds_read_b64_tr_b16 a[222:223], v238 offset:6144\n"
"v_mfma_f32_16x16x32_bf16 v[180:183], a[228:231], a[44:47], v[180:183]\n"
"s_waitcnt lgkmcnt(6)\n"
"v_mfma_f32_16x16x32_bf16 v[172:175], a[232:235], a[48:51], v[172:175]\n"
"ds_read_b64_tr_b16 a[224:225], v238 offset:8192\n"
"v_cvt_pk_bf16_f32 v208, v160, v161\n"
"v_mfma_f32_16x16x32_bf16 v[176:179], a[232:235], a[52:55], v[176:179]\n"
"ds_read_b64_tr_b16 a[226:227], v238 offset:10240\n"
"v_mfma_f32_16x16x32_bf16 v[180:183], a[232:235], a[56:59], v[180:183]\n"
"v_cvt_pk_bf16_f32 v209, v162, v163\n"
"v_cvt_pk_bf16_f32 v212, v164, v165\n"
"s_waitcnt lgkmcnt(6)\n"
"v_mfma_f32_16x16x32_bf16 v[172:175], a[236:239], a[60:63], v[172:175]\n"
"ds_read_b64_tr_b16 a[228:229], v239 offset:0\n"
"v_cvt_pk_bf16_f32 v213, v166, v167\n"
"v_mfma_f32_16x16x32_bf16 v[176:179], a[236:239], a[64:67], v[176:179]\n"
"ds_read_b64_tr_b16 a[230:231], v239 offset:2048\n"
"v_mfma_f32_16x16x32_bf16 v[180:183], a[236:239], a[68:71], v[180:183]\n"
"v_cvt_pk_bf16_f32 v216, v168, v169\n"
"v_cvt_pk_bf16_f32 v217, v170, v171\n"
"s_waitcnt lgkmcnt(6)\n"
"v_mfma_f32_16x16x32_bf16 v[184:187], a[216:219], a[36:39], v[184:187]\n"
"ds_read_b64_tr_b16 a[232:233], v239 offset:4096\n"
"v_mfma_f32_16x16x32_bf16 v[188:191], a[216:219], a[40:43], v[188:191]\n"
"ds_read_b64_tr_b16 a[234:235], v239 offset:6144\n"
"v_mfma_f32_16x16x32_bf16 v[192:195], a[216:219], a[44:47], v[192:195]\n"
"s_waitcnt lgkmcnt(6)\n"
"v_mfma_f32_16x16x32_bf16 v[184:187], a[220:223], a[48:51], v[184:187]\n"
"ds_read_b64_tr_b16 a[236:237], v239 offset:8192\n"
"v_cvt_pk_bf16_f32 v210, v172, v173\n"
"v_mfma_f32_16x16x32_bf16 v[188:191], a[220:223], a[52:55], v[188:191]\n"
"ds_read_b64_tr_b16 a[238:239], v239 offset:10240\n"
"v_mfma_f32_16x16x32_bf16 v[192:195], a[220:223], a[56:59], v[192:195]\n"
"v_cvt_pk_bf16_f32 v211, v174, v175\n"
"v_cvt_pk_bf16_f32 v214, v176, v177\n"
"s_waitcnt lgkmcnt(6)\n"
"v_mfma_f32_16x16x32_bf16 v[184:187], a[224:227], a[60:63], v[184:187]\n"
"v_cvt_pk_bf16_f32 v215, v178, v179\n"
"v_mfma_f32_16x16x32_bf16 v[188:191], a[224:227], a[64:67], v[188:191]\n"
"v_cvt_pk_bf16_f32 v218, v180, v181\n"
"v_cvt_pk_bf16_f32 v219, v182, v183\n"
"v_mfma_f32_16x16x32_bf16 v[192:195], a[224:227], a[68:71], v[192:195]\n"
"s_waitcnt lgkmcnt(4)\n"
"v_mfma_f32_16x16x32_bf16 v[196:199], a[228:231], a[36:39], v[196:199]\n"
"v_mfma_f32_16x16x32_bf16 v[200:203], a[228:231], a[40:43], v[200:203]\n"
"v_mfma_f32_16x16x32_bf16 v[204:207], a[228:231], a[44:47], v[204:207]\n"
"buffer_store_dwordx4 v[208:211], %[stv], %[rWs], s73 offen offset:0 sc1\n"
"s_waitcnt lgkmcnt(2)\n"
"v_mfma_f32_16x16x32_bf16 v[196:199], a[232:235], a[48:51], v[196:199]\n"
"buffer_store_dwordx4 v[212:215], %[stv], %[rWs], s73 offen offset:1024 sc1\n"
"v_mfma_f32_16x16x32_bf16 v[200:203], a[232:235], a[52:55], v[200:203]\n"
"buffer_store_dwordx4 v[216:219], %[stv], %[rWs], s73 offen offset:2048 sc1\n"
"v_mfma_f32_16x16x32_bf16 v[204:207], a[232:235], a[56:59], v[204:207]\n"
"s_add_u32 s73, s73, 3072\n"
"v_cvt_pk_bf16_f32 v220, v184, v185\n"
"s_waitcnt lgkmcnt(0)\n"
"v_mfma_f32_16x16x32_bf16 v[196:199], a[236:239], a[60:63], v[196:199]\n"
"v_cvt_pk_bf16_f32 v221, v186, v187\n"
"v_cvt_pk_bf16_f32 v224, v188, v189\n"
"v_mfma_f32_16x16x32_bf16 v[200:203], a[236:239], a[64:67], v[200:203]\n"
"v_cvt_pk_bf16_f32 v225, v190, v191\n"
"v_mfma_f32_16x16x32_bf16 v[204:207], a[236:239], a[68:71], v[204:207]\n"
"v_cvt_pk_bf16_f32 v228, v192, v193\n"
"v_cvt_pk_bf16_f32 v229, v194, v195\n"
"s_nop 0\n"
"v_cvt_pk_bf16_f32 v222, v196, v197\n"
"v_cvt_pk_bf16_f32 v223, v198, v199\n"
"s_nop 0\n"
"v_cvt_pk_bf16_f32 v226, v200, v201\n"
"v_cvt_pk_bf16_f32 v227, v202, v203\n"
"v_cvt_pk_bf16_f32 v230, v204, v205\n"
"v_cvt_pk_bf16_f32 v231, v206, v207\n"
"buffer_store_dwordx4 v[220:223], %[stv], %[rWs], s73 offen offset:0 sc1\n"
"buffer_store_dwordx4 v[224:227], %[stv], %[rWs], s73 offen offset:1024 sc1\n"
"buffer_store_dwordx4 v[228:231], %[stv], %[rWs], s73 offen offset:2048 sc1\n"
"s_add_u32 s73, s73, 3072\n"
"s_cmp_eq_u32 s73, s58\n"
"s_cselect_b32 s73, %[st0], s73\n"
"buffer_load_dword %[rdK], v254, %[rWs], %[flg] offen sc1\n"
"s_waitcnt vmcnt(7) lgkmcnt(0)\n"
"v_lshlrev_b32 v88, 16, v16\n"
"v_and_b32 v89, s80, v16\n"
"v_lshlrev_b32 v90, 16, v17\n"
"v_and_b32 v91, s80, v17\n"
"v_lshlrev_b32 v92, 16, v18\n"
"v_and_b32 v93, s80, v18\n"
"v_lshlrev_b32 v94, 16, v19\n"
"v_and_b32 v95, s80, v19\n"
"v_lshlrev_b32 v104, 16, v20\n"
"v_and_b32 v105, s80, v20\n"
"v_lshlrev_b32 v106, 16, v21\n"
"v_and_b32 v107, s80, v21\n"
"v_lshlrev_b32 v108, 16, v22\n"
"v_and_b32 v109, s80, v22\n"
"v_lshlrev_b32 v110, 16, v23\n"
"v_and_b32 v111, s80, v23\n"
"v_pk_add_f32 v[88:89], v[88:89], v[104:105]\n"
"v_pk_add_f32 v[90:91], v[90:91], v[106:107]\n"
"v_pk_add_f32 v[92:93], v[92:93], v[108:109]\n"
"v_pk_add_f32 v[94:95], v[94:95], v[110:111]\n"
"v_lshlrev_b32 v104, 16, v24\n"
"v_and_b32 v105, s80, v24\n"
"v_lshlrev_b32 v106, 16, v25\n"
"v_and_b32 v107, s80, v25\n"
"v_lshlrev_b32 v108, 16, v26\n"
"v_and_b32 v109, s80, v26\n"
"v_lshlrev_b32 v110, 16, v27\n"
"v_and_b32 v111, s80, v27\n"
"v_pk_add_f32 v[88:89], v[88:89], v[104:105]\n"
"v_pk_add_f32 v[90:91], v[90:91], v[106:107]\n"
"v_pk_add_f32 v[92:93], v[92:93], v[108:109]\n"
"v_pk_add_f32 v[94:95], v[94:95], v[110:111]\n"
"v_lshlrev_b32 v104, 16, v28\n"
"v_and_b32 v105, s80, v28\n"
"v_lshlrev_b32 v106, 16, v29\n"
"v_and_b32 v107, s80, v29\n"
"v_lshlrev_b32 v108, 16, v30\n"
"v_and_b32 v109, s80, v30\n"
"v_lshlrev_b32 v110, 16, v31\n"
"v_and_b32 v111, s80, v31\n"
"v_pk_add_f32 v[88:89], v[88:89], v[104:105]\n"
"v_pk_add_f32 v[90:91], v[90:91], v[106:107]\n"
"v_pk_add_f32 v[92:93], v[92:93], v[108:109]\n"
"v_pk_add_f32 v[94:95], v[94:95], v[110:111]\n"
"v_lshlrev_b32 v104, 16, v32\n"
"v_and_b32 v105, s80, v32\n"
"v_lshlrev_b32 v106, 16, v33\n"
"v_and_b32 v107, s80, v33\n"
"v_lshlrev_b32 v108, 16, v34\n"
"v_and_b32 v109, s80, v34\n"
"v_lshlrev_b32 v110, 16, v35\n"
"v_and_b32 v111, s80, v35\n"
"v_pk_add_f32 v[88:89], v[88:89], v[104:105]\n"
"v_pk_add_f32 v[90:91], v[90:91], v[106:107]\n"
"v_pk_add_f32 v[92:93], v[92:93], v[108:109]\n"
"v_pk_add_f32 v[94:95], v[94:95], v[110:111]\n"
"v_lshlrev_b32 v104, 16, v36\n"
"v_and_b32 v105, s80, v36\n"
"v_lshlrev_b32 v106, 16, v37\n"
"v_and_b32 v107, s80, v37\n"
"v_lshlrev_b32 v108, 16, v38\n"
"v_and_b32 v109, s80, v38\n"
"v_lshlrev_b32 v110, 16, v39\n"
"v_and_b32 v111, s80, v39\n"
"v_pk_add_f32 v[88:89], v[88:89], v[104:105]\n"
"v_pk_add_f32 v[90:91], v[90:91], v[106:107]\n"
"v_pk_add_f32 v[92:93], v[92:93], v[108:109]\n"
"v_pk_add_f32 v[94:95], v[94:95], v[110:111]\n"
"v_lshlrev_b32 v104, 16, v40\n"
"v_and_b32 v105, s80, v40\n"
"v_lshlrev_b32 v106, 16, v41\n"
"v_and_b32 v107, s80, v41\n"
"v_lshlrev_b32 v108, 16, v42\n"
"v_and_b32 v109, s80, v42\n"
"v_lshlrev_b32 v110, 16, v43\n"
"v_and_b32 v111, s80, v43\n"
"v_pk_add_f32 v[88:89], v[88:89], v[104:105]\n"
"v_pk_add_f32 v[90:91], v[90:91], v[106:107]\n"
"v_pk_add_f32 v[92:93], v[92:93], v[108:109]\n"
"v_pk_add_f32 v[94:95], v[94:95], v[110:111]\n"
"v_lshlrev_b32 v104, 16, v44\n"
"v_and_b32 v105, s80, v44\n"
"v_lshlrev_b32 v106, 16, v45\n"
"v_and_b32 v107, s80, v45\n"
"v_lshlrev_b32 v108, 16, v46\n"
"v_and_b32 v109, s80, v46\n"
"v_lshlrev_b32 v110, 16, v47\n"
"v_and_b32 v111, s80, v47\n"
"v_pk_add_f32 v[88:89], v[88:89], v[104:105]\n"
"v_pk_add_f32 v[90:91], v[90:91], v[106:107]\n"
"v_pk_add_f32 v[92:93], v[92:93], v[108:109]\n"
"v_pk_add_f32 v[94:95], v[94:95], v[110:111]\n"
"v_cvt_pk_bf16_f32 %[dKV0], v88, v89\n"
"v_cvt_pk_bf16_f32 %[dKV1], v90, v91\n"
"v_cvt_pk_bf16_f32 %[dKV2], v92, v93\n"
"v_cvt_pk_bf16_f32 %[dW0], v94, v95\n"
"v_lshlrev_b32 v96, 16, v48\n"
"v_and_b32 v97, s80, v48\n"
"v_lshlrev_b32 v98, 16, v49\n"
"v_and_b32 v99, s80, v49\n"
"v_lshlrev_b32 v100, 16, v50\n"
"v_and_b32 v101, s80, v50\n"
"v_lshlrev_b32 v102, 16, v51\n"
"v_and_b32 v103, s80, v51\n"
"v_lshlrev_b32 v104, 16, v52\n"
"v_and_b32 v105, s80, v52\n"
"v_lshlrev_b32 v106, 16, v53\n"
"v_and_b32 v107, s80, v53\n"
"v_lshlrev_b32 v108, 16, v54\n"
"v_and_b32 v109, s80, v54\n"
"v_lshlrev_b32 v110, 16, v55\n"
"v_and_b32 v111, s80, v55\n"
"v_pk_add_f32 v[96:97], v[96:97], v[104:105]\n"
"v_pk_add_f32 v[98:99], v[98:99], v[106:107]\n"
"v_pk_add_f32 v[100:101], v[100:101], v[108:109]\n"
"v_pk_add_f32 v[102:103], v[102:103], v[110:111]\n"
"v_lshlrev_b32 v104, 16, v56\n"
"v_and_b32 v105, s80, v56\n"
"v_lshlrev_b32 v106, 16, v57\n"
"v_and_b32 v107, s80, v57\n"
"v_lshlrev_b32 v108, 16, v58\n"
"v_and_b32 v109, s80, v58\n"
"v_lshlrev_b32 v110, 16, v59\n"
"v_and_b32 v111, s80, v59\n"
"v_pk_add_f32 v[96:97], v[96:97], v[104:105]\n"
"v_pk_add_f32 v[98:99], v[98:99], v[106:107]\n"
"v_pk_add_f32 v[100:101], v[100:101], v[108:109]\n"
"v_pk_add_f32 v[102:103], v[102:103], v[110:111]\n"
"v_lshlrev_b32 v104, 16, v60\n"
"v_and_b32 v105, s80, v60\n"
"v_lshlrev_b32 v106, 16, v61\n"
"v_and_b32 v107, s80, v61\n"
"v_lshlrev_b32 v108, 16, v62\n"
"v_and_b32 v109, s80, v62\n"
"v_lshlrev_b32 v110, 16, v63\n"
"v_and_b32 v111, s80, v63\n"
"v_pk_add_f32 v[96:97], v[96:97], v[104:105]\n"
"v_pk_add_f32 v[98:99], v[98:99], v[106:107]\n"
"v_pk_add_f32 v[100:101], v[100:101], v[108:109]\n"
"v_pk_add_f32 v[102:103], v[102:103], v[110:111]\n"
"v_lshlrev_b32 v104, 16, v64\n"
"v_and_b32 v105, s80, v64\n"
"v_lshlrev_b32 v106, 16, v65\n"
"v_and_b32 v107, s80, v65\n"
"v_lshlrev_b32 v108, 16, v66\n"
"v_and_b32 v109, s80, v66\n"
"v_lshlrev_b32 v110, 16, v67\n"
"v_and_b32 v111, s80, v67\n"
"v_pk_add_f32 v[96:97], v[96:97], v[104:105]\n"
"v_pk_add_f32 v[98:99], v[98:99], v[106:107]\n"
"v_pk_add_f32 v[100:101], v[100:101], v[108:109]\n"
"v_pk_add_f32 v[102:103], v[102:103], v[110:111]\n"
"v_lshlrev_b32 v104, 16, v68\n"
"v_and_b32 v105, s80, v68\n"
"v_lshlrev_b32 v106, 16, v69\n"
"v_and_b32 v107, s80, v69\n"
"v_lshlrev_b32 v108, 16, v70\n"
"v_and_b32 v109, s80, v70\n"
"v_lshlrev_b32 v110, 16, v71\n"
"v_and_b32 v111, s80, v71\n"
"v_pk_add_f32 v[96:97], v[96:97], v[104:105]\n"
"v_pk_add_f32 v[98:99], v[98:99], v[106:107]\n"
"v_pk_add_f32 v[100:101], v[100:101], v[108:109]\n"
"v_pk_add_f32 v[102:103], v[102:103], v[110:111]\n"
"v_lshlrev_b32 v104, 16, v72\n"
"v_and_b32 v105, s80, v72\n"
"v_lshlrev_b32 v106, 16, v73\n"
"v_and_b32 v107, s80, v73\n"
"v_lshlrev_b32 v108, 16, v74\n"
"v_and_b32 v109, s80, v74\n"
"v_lshlrev_b32 v110, 16, v75\n"
"v_and_b32 v111, s80, v75\n"
"v_pk_add_f32 v[96:97], v[96:97], v[104:105]\n"
"v_pk_add_f32 v[98:99], v[98:99], v[106:107]\n"
"v_pk_add_f32 v[100:101], v[100:101], v[108:109]\n"
"v_pk_add_f32 v[102:103], v[102:103], v[110:111]\n"
"v_lshlrev_b32 v104, 16, v76\n"
"v_and_b32 v105, s80, v76\n"
"v_lshlrev_b32 v106, 16, v77\n"
"v_and_b32 v107, s80, v77\n"
"v_lshlrev_b32 v108, 16, v78\n"
"v_and_b32 v109, s80, v78\n"
"v_lshlrev_b32 v110, 16, v79\n"
"v_and_b32 v111, s80, v79\n"
"v_pk_add_f32 v[96:97], v[96:97], v[104:105]\n"
"v_pk_add_f32 v[98:99], v[98:99], v[106:107]\n"
"v_pk_add_f32 v[100:101], v[100:101], v[108:109]\n"
"v_pk_add_f32 v[102:103], v[102:103], v[110:111]\n"
"v_cvt_pk_bf16_f32 %[dW1], v96, v97\n"
"v_cvt_pk_bf16_f32 %[dW2], v98, v99\n"
"v_cvt_pk_bf16_f32 %[rdV], v100, v101\n"
"v_cvt_pk_bf16_f32 %[rdW], v102, v103\n"
"s_waitcnt vmcnt(0) lgkmcnt(0)\n"
"s_branch Lend_%=\n"
"Lslow_%=:\n"
"v_max3_f32 v244, v160, v161, v162\n"
"v_max3_f32 v244, v244, v163, v172\n"
"v_max3_f32 v244, v244, v173, v174\n"
"v_max3_f32 v244, v244, v175, s71\n"
"v_mov_b32 v242, v244\n"
"s_nop 1\n"
"v_permlane32_swap_b32 v244, v242\n"
"s_nop 1\n"
"v_max_f32 v244, v244, v242\n"
"v_mov_b32 v242, v244\n"
"s_nop 1\n"
"v_permlane16_swap_b32 v244, v242\n"
"s_nop 1\n"
"v_max_f32 v244, v244, v242\n"
"v_sub_f32 v160, v160, v244\n"
"v_sub_f32 v161, v161, v244\n"
"v_sub_f32 v162, v162, v244\n"
"v_sub_f32 v163, v163, v244\n"
"v_sub_f32 v172, v172, v244\n"
"v_sub_f32 v173, v173, v244\n"
"v_sub_f32 v174, v174, v244\n"
"v_sub_f32 v175, v175, v244\n"
"v_sub_f32 v196, v196, v244\n"
"v_sub_f32 v197, v197, v244\n"
"v_sub_f32 v198, v198, v244\n"
"v_sub_f32 v199, v199, v244\n"
"v_max3_f32 v245, v164, v165, v166\n"
"v_max3_f32 v245, v245, v167, v176\n"
"v_max3_f32 v245, v245, v177, v178\n"
"v_max3_f32 v245, v245, v179, s71\n"
"v_mov_b32 v242, v245\n"
"s_nop 1\n"
"v_permlane32_swap_b32 v245, v242\n"
"s_nop 1\n"
"v_max_f32 v245, v245, v242\n"
"v_mov_b32 v242, v245\n"
"s_nop 1\n"
"v_permlane16_swap_b32 v245, v242\n"
"s_nop 1\n"
"v_max_f32 v245, v245, v242\n"
"v_sub_f32 v164, v164, v245\n"
"v_sub_f32 v165, v165, v245\n"
"v_sub_f32 v166, v166, v245\n"
"v_sub_f32 v167, v167, v245\n"
"v_sub_f32 v176, v176, v245\n"
"v_sub_f32 v177, v177, v245\n"
"v_sub_f32 v178, v178, v245\n"
"v_sub_f32 v179, v179, v245\n"
"v_sub_f32 v200, v200, v245\n"
"v_sub_f32 v201, v201, v245\n"
"v_sub_f32 v202, v202, v245\n"
"v_sub_f32 v203, v203, v245\n"
"v_max3_f32 v246, v168, v169, v170\n"
"v_max3_f32 v246, v246, v171, v180\n"
"v_max3_f32 v246, v246, v181, v182\n"
"v_max3_f32 v246, v246, v183, s71\n"
"v_mov_b32 v242, v246\n"
"s_nop 1\n"
"v_permlane32_swap_b32 v246, v242\n"
"s_nop 1\n"
"v_max_f32 v246, v246, v242\n"
"v_mov_b32 v242, v246\n"
"s_nop 1\n"
"v_permlane16_swap_b32 v246, v242\n"
"s_nop 1\n"
"v_max_f32 v246, v246, v242\n"
"v_sub_f32 v168, v168, v246\n"
"v_sub_f32 v169, v169, v246\n"
"v_sub_f32 v170, v170, v246\n"
"v_sub_f32 v171, v171, v246\n"
"v_sub_f32 v180, v180, v246\n"
"v_sub_f32 v181, v181, v246\n"
"v_sub_f32 v182, v182, v246\n"
"v_sub_f32 v183, v183, v246\n"
"v_sub_f32 v204, v204, v246\n"
"v_sub_f32 v205, v205, v246\n"
"v_sub_f32 v206, v206, v246\n"
"v_sub_f32 v207, v207, v246\n"
"s_cmp_lg_u32 s74, 0\n"
"s_cbranch_scc1 Lskip_%=\n"
"v_exp_f32 v244, -v244\n"
"s_nop 0\n"
"v_mul_f32 v208, v208, v244\n"
"v_mul_f32 v209, v209, v244\n"
"v_mul_f32 v210, v210, v244\n"
"v_mul_f32 v211, v211, v244\n"
"v_accvgpr_read_b32 v248, a0\n"
"v_accvgpr_read_b32 v249, a1\n"
"v_accvgpr_read_b32 v250, a2\n"
"v_accvgpr_read_b32 v251, a3\n"
"v_mul_f32 v248, v248, v244\n"
"v_mul_f32 v249, v249, v244\n"
"v_mul_f32 v250, v250, v244\n"
"v_mul_f32 v251, v251, v244\n"
"v_accvgpr_write_b32 a0, v248\n"
"v_accvgpr_write_b32 a1, v249\n"
"v_accvgpr_write_b32 a2, v250\n"
"v_accvgpr_write_b32 a3, v251\n"
"v_accvgpr_read_b32 v248, a12\n"
"v_accvgpr_read_b32 v249, a13\n"
"v_accvgpr_read_b32 v250, a14\n"
"v_accvgpr_read_b32 v251, a15\n"
"v_mul_f32 v248, v248, v244\n"
"v_mul_f32 v249, v249, v244\n"
"v_mul_f32 v250, v250, v244\n"
"v_mul_f32 v251, v251, v244\n"
"v_accvgpr_write_b32 a12, v248\n"
"v_accvgpr_write_b32 a13, v249\n"
"v_accvgpr_write_b32 a14, v250\n"
"v_accvgpr_write_b32 a15, v251\n"
"v_accvgpr_read_b32 v248, a24\n"
"v_accvgpr_read_b32 v249, a25\n"
"v_accvgpr_read_b32 v250, a26\n"
"v_accvgpr_read_b32 v251, a27\n"
"v_mul_f32 v248, v248, v244\n"
"v_mul_f32 v249, v249, v244\n"
"v_mul_f32 v250, v250, v244\n"
"v_mul_f32 v251, v251, v244\n"
"v_accvgpr_write_b32 a24, v248\n"
"v_accvgpr_write_b32 a25, v249\n"
"v_accvgpr_write_b32 a26, v250\n"
"v_accvgpr_write_b32 a27, v251\n"
"v_accvgpr_read_b32 v248, a36\n"
"v_accvgpr_read_b32 v249, a37\n"
"v_accvgpr_read_b32 v250, a38\n"
"v_accvgpr_read_b32 v251, a39\n"
"v_mul_f32 v248, v248, v244\n"
"v_mul_f32 v249, v249, v244\n"
"v_mul_f32 v250, v250, v244\n"
"v_mul_f32 v251, v251, v244\n"
"v_accvgpr_write_b32 a36, v248\n"
"v_accvgpr_write_b32 a37, v249\n"
"v_accvgpr_write_b32 a38, v250\n"
"v_accvgpr_write_b32 a39, v251\n"
"v_accvgpr_read_b32 v248, a48\n"
"v_accvgpr_read_b32 v249, a49\n"
"v_accvgpr_read_b32 v250, a50\n"
"v_accvgpr_read_b32 v251, a51\n"
"v_mul_f32 v248, v248, v244\n"
"v_mul_f32 v249, v249, v244\n"
"v_mul_f32 v250, v250, v244\n"
"v_mul_f32 v251, v251, v244\n"
"v_accvgpr_write_b32 a48, v248\n"
"v_accvgpr_write_b32 a49, v249\n"
"v_accvgpr_write_b32 a50, v250\n"
"v_accvgpr_write_b32 a51, v251\n"
"v_accvgpr_read_b32 v248, a60\n"
"v_accvgpr_read_b32 v249, a61\n"
"v_accvgpr_read_b32 v250, a62\n"
"v_accvgpr_read_b32 v251, a63\n"
"v_mul_f32 v248, v248, v244\n"
"v_mul_f32 v249, v249, v244\n"
"v_mul_f32 v250, v250, v244\n"
"v_mul_f32 v251, v251, v244\n"
"v_accvgpr_write_b32 a60, v248\n"
"v_accvgpr_write_b32 a61, v249\n"
"v_accvgpr_write_b32 a62, v250\n"
"v_accvgpr_write_b32 a63, v251\n"
"v_accvgpr_read_b32 v248, a72\n"
"v_accvgpr_read_b32 v249, a73\n"
"v_accvgpr_read_b32 v250, a74\n"
"v_accvgpr_read_b32 v251, a75\n"
"v_mul_f32 v248, v248, v244\n"
"v_mul_f32 v249, v249, v244\n"
"v_mul_f32 v250, v250, v244\n"
"v_mul_f32 v251, v251, v244\n"
"v_accvgpr_write_b32 a72, v248\n"
"v_accvgpr_write_b32 a73, v249\n"
"v_accvgpr_write_b32 a74, v250\n"
"v_accvgpr_write_b32 a75, v251\n"
"v_accvgpr_read_b32 v248, a84\n"
"v_accvgpr_read_b32 v249, a85\n"
"v_accvgpr_read_b32 v250, a86\n"
"v_accvgpr_read_b32 v251, a87\n"
"v_mul_f32 v248, v248, v244\n"
"v_mul_f32 v249, v249, v244\n"
"v_mul_f32 v250, v250, v244\n"
"v_mul_f32 v251, v251, v244\n"
"v_accvgpr_write_b32 a84, v248\n"
"v_accvgpr_write_b32 a85, v249\n"
"v_accvgpr_write_b32 a86, v250\n"
"v_accvgpr_write_b32 a87, v251\n"
"v_accvgpr_read_b32 v248, a96\n"
"v_accvgpr_read_b32 v249, a97\n"
"v_accvgpr_read_b32 v250, a98\n"
"v_accvgpr_read_b32 v251, a99\n"
"v_mul_f32 v248, v248, v244\n"
"v_mul_f32 v249, v249, v244\n"
"v_mul_f32 v250, v250, v244\n"
"v_mul_f32 v251, v251, v244\n"
"v_accvgpr_write_b32 a96, v248\n"
"v_accvgpr_write_b32 a97, v249\n"
"v_accvgpr_write_b32 a98, v250\n"
"v_accvgpr_write_b32 a99, v251\n"
"v_accvgpr_read_b32 v248, a108\n"
"v_accvgpr_read_b32 v249, a109\n"
"v_accvgpr_read_b32 v250, a110\n"
"v_accvgpr_read_b32 v251, a111\n"
"v_mul_f32 v248, v248, v244\n"
"v_mul_f32 v249, v249, v244\n"
"v_mul_f32 v250, v250, v244\n"
"v_mul_f32 v251, v251, v244\n"
"v_accvgpr_write_b32 a108, v248\n"
"v_accvgpr_write_b32 a109, v249\n"
"v_accvgpr_write_b32 a110, v250\n"
"v_accvgpr_write_b32 a111, v251\n"
"v_accvgpr_read_b32 v248, a120\n"
"v_accvgpr_read_b32 v249, a121\n"
"v_accvgpr_read_b32 v250, a122\n"
"v_accvgpr_read_b32 v251, a123\n"
"v_mul_f32 v248, v248, v244\n"
"v_mul_f32 v249, v249, v244\n"
"v_mul_f32 v250, v250, v244\n"
"v_mul_f32 v251, v251, v244\n"
"v_accvgpr_write_b32 a120, v248\n"
"v_accvgpr_write_b32 a121, v249\n"
"v_accvgpr_write_b32 a122, v250\n"
"v_accvgpr_write_b32 a123, v251\n"
"v_accvgpr_read_b32 v248, a132\n"
"v_accvgpr_read_b32 v249, a133\n"
"v_accvgpr_read_b32 v250, a134\n"
"v_accvgpr_read_b32 v251, a135\n"
"v_mul_f32 v248, v248, v244\n"
"v_mul_f32 v249, v249, v244\n"
"v_mul_f32 v250, v250, v244\n"
"v_mul_f32 v251, v251, v244\n"
"v_accvgpr_write_b32 a132, v248\n"
"v_accvgpr_write_b32 a133, v249\n"
"v_accvgpr_write_b32 a134, v250\n"
"v_accvgpr_write_b32 a135, v251\n"
"v_exp_f32 v245, -v245\n"
"s_nop 0\n"
"v_mul_f32 v212, v212, v245\n"
"v_mul_f32 v213, v213, v245\n"
"v_mul_f32 v214, v214, v245\n"
"v_mul_f32 v215, v215, v245\n"
"v_accvgpr_read_b32 v248, a4\n"
"v_accvgpr_read_b32 v249, a5\n"
"v_accvgpr_read_b32 v250, a6\n"
"v_accvgpr_read_b32 v251, a7\n"
"v_mul_f32 v248, v248, v245\n"
"v_mul_f32 v249, v249, v245\n"
"v_mul_f32 v250, v250, v245\n"
"v_mul_f32 v251, v251, v245\n"
"v_accvgpr_write_b32 a4, v248\n"
"v_accvgpr_write_b32 a5, v249\n"
"v_accvgpr_write_b32 a6, v250\n"
"v_accvgpr_write_b32 a7, v251\n"
"v_accvgpr_read_b32 v248, a16\n"
"v_accvgpr_read_b32 v249, a17\n"
"v_accvgpr_read_b32 v250, a18\n"
"v_accvgpr_read_b32 v251, a19\n"
"v_mul_f32 v248, v248, v245\n"
"v_mul_f32 v249, v249, v245\n"
"v_mul_f32 v250, v250, v245\n"
"v_mul_f32 v251, v251, v245\n"
"v_accvgpr_write_b32 a16, v248\n"
"v_accvgpr_write_b32 a17, v249\n"
"v_accvgpr_write_b32 a18, v250\n"
"v_accvgpr_write_b32 a19, v251\n"
"v_accvgpr_read_b32 v248, a28\n"
"v_accvgpr_read_b32 v249, a29\n"
"v_accvgpr_read_b32 v250, a30\n"
"v_accvgpr_read_b32 v251, a31\n"
"v_mul_f32 v248, v248, v245\n"
"v_mul_f32 v249, v249, v245\n"
"v_mul_f32 v250, v250, v245\n"
"v_mul_f32 v251, v251, v245\n"
"v_accvgpr_write_b32 a28, v248\n"
"v_accvgpr_write_b32 a29, v249\n"
"v_accvgpr_write_b32 a30, v250\n"
"v_accvgpr_write_b32 a31, v251\n"
"v_accvgpr_read_b32 v248, a40\n"
"v_accvgpr_read_b32 v249, a41\n"
"v_accvgpr_read_b32 v250, a42\n"
"v_accvgpr_read_b32 v251, a43\n"
"v_mul_f32 v248, v248, v245\n"
"v_mul_f32 v249, v249, v245\n"
"v_mul_f32 v250, v250, v245\n"
"v_mul_f32 v251, v251, v245\n"
"v_accvgpr_write_b32 a40, v248\n"
"v_accvgpr_write_b32 a41, v249\n"
"v_accvgpr_write_b32 a42, v250\n"
"v_accvgpr_write_b32 a43, v251\n"
"v_accvgpr_read_b32 v248, a52\n"
"v_accvgpr_read_b32 v249, a53\n"
"v_accvgpr_read_b32 v250, a54\n"
"v_accvgpr_read_b32 v251, a55\n"
"v_mul_f32 v248, v248, v245\n"
"v_mul_f32 v249, v249, v245\n"
"v_mul_f32 v250, v250, v245\n"
"v_mul_f32 v251, v251, v245\n"
"v_accvgpr_write_b32 a52, v248\n"
"v_accvgpr_write_b32 a53, v249\n"
"v_accvgpr_write_b32 a54, v250\n"
"v_accvgpr_write_b32 a55, v251\n"
"v_accvgpr_read_b32 v248, a64\n"
"v_accvgpr_read_b32 v249, a65\n"
"v_accvgpr_read_b32 v250, a66\n"
"v_accvgpr_read_b32 v251, a67\n"
"v_mul_f32 v248, v248, v245\n"
"v_mul_f32 v249, v249, v245\n"
"v_mul_f32 v250, v250, v245\n"
"v_mul_f32 v251, v251, v245\n"
"v_accvgpr_write_b32 a64, v248\n"
"v_accvgpr_write_b32 a65, v249\n"
"v_accvgpr_write_b32 a66, v250\n"
"v_accvgpr_write_b32 a67, v251\n"
"v_accvgpr_read_b32 v248, a76\n"
"v_accvgpr_read_b32 v249, a77\n"
"v_accvgpr_read_b32 v250, a78\n"
"v_accvgpr_read_b32 v251, a79\n"
"v_mul_f32 v248, v248, v245\n"
"v_mul_f32 v249, v249, v245\n"
"v_mul_f32 v250, v250, v245\n"
"v_mul_f32 v251, v251, v245\n"
"v_accvgpr_write_b32 a76, v248\n"
"v_accvgpr_write_b32 a77, v249\n"
"v_accvgpr_write_b32 a78, v250\n"
"v_accvgpr_write_b32 a79, v251\n"
"v_accvgpr_read_b32 v248, a88\n"
"v_accvgpr_read_b32 v249, a89\n"
"v_accvgpr_read_b32 v250, a90\n"
"v_accvgpr_read_b32 v251, a91\n"
"v_mul_f32 v248, v248, v245\n"
"v_mul_f32 v249, v249, v245\n"
"v_mul_f32 v250, v250, v245\n"
"v_mul_f32 v251, v251, v245\n"
"v_accvgpr_write_b32 a88, v248\n"
"v_accvgpr_write_b32 a89, v249\n"
"v_accvgpr_write_b32 a90, v250\n"
"v_accvgpr_write_b32 a91, v251\n"
"v_accvgpr_read_b32 v248, a100\n"
"v_accvgpr_read_b32 v249, a101\n"
"v_accvgpr_read_b32 v250, a102\n"
"v_accvgpr_read_b32 v251, a103\n"
"v_mul_f32 v248, v248, v245\n"
"v_mul_f32 v249, v249, v245\n"
"v_mul_f32 v250, v250, v245\n"
"v_mul_f32 v251, v251, v245\n"
"v_accvgpr_write_b32 a100, v248\n"
"v_accvgpr_write_b32 a101, v249\n"
"v_accvgpr_write_b32 a102, v250\n"
"v_accvgpr_write_b32 a103, v251\n"
"v_accvgpr_read_b32 v248, a112\n"
"v_accvgpr_read_b32 v249, a113\n"
"v_accvgpr_read_b32 v250, a114\n"
"v_accvgpr_read_b32 v251, a115\n"
"v_mul_f32 v248, v248, v245\n"
"v_mul_f32 v249, v249, v245\n"
"v_mul_f32 v250, v250, v245\n"
"v_mul_f32 v251, v251, v245\n"
"v_accvgpr_write_b32 a112, v248\n"
"v_accvgpr_write_b32 a113, v249\n"
"v_accvgpr_write_b32 a114, v250\n"
"v_accvgpr_write_b32 a115, v251\n"
"v_accvgpr_read_b32 v248, a124\n"
"v_accvgpr_read_b32 v249, a125\n"
"v_accvgpr_read_b32 v250, a126\n"
"v_accvgpr_read_b32 v251, a127\n"
"v_mul_f32 v248, v248, v245\n"
"v_mul_f32 v249, v249, v245\n"
"v_mul_f32 v250, v250, v245\n"
"v_mul_f32 v251, v251, v245\n"
"v_accvgpr_write_b32 a124, v248\n"
"v_accvgpr_write_b32 a125, v249\n"
"v_accvgpr_write_b32 a126, v250\n"
"v_accvgpr_write_b32 a127, v251\n"
"v_accvgpr_read_b32 v248, a136\n"
"v_accvgpr_read_b32 v249, a137\n"
"v_accvgpr_read_b32 v250, a138\n"
"v_accvgpr_read_b32 v251, a139\n"
"v_mul_f32 v248, v248, v245\n"
"v_mul_f32 v249, v249, v245\n"
"v_mul_f32 v250, v250, v245\n"
"v_mul_f32 v251, v251, v245\n"
"v_accvgpr_write_b32 a136, v248\n"
"v_accvgpr_write_b32 a137, v249\n"
"v_accvgpr_write_b32 a138, v250\n"
"v_accvgpr_write_b32 a139, v251\n"
"v_exp_f32 v246, -v246\n"
"s_nop 0\n"
"v_mul_f32 v216, v216, v246\n"
"v_mul_f32 v217, v217, v246\n"
"v_mul_f32 v218, v218, v246\n"
"v_mul_f32 v219, v219, v246\n"
"v_accvgpr_read_b32 v248, a8\n"
"v_accvgpr_read_b32 v249, a9\n"
"v_accvgpr_read_b32 v250, a10\n"
"v_accvgpr_read_b32 v251, a11\n"
"v_mul_f32 v248, v248, v246\n"
"v_mul_f32 v249, v249, v246\n"
"v_mul_f32 v250, v250, v246\n"
"v_mul_f32 v251, v251, v246\n"
"v_accvgpr_write_b32 a8, v248\n"
"v_accvgpr_write_b32 a9, v249\n"
"v_accvgpr_write_b32 a10, v250\n"
"v_accvgpr_write_b32 a11, v251\n"
"v_accvgpr_read_b32 v248, a20\n"
"v_accvgpr_read_b32 v249, a21\n"
"v_accvgpr_read_b32 v250, a22\n"
"v_accvgpr_read_b32 v251, a23\n"
"v_mul_f32 v248, v248, v246\n"
"v_mul_f32 v249, v249, v246\n"
"v_mul_f32 v250, v250, v246\n"
"v_mul_f32 v251, v251, v246\n"
"v_accvgpr_write_b32 a20, v248\n"
"v_accvgpr_write_b32 a21, v249\n"
"v_accvgpr_write_b32 a22, v250\n"
"v_accvgpr_write_b32 a23, v251\n"
"v_accvgpr_read_b32 v248, a32\n"
"v_accvgpr_read_b32 v249, a33\n"
"v_accvgpr_read_b32 v250, a34\n"
"v_accvgpr_read_b32 v251, a35\n"
"v_mul_f32 v248, v248, v246\n"
"v_mul_f32 v249, v249, v246\n"
"v_mul_f32 v250, v250, v246\n"
"v_mul_f32 v251, v251, v246\n"
"v_accvgpr_write_b32 a32, v248\n"
"v_accvgpr_write_b32 a33, v249\n"
"v_accvgpr_write_b32 a34, v250\n"
"v_accvgpr_write_b32 a35, v251\n"
"v_accvgpr_read_b32 v248, a44\n"
"v_accvgpr_read_b32 v249, a45\n"
"v_accvgpr_read_b32 v250, a46\n"
"v_accvgpr_read_b32 v251, a47\n"
"v_mul_f32 v248, v248, v246\n"
"v_mul_f32 v249, v249, v246\n"
"v_mul_f32 v250, v250, v246\n"
"v_mul_f32 v251, v251, v246\n"
"v_accvgpr_write_b32 a44, v248\n"
"v_accvgpr_write_b32 a45, v249\n"
"v_accvgpr_write_b32 a46, v250\n"
"v_accvgpr_write_b32 a47, v251\n"
"v_accvgpr_read_b32 v248, a56\n"
"v_accvgpr_read_b32 v249, a57\n"
"v_accvgpr_read_b32 v250, a58\n"
"v_accvgpr_read_b32 v251, a59\n"
"v_mul_f32 v248, v248, v246\n"
"v_mul_f32 v249, v249, v246\n"
"v_mul_f32 v250, v250, v246\n"
"v_mul_f32 v251, v251, v246\n"
"v_accvgpr_write_b32 a56, v248\n"
"v_accvgpr_write_b32 a57, v249\n"
"v_accvgpr_write_b32 a58, v250\n"
"v_accvgpr_write_b32 a59, v251\n"
"v_accvgpr_read_b32 v248, a68\n"
"v_accvgpr_read_b32 v249, a69\n"
"v_accvgpr_read_b32 v250, a70\n"
"v_accvgpr_read_b32 v251, a71\n"
"v_mul_f32 v248, v248, v246\n"
"v_mul_f32 v249, v249, v246\n"
"v_mul_f32 v250, v250, v246\n"
"v_mul_f32 v251, v251, v246\n"
"v_accvgpr_write_b32 a68, v248\n"
"v_accvgpr_write_b32 a69, v249\n"
"v_accvgpr_write_b32 a70, v250\n"
"v_accvgpr_write_b32 a71, v251\n"
"v_accvgpr_read_b32 v248, a80\n"
"v_accvgpr_read_b32 v249, a81\n"
"v_accvgpr_read_b32 v250, a82\n"
"v_accvgpr_read_b32 v251, a83\n"
"v_mul_f32 v248, v248, v246\n"
"v_mul_f32 v249, v249, v246\n"
"v_mul_f32 v250, v250, v246\n"
"v_mul_f32 v251, v251, v246\n"
"v_accvgpr_write_b32 a80, v248\n"
"v_accvgpr_write_b32 a81, v249\n"
"v_accvgpr_write_b32 a82, v250\n"
"v_accvgpr_write_b32 a83, v251\n"
"v_accvgpr_read_b32 v248, a92\n"
"v_accvgpr_read_b32 v249, a93\n"
"v_accvgpr_read_b32 v250, a94\n"
"v_accvgpr_read_b32 v251, a95\n"
"v_mul_f32 v248, v248, v246\n"
"v_mul_f32 v249, v249, v246\n"
"v_mul_f32 v250, v250, v246\n"
"v_mul_f32 v251, v251, v246\n"
"v_accvgpr_write_b32 a92, v248\n"
"v_accvgpr_write_b32 a93, v249\n"
"v_accvgpr_write_b32 a94, v250\n"
"v_accvgpr_write_b32 a95, v251\n"
"v_accvgpr_read_b32 v248, a104\n"
"v_accvgpr_read_b32 v249, a105\n"
"v_accvgpr_read_b32 v250, a106\n"
"v_accvgpr_read_b32 v251, a107\n"
"v_mul_f32 v248, v248, v246\n"
"v_mul_f32 v249, v249, v246\n"
"v_mul_f32 v250, v250, v246\n"
"v_mul_f32 v251, v251, v246\n"
"v_accvgpr_write_b32 a104, v248\n"
"v_accvgpr_write_b32 a105, v249\n"
"v_accvgpr_write_b32 a106, v250\n"
"v_accvgpr_write_b32 a107, v251\n"
"v_accvgpr_read_b32 v248, a116\n"
"v_accvgpr_read_b32 v249, a117\n"
"v_accvgpr_read_b32 v250, a118\n"
"v_accvgpr_read_b32 v251, a119\n"
"v_mul_f32 v248, v248, v246\n"
"v_mul_f32 v249, v249, v246\n"
"v_mul_f32 v250, v250, v246\n"
"v_mul_f32 v251, v251, v246\n"
"v_accvgpr_write_b32 a116, v248\n"
"v_accvgpr_write_b32 a117, v249\n"
"v_accvgpr_write_b32 a118, v250\n"
"v_accvgpr_write_b32 a119, v251\n"
"v_accvgpr_read_b32 v248, a128\n"
"v_accvgpr_read_b32 v249, a129\n"
"v_accvgpr_read_b32 v250, a130\n"
"v_accvgpr_read_b32 v251, a131\n"
"v_mul_f32 v248, v248, v246\n"
"v_mul_f32 v249, v249, v246\n"
"v_mul_f32 v250, v250, v246\n"
"v_mul_f32 v251, v251, v246\n"
"v_accvgpr_write_b32 a128, v248\n"
"v_accvgpr_write_b32 a129, v249\n"
"v_accvgpr_write_b32 a130, v250\n"
"v_accvgpr_write_b32 a131, v251\n"
"v_accvgpr_read_b32 v248, a140\n"
"v_accvgpr_read_b32 v249, a141\n"
"v_accvgpr_read_b32 v250, a142\n"
"v_accvgpr_read_b32 v251, a143\n"
"v_mul_f32 v248, v248, v246\n"
"v_mul_f32 v249, v249, v246\n"
"v_mul_f32 v250, v250, v246\n"
"v_mul_f32 v251, v251, v246\n"
"v_accvgpr_write_b32 a140, v248\n"
"v_accvgpr_write_b32 a141, v249\n"
"v_accvgpr_write_b32 a142, v250\n"
"v_accvgpr_write_b32 a143, v251\n"
"Lskip_%=:\n"
"s_mov_b32 s70, 0x41000000\n"
"s_mov_b32 s71, 0\n"
"s_mov_b32 s74, 0\n"
"s_nop 1\n"
"s_setpc_b64 s[76:77]\n"
"Lend_%=:\n"
    : [y] "=&v"(y), "+s"(w2), [rdK] "+v"(cur), [rdV] "+v"(res[6]), [rdW] "+v"(res[7]),
      [dKV0] "+v"(res[0]), [dKV1] "+v"(res[1]), [dKV2] "+v"(res[2]), [dW0] "+v"(res[3]), [dW1] "+v"(res[4]), [dW2] "+v"(res[5])
    : [rdX] "v"(rdX), [rdO] "v"(rdO),
      [dX] "v"(dX), [po] "v"(po), [exW] "v"(exW), [exR] "v"(exR), [stv] "v"(stv),
      [rX] "s"(rX), [rK] "s"(rK), [rV] "s"(rV), [rWq] "s"(rWq), [rWo] "s"(rWo), [rWs] "s"(rWs),
      [m0b] "s"(m0b), [m0x] "s"(m0x), [k0] "s"(k0), [wq0] "s"(wq0), [x0] "s"(x0), [wo0] "s"(wo0), [wo1] "s"(wo1),
      [st0] "s"(st0), [flg] "s"(flg), [rot] "s"(rot), [rotc] "s"(rotc), [so0] "s"(so[0]), [so1] "s"(so[1]), [fl1] "s"(fl1)
    : "memory", "vcc", "scc",
"v16", "v17", "v18", "v19", "v20", "v21", "v22", "v23", "v24", "v25", "v26", "v27", "v28", "v29", "v30", "v31", "v32", "v33", "v34", "v35", "v36", "v37", "v38", "v39", "v40", "v41", "v42", "v43", "v44", "v45", "v46", "v47", "v48", "v49", "v50", "v51", "v52", "v53", "v54", "v55", "v56", "v57", "v58", "v59", "v60", "v61", "v62", "v63", "v64", "v65", "v66", "v67", "v68", "v69", "v70", "v71", "v72", "v73", "v74", "v75", "v76", "v77", "v78", "v79", "v80", "v81", "v82", "v83", "v84", "v85", "v86", "v87", "v88", "v89", "v90", "v91", "v92", "v93", "v94", "v95", "v96", "v97", "v98", "v99", "v100", "v101", "v102", "v103", "v104", "v105", "v106", "v107", "v108", "v109", "v110", "v111", "v112", "v113", "v114", "v115", "v116", "v117", "v118", "v119", "v120", "v121", "v122", "v123", "v124", "v125", "v126", "v127", "v128", "v129", "v130", "v131", "v132", "v133", "v134", "v135", "v136", "v137", "v138", "v139", "v140", "v141", "v142", "v143", "v144", "v145", "v146", "v147", "v148", "v149", "v150", "v151", "v152", "v153", "v154", "v155", "v156", "v157", "v158", "v159", "v160", "v161", "v162", "v163", "v164", "v165", "v166", "v167", "v168", "v169", "v170", "v171", "v172", "v173", "v174", "v175", "v176", "v177", "v178", "v179", "v180", "v181", "v182", "v183", "v184", "v185", "v186", "v187", "v188", "v189", "v190", "v191", "v192", "v193", "v194", "v195", "v196", "v197", "v198", "v199", "v200", "v201", "v202", "v203", "v204", "v205", "v206", "v207", "v208", "v209", "v210", "v211", "v212", "v213", "v214", "v215", "v216", "v217", "v218", "v219", "v220", "v221", "v222", "v223", "v224", "v225", "v226", "v227", "v228", "v229", "v230", "v231", "v232", "v233", "v234", "v235", "v236", "v237", "v238", "v239", "v240", "v242", "v243", "v244", "v245", "v246", "v247", "v248", "v249", "v250", "v251", "v252", "v253", "v254", "v255",
"a0", "a1", "a2", "a3", "a4", "a5", "a6", "a7", "a8", "a9", "a10", "a11", "a12", "a13", "a14", "a15", "a16", "a17", "a18", "a19", "a20", "a21", "a22", "a23", "a24", "a25", "a26", "a27", "a28", "a29", "a30", "a31", "a32", "a33", "a34", "a35", "a36", "a37", "a38", "a39", "a40", "a41", "a42", "a43", "a44", "a45", "a46", "a47", "a48", "a49", "a50", "a51", "a52", "a53", "a54", "a55", "a56", "a57", "a58", "a59", "a60", "a61", "a62", "a63", "a64", "a65", "a66", "a67", "a68", "a69", "a70", "a71", "a72", "a73", "a74", "a75", "a76", "a77", "a78", "a79", "a80", "a81", "a82", "a83", "a84", "a85", "a86", "a87", "a88", "a89", "a90", "a91", "a92", "a93", "a94", "a95", "a96", "a97", "a98", "a99", "a100", "a101", "a102", "a103", "a104", "a105", "a106", "a107", "a108", "a109", "a110", "a111", "a112", "a113", "a114", "a115", "a116", "a117", "a118", "a119", "a120", "a121", "a122", "a123", "a124", "a125", "a126", "a127", "a128", "a129", "a130", "a131", "a132", "a133", "a134", "a135", "a136", "a137", "a138", "a139", "a140", "a141", "a142", "a143", "a144", "a145", "a146", "a147", "a148", "a149", "a150", "a151", "a152", "a153", "a154", "a155", "a156", "a157", "a158", "a159", "a160", "a161", "a162", "a163", "a164", "a165", "a166", "a167", "a168", "a169", "a170", "a171", "a172", "a173", "a174", "a175", "a176", "a177", "a178", "a179", "a180", "a181", "a182", "a183", "a184", "a185", "a186", "a187", "a188", "a189", "a190", "a191", "a192", "a193", "a194", "a195", "a196", "a197", "a198", "a199", "a200", "a201", "a202", "a203", "a204", "a205", "a206", "a207", "a208", "a209", "a210", "a211", "a212", "a213", "a214", "a215", "a216", "a217", "a218", "a219", "a220", "a221", "a222", "a223", "a224", "a225", "a226", "a227", "a228", "a229", "a230", "a231", "a232", "a233", "a234", "a235", "a236", "a237", "a238", "a239", "a240", "a241", "a242", "a243", "a244", "a245", "a246", "a247", "a248", "a249", "a250", "a251", "a252", "a253", "a254", "a255",
"s56", "s57", "s58", "s59", "s60", "s61", "s62", "s63", "s64", "s65", "s66", "s67", "s68", "s69", "s70", "s71", "s72", "s73", "s74", "s75", "s76", "s77", "s78", "s79", "s80", "s81");
  unsigned ln;
  asm volatile("v_mbcnt_lo_u32_b32 %0, -1, 0\n\tv_mbcnt_hi_u32_b32 %0, -1, %0" : "=v"(ln));
  gu32* F = (gu32*)((char*)ws + flg);
  unsigned fl = (unsigned)__ballot(cur != y) | (1u << g);
  __builtin_amdgcn_s_barrier();
  if (RFL(w2) == 0 && ln == g) __hip_atomic_store(F + g, y + 1, __ATOMIC_RELAXED, __HIP_MEMORY_SCOPE_AGENT);
  w2 = RFL(w2);
  UNIT(2, w2) UNIT(3, w2)
#pragma unroll 1
  for (unsigned spins = 0; spins < (1u << 22); ++spins) {
    if (((fl | (fl >> 8)) & 255u) == 255u) break;
    fl |= (unsigned)__ballot(__hip_atomic_load(F + (ln & 15), __ATOMIC_RELAXED, __HIP_MEMORY_SCOPE_AGENT) != y);
  }
  __builtin_amdgcn_fence(__ATOMIC_ACQUIRE, "wavefront");
  asm volatile("" ::: "memory");
  const __amdgpu_buffer_rsrc_t rp = __builtin_amdgcn_make_buffer_rsrc(ws, 0, PART_BYTES, 0x00020000);
  const __amdgpu_buffer_rsrc_t ro = __builtin_amdgcn_make_buffer_rsrc(out, 0, 1536u * 1280 * 2, 0x00020000);
  const unsigned vl = ln * 16 + ((ln >> 3) & 1) * 2944;
  const unsigned ol = (ln & 7) * 2560 + ((ln >> 3) & 1) * 64 + ((ln >> 4) & 1) * 32 + (ln >> 5) * 16;
  u32x4 d[2][8];
  auto put = [&](int k) {
    const u32x2 e0 = __builtin_amdgcn_permlane16_swap(res[4 * k], res[4 * k + 2], false, false);
    const u32x2 e1 = __builtin_amdgcn_permlane16_swap(res[4 * k + 1], res[4 * k + 3], false, false);
    const u32x4 o = {e0[0], e1[0], e0[1], e1[1]};
    __builtin_amdgcn_raw_buffer_store_b128(o, ro, (k < 3 || w2 < 3) ? ol : 0x80000000u, oo[k], 0);
  };
#pragma unroll
  for (int p2 = 0; p2 < 8; ++p2) d[0][p2] = __builtin_amdgcn_raw_buffer_load_b128(rp, vl, so[2] + p2 * 122880, 16);
#pragma unroll 1
  for (unsigned spins = 0; spins < (1u << 22); ++spins) {
    if ((fl & 255u) == 255u || w2 == 3) break;
    fl |= (unsigned)__ballot(__hip_atomic_load(F + (ln & 7), __ATOMIC_RELAXED, __HIP_MEMORY_SCOPE_AGENT) != y);
  }
  __builtin_amdgcn_fence(__ATOMIC_ACQUIRE, "wavefront");
  asm volatile("" ::: "memory");
#pragma unroll
  for (int p2 = 0; p2 < 8; ++p2)
    d[1][p2] = __builtin_amdgcn_raw_buffer_load_b128(rp, w2 < 3 ? vl : 0x80000000u, so[3] + p2 * 122880, 16);
  put(0);
  put(1);
#pragma unroll
  for (int k = 0; k < 2; ++k) {
#pragma unroll
    for (int j = 0; j < 4; ++j) {
      float s0 = 0.f, s1 = 0.f;
#pragma unroll
      for (int p2 = 0; p2 < 8; ++p2) {
        s0 += __uint_as_float(d[k][p2][j] << 16);
        s1 += __uint_as_float(d[k][p2][j] & 0xffff0000u);
      }
      res[8 + 4 * k + j] = pack_bf16(s0, s1);
    }
    put(2 + k);
  }
}

extern "C" void kernel_launch(void* const* d_in, const int* in_sizes, int n_in, void* d_out, int out_size,
                              void* d_ws, size_t ws_size, hipStream_t stream) {
  attn_fused_kernel<<<256, 256, 0, stream>>>(d_in[0], d_in[1], d_in[2], d_in[3], d_in[4],
                                             d_in[5], d_in[6], d_in[7], d_in[8], d_in[9], d_ws, (u32x2*)d_out);
}
```
